```python
import torch

def _softmax(x, axis=-1):
    return torch.softmax(x, dim=axis)
def _gelu(x):
    return torch.nn.functional.gelu(x, approximate="tanh")
def _silu(x):
    return torch.nn.functional.silu(x)
def _sigmoid(x):
    return torch.sigmoid(x)
def _layernorm(x, axis=-1, eps=1e-5):
    m = x.mean(dim=axis, keepdim=True)
    v = x.var(dim=axis, keepdim=True, unbiased=False)
    return (x - m) / torch.sqrt(v + eps)
def _scatter_add(tgt, idx, src, axis=0):
    return tgt.clone().index_add_(axis, idx, src)
def _segment_sum(src, seg, n):
    out = torch.zeros((n,) + src.shape[1:], dtype=src.dtype)
    return out.index_add_(0, seg, src)
def _pad(x, pw):
    flat = tuple(v for pair in reversed(pw) for v in pair)
    return torch.nn.functional.pad(x, flat)

kernel_name = 'standalone__deformable_dcnv1__vision'

def setup_inputs():

    _w = (lambda a: (lambda t: t.to(torch.bfloat16) if t.is_floating_point() else t)(torch.as_tensor(a)))
    H = W = 224
    C = 96
    K = 5
    return {
        "x": _w((lambda *s: torch.randn(*s, dtype=torch.bfloat16) * 0.05)(1, H, W, C)),
        "offset": _w((lambda *s: torch.randn(*s, dtype=torch.bfloat16) * 0.05)(1, H, W, 2 * K * K) * 0.5),
        "w": _w((lambda *s: torch.randn(*s, dtype=torch.bfloat16) * 0.05)(K, K, C, C) * (K * K * C) ** -0.5),
    }

def reference(x, offset, w):
    B, H, W, C = x.shape
    K = 5
    hh = (lambda x, dt: x.to(dt))(torch.arange(H), x.dtype).reshape(1, H, 1)
    ww = (lambda x, dt: x.to(dt))(torch.arange(W), x.dtype).reshape(1, 1, W)
    xf = x.reshape(B, H * W, C)
    def _gather(yi, xi):
        lin = (yi * W + xi).reshape(B, H * W)
        idx = (lambda x, n, axis: torch.repeat_interleave(x, n, dim=axis))(lin[..., None], C, axis=-1)
        return (lambda x, i, axis: torch.take_along_dim(x, i, dim=axis))(xf, idx, axis=1).reshape(B, H, W, C)
    taps = []
    for ki in range(K):
        for kj in range(K):
            t = ki * K + kj
            dy = offset[..., 2 * t]
            dx = offset[..., 2 * t + 1]
            py = torch.clip(hh + (ki - 0.5 * (K - 1)) + dy, 0.0, H - 1.0)
            px = torch.clip(ww + (kj - 0.5 * (K - 1)) + dx, 0.0, W - 1.0)
            y0 = torch.floor(py)
            x0 = torch.floor(px)
            y1 = torch.clip(y0 + 1.0, 0.0, H - 1.0)
            x1 = torch.clip(x0 + 1.0, 0.0, W - 1.0)
            wy = (py - y0)[..., None]
            wx = (px - x0)[..., None]
            y0i = (lambda x, dt: x.to(dt))(y0, torch.int64)
            y1i = (lambda x, dt: x.to(dt))(y1, torch.int64)
            x0i = (lambda x, dt: x.to(dt))(x0, torch.int64)
            x1i = (lambda x, dt: x.to(dt))(x1, torch.int64)
            v = (1 - wy) * (1 - wx) * _gather(y0i, x0i) + (1 - wy) * wx * _gather(y0i, x1i) + wy * (1 - wx) * _gather(y1i, x0i) + wy * wx * _gather(y1i, x1i)
            taps.append(v)
    samp = torch.stack(taps, 3)
    return torch.einsum("bhwkc,kco->bhwo", samp, w.reshape(K * K, C, C))
```

```cpp
#include <hip/hip_runtime.h>
#include <hip/hip_bf16.h>
#include <stdint.h>

#define ROUND_POS 1

static constexpr int H = 224, W = 224, C = 96, KK = 5;

__device__ __forceinline__ float bf2f(uint16_t v) { return __uint_as_float(((uint32_t)v) << 16); }
__device__ __forceinline__ float rbf(float f) {
    uint32_t u = __float_as_uint(f);
    u = (u + 0x7FFFu + ((u >> 16) & 1u)) & 0xFFFF0000u;
    return __uint_as_float(u);
}

__global__ __launch_bounds__(96) void naive_kernel(const uint16_t* __restrict__ x, const uint16_t* __restrict__ off,
                                                   const uint16_t* __restrict__ w, uint16_t* __restrict__ out) {
    __shared__ float samp[C];
    const int p = blockIdx.x;
    const int h = p / W, ww = p % W;
    const int c = threadIdx.x;
    float acc = 0.f;
    for (int t = 0; t < KK * KK; ++t) {
        const int ki = t / KK, kj = t % KK;
        float dy = bf2f(off[p * 50 + 2 * t]);
        float dx = bf2f(off[p * 50 + 2 * t + 1]);
        float py = (float)(h + ki - 2) + dy;
        float px = (float)(ww + kj - 2) + dx;
        py = rbf(py); px = rbf(px);
        py = fminf(fmaxf(py, 0.f), (float)(H - 1));
        px = fminf(fmaxf(px, 0.f), (float)(W - 1));
        float y0 = floorf(py), x0 = floorf(px);
        float wy = py - y0, wx = px - x0;
        int y0i = (int)y0, x0i = (int)x0;
        int y1i = min(y0i + 1, H - 1), x1i = min(x0i + 1, W - 1);
        float a = bf2f(x[(y0i * W + x0i) * C + c]);
        float b = bf2f(x[(y0i * W + x1i) * C + c]);
        float cc = bf2f(x[(y1i * W + x0i) * C + c]);
        float d = bf2f(x[(y1i * W + x1i) * C + c]);
        float v = (1 - wy) * (1 - wx) * a + (1 - wy) * wx * b + wy * (1 - wx) * cc + wy * wx * d;
        __syncthreads();
        samp[c] = v;
        __syncthreads();
        const uint16_t* wt = w + t * C * C;
        for (int k = 0; k < C; ++k) acc += samp[k] * bf2f(wt[k * C + c]);
    }
    uint32_t u = __float_as_uint(rbf(acc));
    out[p * C + c] = (uint16_t)(u >> 16);
}

extern "C" void kernel_launch(void* const* d_in, const int* in_sizes, int n_in,
                              void* d_out, int out_size, void* d_ws, size_t ws_size,
                              hipStream_t stream) {
    naive_kernel<<<H * W, 96, 0, stream>>>((const uint16_t*)d_in[0], (const uint16_t*)d_in[1],
                                           (const uint16_t*)d_in[2], (uint16_t*)d_out);
}
```

```cpp
#include <hip/hip_runtime.h>
#include <stdint.h>

#define DCN_CLOB "v1", "v2", "v3", "v4", "v5", "v6", "v7", "v8", "v9", "v10", "v11", "v12", "v13", "v14", "v15", "v16", "v17", "v18", "v19", "v20", "v21", "v22", "v23", "v24", "v25", "v26", "v27", "v28", "v29", "v30", "v31", "v32", "v33", "v34", "v35", "v36", "v37", "v38", "v39", "v40", "v41", "v42", "v43", "v44", "v45", "v46", "v47", "v48", "v49", "v50", "v51", "v52", "v53", "v54", "v55", "v56", "v57", "v58", "v59", "v60", "v61", "v62", "v63", "v64", "v65", "v66", "v67", "v68", "v69", "v70", "v71", "v72", "v73", "v74", "v75", "v76", "v77", "v78", "v79", "v80", "v81", "v82", "v83", "v84", "v85", "v86", "v87", "v88", "v89", "v90", "v91", "v92", "v93", "v94", "v95", "v96", "v97", "v98", "v99", "v100", "v101", "v102", "v103", "v104", "v105", "v106", "v107", "v108", "v109", "v110", "v111", "v112", "v113", "v114", "v115", "v116", "v117", "v118", "v119", "v120", "v121", "v122", "v123", "v124", "v125", "v126", "v127", "v128", "v129", "v130", "v131", "v132", "v133", "v134", "v135", "v136", "v137", "v138", "v139", "v140", "v141", "v142", "v143", "v144", "v145", "v146", "v147", "v148", "v149", "v150", "v151", "v152", "v153", "v154", "v155", "v156", "v157", "v158", "v159", "v160", "v161", "v162", "v163", "v164", "v165", "v166", "v167", "v168", "v169", "v170", "v171", "v172", "v173", "v174", "v175", "v176", "v177", "v178", "v179", "v180", "v181", "v182", "v183", "v184", "v185", "v186", "v187", "v188", "v189", "v190", "v191", "v192", "v193", "v194", "v195", "v196", "v197", "v198", "v199", "v200", "v201", "v202", "v203", "v204", "v205", "v206", "v207", "v208", "v209", "v210", "v211", "v212", "v213", "v214", "v215", "v216", "v217", "v218", "v219", "v220", "v221", "v222", "v223", "v224", "v225", "v226", "v227", "v228", "v229", "v230", "v231", "v232", "v233", "v234", "v235", "v236", "v237", "v238", "v239", "v240", "v241", "v242", "v243", "v244", "v245", "v246", "v247", "v248", "v249", "v250", "v251", "v252", "v253", "v254", "v255", "a0", "a1", "a2", "a3", "a4", "a5", "a6", "a7", "a8", "a9", "a10", "a11", "a12", "a13", "a14", "a15", "a16", "a17", "a18", "a19", "a20", "a21", "a22", "a23", "a24", "a25", "a26", "a27", "a28", "a29", "a30", "a31", "a32", "a33", "a34", "a35", "a36", "a37", "a38", "a39", "a40", "a41", "a42", "a43", "a44", "a45", "a46", "a47", "a48", "a49", "a50", "a51", "a52", "a53", "a54", "a55", "a56", "a57", "a58", "a59", "a60", "a61", "a62", "a63", "a64", "a65", "a66", "a67", "a68", "a69", "a70", "a71", "a72", "a73", "a74", "a75", "a76", "a77", "a78", "a79", "a80", "a81", "a82", "a83", "a84", "a85", "a86", "a87", "a88", "a89", "a90", "a91", "a92", "a93", "a94", "a95", "a96", "a97", "a98", "a99", "a100", "a101", "a102", "a103", "a104", "a105", "a106", "a107", "a108", "a109", "a110", "a111", "a112", "a113", "a114", "a115", "a116", "a117", "a118", "a119", "a120", "a121", "a122", "a123", "a124", "a125", "a126", "a127", "a128", "a129", "a130", "a131", "a132", "a133", "a134", "a135", "a136", "a137", "a138", "a139", "a140", "a141", "a142", "a143", "a144", "a145", "a146", "a147", "a148", "a149", "a150", "a151", "a152", "a153", "a154", "a155", "a156", "a157", "a158", "a159", "a160", "a161", "a162", "a163", "a164", "a165", "a166", "a167", "a168", "a169", "a170", "a171", "a172", "a173", "a174", "a175", "s10", "s11", "s12", "s13", "s14", "s15", "s16", "s17", "s18", "s19", "s20", "s25", "s26", "s27", "s28", "s29", "s30", "s31", "s36", "s37", "s38", "s39", "s40", "s41", "s42", "s43", "s44", "s45", "s46", "s47", "s48", "s49", "s50", "s51", "s52", "s53", "s54", "s55", "s56", "s57", "s58", "s59", "s60", "s61", "s62", "s63", "s64", "s65", "s66", "s67", "s68", "s69", "s70", "s71", "s72", "s73", "s74", "s75", "s76", "s77", "s78", "s79", "s80", "s81", "s82", "s83", "vcc", "scc", "memory"
#define DCN_LDS_BYTES 163328


typedef uint32_t u32;
typedef __attribute__((ext_vector_type(2))) float f32x2;
typedef __attribute__((ext_vector_type(4))) float f32x4;
typedef __attribute__((ext_vector_type(2))) __bf16 bf16x2;
typedef __attribute__((ext_vector_type(8))) __bf16 bf16x8;
typedef __attribute__((ext_vector_type(4))) u32 u32x4;
typedef __attribute__((address_space(3))) u32x4 lds_u32x4;
typedef __attribute__((address_space(3))) uint16_t lds_u16;

static constexpr int H = 224, W = 224, C = 96;
static constexpr int PS = 208;

__device__ const uint32_t __attribute__((aligned(128))) TILE_TABLE[256] = {
    0x03004000, 0x083102b9, 0x00000006, 0x194a0419, 0x00000009, 0x217c06d5, 0x0000000c, 0x10184152,
    0x02004001, 0x303102ba, 0x00000007, 0x38630575, 0x0000000a, 0x40950831, 0x0100000d, 0x28ae0990,
    0x00000003, 0x513102bb, 0x00000008, 0x58630576, 0x0000000b, 0x60950832, 0x000c00b3, 0x49ae0991,
    0x00000004, 0x704a0417, 0x03064054, 0x79630577, 0x030c40a8, 0x81950833, 0x000c00b4, 0x68c70aed,
    0x00000005, 0x904a0418, 0x02064055, 0x987c06d3, 0x020c40a9, 0x00ae098f, 0x010c00b5, 0x88c70aee,
    0x000c00ab, 0x04da0bef, 0x000c00ae, 0x007c06d4, 0x000c00b1, 0x04da0bf3, 0x011240fc, 0x01c70aef,
    0x000c00ac, 0x002481ff, 0x000c00af, 0x04da0bf1, 0x000c00b2, 0x00728646, 0x011a416c, 0x04da0bf5,
    0x000c00ad, 0x00248200, 0x000c00b0, 0x004b8422, 0x00180158, 0x007f86f9, 0x0018015b, 0x00a6891c,
    0x00180153, 0x00248201, 0x00180155, 0x004b8423, 0x00180159, 0x007f86fa, 0x0018015c, 0x00a6891d,
    0x00180154, 0x00248202, 0x00180156, 0x004b8424, 0x0018015a, 0x007f86fb, 0x0118015d, 0x00a6891e,
    0x012221dc, 0x00248203, 0x00180157, 0x005684bf, 0x007f86f5, 0x007f86fc, 0x00a6891a, 0x00b389d1,
    0x002221dd, 0x00248204, 0x004b8421, 0x005684c0, 0x007f86f6, 0x0088877b, 0x00a6891b, 0x00b389d2,
    0x002481fb, 0x01248205, 0x01500460, 0x015684c1, 0x007f86f7, 0x0088877c, 0x01b009a0, 0x00b389d3,
    0x002481fc, 0x003182b5, 0x00500461, 0x005884d7, 0x007f86f8, 0x0188877d, 0x00b009a1, 0x00b389d4,
    0x002481fd, 0x003182b6, 0x005884d3, 0x005884d8, 0x01800700, 0x008c87af, 0x00b389cd, 0x00ba8a37,
    0x002481fe, 0x003182b7, 0x005884d4, 0x005884d9, 0x00800701, 0x008c87b0, 0x00b389ce, 0x00ba8a38,
    0x012c0268, 0x003182b8, 0x005884d5, 0x005884da, 0x018c07a8, 0x008c87b1, 0x00b389cf, 0x01ba8a39,
    0x002c0269, 0x003d8361, 0x005884d6, 0x0065858d, 0x008c07a9, 0x008c87b2, 0x00b389d0, 0x00c08a87,
    0x003182b1, 0x003d8362, 0x015c0508, 0x0065858e, 0x008c87ab, 0x00998864, 0x01bc0a48, 0x00c08a88,
    0x003182b2, 0x013d8363, 0x005c0509, 0x0065858f, 0x008c87ac, 0x00998865, 0x00bc0a49, 0x00c08a89,
    0x003182b3, 0x003e836a, 0x00658589, 0x00658590, 0x008c87ad, 0x00998866, 0x00c08a83, 0x00c08a8a,
    0x003182b4, 0x003e836b, 0x0065858a, 0x006f861d, 0x008c87ae, 0x00998867, 0x00c08a84, 0x00cd8b3d,
    0x01380310, 0x003e836c, 0x0065858b, 0x006f861e, 0x01980850, 0x00998868, 0x00c08a85, 0x00cd8b3e,
    0x00380311, 0x003e836d, 0x0065858c, 0x016f861f, 0x00980851, 0x00a188d9, 0x00c08a86, 0x00cd8b3f,
    0x003e8367, 0x003e836e, 0x016805b0, 0x00728643, 0x00998861, 0x00a188da, 0x01c80af0, 0x00cd8b40,
    0x003e8368, 0x004b8420, 0x006805b1, 0x00728644, 0x00998862, 0x01a188db, 0x00c80af1, 0x00d38b95,
    0x003e8369, 0x001240fd, 0x0072863f, 0x00728645, 0x00998863, 0x00084072, 0x00cd8b39, 0x00d38b96,
    0x014403b8, 0x001a416d, 0x00728640, 0x00004002, 0x01a408f8, 0x001040e2, 0x00cd8b3a, 0x01d38b97,
    0x004403b9, 0x002001c2, 0x00728641, 0x00500462, 0x00a408f9, 0x00800702, 0x00cd8b3b, 0x00b009a2,
    0x004b841d, 0x002c026a, 0x00728642, 0x005c050a, 0x00a68917, 0x008c07aa, 0x00cd8b3c, 0x00bc0a4a,
    0x004b841e, 0x00380312, 0x01740658, 0x006805b2, 0x00a68918, 0x00980852, 0x01d40b98, 0x00c80af2,
    0x004b841f, 0x004403ba, 0x00740659, 0x0074065a, 0x00a68919, 0x00a408fa, 0x00d40b99, 0x00d40b9a,
};


__device__ __forceinline__ float bf_lo(u32 d) { return __uint_as_float(d << 16); }
__device__ __forceinline__ float bf_hi(u32 d) { return __uint_as_float(d & 0xffff0000u); }
__device__ __forceinline__ u32 pack_bf16(float a, float b) {
    bf16x2 v = __builtin_convertvector((f32x2){a, b}, bf16x2);
    return __builtin_bit_cast(u32, v);
}
__device__ __forceinline__ int clampi(int v, int lo, int hi) { return min(max(v, lo), hi); }

__device__ __forceinline__ void dcn_generic(const uint16_t* __restrict__ x, const uint16_t* __restrict__ off,
                                            const uint16_t* __restrict__ w, uint16_t* __restrict__ out,
                                            u32 lds0, int row0, int row3, int x0, int geom, int tid) {
    const int lane = tid & 63, wave = __builtin_amdgcn_readfirstlane(tid >> 6);
    const int r = lane & 15, q = lane >> 4;
    f32x4 acc[4][6];
#pragma unroll
    for (int s = 0; s < 4; ++s)
#pragma unroll
        for (int n = 0; n < 6; ++n) acc[s][n] = (f32x4){0.f, 0.f, 0.f, 0.f};
    const int nstrip = (geom >> 2) + ((geom == 13 && wave == 0) ? 1 : 0);
    const int xx = x0 + (lane >> 2), src = (4 * r + q) * 4;
    u32 wreg[18], oreg[4];
    auto fetch = [&](int t) {
#pragma unroll
        for (int k = 0; k < 18; ++k) wreg[k] = ((const u32*)w)[t * (C * C / 2) + tid + 256 * k];
#pragma unroll
        for (int s = 0; s < 4; ++s)
            oreg[s] = ((const u32*)off)[(min(s < 3 ? row0 + s : row3, H - 1) * W + xx) * 25 + t];
    };
    fetch(0);
    for (int t = 0; t < 25; ++t) {
        __syncthreads();
#pragma unroll
        for (int k = 0; k < 18; ++k) {
            const int e = tid + 256 * k, c = e / (C / 2), o = 2 * (e % (C / 2));
            *(lds_u16*)(lds0 + o * PS + c * 2) = (uint16_t)wreg[k];
            *(lds_u16*)(lds0 + (o + 1) * PS + c * 2) = (uint16_t)(wreg[k] >> 16);
        }
        const u32 ocur[4] = {oreg[0], oreg[1], oreg[2], oreg[3]};
        __syncthreads();
        fetch(min(t + 1, 24));
        const int ki = t / 5, kj = t % 5;
#pragma unroll
        for (int s = 0; s < 4; ++s) {
            if (s < nstrip) {
                const int y = s < 3 ? row0 + s : row3;
                const u32 pp = pack_bf16((float)(y + ki - 2) + bf_lo(ocur[s]), (float)(xx + kj - 2) + bf_hi(ocur[s]));
                const float py = fminf(fmaxf(bf_lo(pp), 0.f), (float)(H - 1));
                const float px = fminf(fmaxf(bf_hi(pp), 0.f), (float)(W - 1));
                const float fy = floorf(py), fx = floorf(px);
                const float wy = py - fy, wx = px - fx;
                const int ya = clampi((int)fy, 0, H - 1), xa = clampi((int)fx, 0, W - 1);
                const int yb = min(ya + 1, H - 1), xb = min(xa + 1, W - 1);
                const float w00 = (1.f - wy) * (1.f - wx), w01 = (1.f - wy) * wx, w10 = wy * (1.f - wx), w11 = wy * wx;
                u32x4 v[3][4];
#pragma unroll
                for (int m = 0; m < 3; ++m) {
                    const int c0 = 32 * m + 8 * (lane & 3);
                    v[m][0] = *(const u32x4*)(x + (ya * W + xa) * C + c0);
                    v[m][1] = *(const u32x4*)(x + (ya * W + xb) * C + c0);
                    v[m][2] = *(const u32x4*)(x + (yb * W + xa) * C + c0);
                    v[m][3] = *(const u32x4*)(x + (yb * W + xb) * C + c0);
                }
#pragma unroll
                for (int m = 0; m < 3; ++m) {
                    u32x4 f;
#pragma unroll
                    for (int k = 0; k < 4; ++k)
                        f[k] = pack_bf16(w00 * bf_lo(v[m][0][k]) + w01 * bf_lo(v[m][1][k]) + w10 * bf_lo(v[m][2][k]) + w11 * bf_lo(v[m][3][k]),
                                         w00 * bf_hi(v[m][0][k]) + w01 * bf_hi(v[m][1][k]) + w10 * bf_hi(v[m][2][k]) + w11 * bf_hi(v[m][3][k]));
#pragma unroll
                    for (int k = 0; k < 4; ++k) f[k] = __builtin_amdgcn_ds_bpermute(src, f[k]);
#pragma unroll
                    for (int n = 0; n < 6; ++n) {
                        const int o = 32 * (n >> 1) + 8 * (r >> 2) + 4 * (n & 1) + (r & 3);
                        const u32x4 wv = *(lds_u32x4*)(lds0 + o * PS + (32 * m + 8 * q) * 2);
                        acc[s][n] = __builtin_amdgcn_mfma_f32_16x16x32_bf16(__builtin_bit_cast(bf16x8, wv),
                                                                            __builtin_bit_cast(bf16x8, f), acc[s][n], 0, 0, 0);
                    }
                }
            }
        }
    }
#pragma unroll
    for (int s = 0; s < 4; ++s) {
        if (s < nstrip) {
            const int y = s < 3 ? row0 + s : row3;
#pragma unroll
            for (int j = 0; j < 3; ++j) {
                const f32x4 a = acc[s][2 * j], b = acc[s][2 * j + 1];
                *(u32x4*)(out + (y * W + x0 + r) * C + 32 * j + 8 * q) =
                    (u32x4){pack_bf16(a[0], a[1]), pack_bf16(a[2], a[3]), pack_bf16(b[0], b[1]), pack_bf16(b[2], b[3])};
            }
        }
    }
}

struct Args { const uint16_t* x; const uint16_t* off; const uint16_t* w; void* ws; uint16_t* out; };
typedef __attribute__((address_space(4))) const Args kernarg_t;

__global__ __launch_bounds__(256, 1) void dcn_main(const uint16_t* x, const uint16_t* off, const uint16_t* w,
                                                   void* ws, uint16_t* out) {
    __shared__ __attribute__((aligned(16))) char smem[DCN_LDS_BYTES];
    uint64_t ka = (uint64_t)__builtin_amdgcn_kernarg_segment_ptr();
    u32 flags, row0, row3, x0, geom;
    u32 tid = threadIdx.x;
    asm volatile(
R"ASM(
.macro M a,b,x
v_mfma_f32_16x16x32_f16 v[\a:\a+3], a[\b:\b+3], v[\x:\x+3], v[\a:\a+3]
.endm
.macro RV d,a,o
ds_read_b128 v[\d:\d+3], v\a offset:\o
.endm
.macro RA d,a,o
ds_read_b128 a[\d:\d+3], v\a offset:\o
.endm
.macro PM d,s,w
v_pk_mul_f16 v\d, v\s, v\w op_sel_hi:[1,0]
.endm
.macro PH d,s,w
v_pk_fma_f16 v\d, v\s, v\w, v\d op_sel:[0,1,0]
.endm
.macro PL d,s,w
v_pk_fma_f16 v\d, v\s, v\w, v\d op_sel_hi:[1,0,1]
.endm
L_start_%=:
s_lshl_b32 s11, s8, 2
s_load_dword s12, s[6:7], s11
s_load_dwordx2 s[60:61], s[4:5], 0x8
v_readfirstlane_b32 s10, v0
s_lshr_b32 s28, s8, 3
s_getpc_b64 s[30:31]
s_lshl_b32 s11, s28, 12
s_min_u32 s11, s11, L_end_%=-L_start_%=-4096
v_lshl_add_u32 v88, v0, 4, s11
s_load_dwordx2 s[64:65], s[4:5], 0x10
s_load_dwordx2 s[36:37], s[4:5], 0x18
global_load_dword v232, v88, s[30:31]
s_load_dwordx2 s[80:81], s[4:5], 0x0
s_load_dwordx2 s[68:69], s[4:5], 0x20
v_and_b32 v6, 63, v0
s_lshr_b32 s10, s10, 6
v_lshlrev_b32 v15, 4, v6
v_cmp_le_u32 vcc, 36, v6
v_add_u32 v88, 20800, v15
s_mov_b32 s57, m0
v_cndmask_b32 v48, v15, v88, vcc
v_cmp_le_u32 vcc, 8, v6
s_mul_i32 s29, s10, 6400
s_add_u32 s29, s29, 136704
v_cndmask_b32 v49, v15, v88, vcc
s_mov_b32 m0, s29
s_mov_b32 s62, 5017600
s_mov_b32 s63, 0x20000
s_mov_b32 s27, -1
s_mul_i32 s21, s10, 3
v_bfrev_b32 v30, -2
s_waitcnt lgkmcnt(0)
s_bfe_u32 s15, s12, 0x3000c
s_and_b32 s23, s12, 0xfff
s_min_u32 s11, s10, s15
s_sub_u32 s21, s21, s11
s_mul_i32 s13, s21, 14
s_add_u32 s13, s13, s23
s_cmp_ge_u32 s12, 0x4000000
s_mul_i32 s13, s13, 1600
s_cbranch_scc1 L_sp_%=
L_spr_%=:
buffer_load_dwordx4 v15, s[60:63], s13 offen offset:0 lds
s_add_u32 s11, s13, 20800
buffer_load_dwordx4 v48, s[60:63], s13 offen offset:1024 lds
s_mul_i32 s28, s10, -67200
buffer_load_dwordx4 v15, s[60:63], s11 offen offset:2048 lds
s_add_u32 s28, s28, 222400
buffer_load_dwordx4 v49, s[60:63], s11 offen offset:3072 lds
v_cmp_le_u32 vcc, 44, v6
s_bitcmp1_b32 s12, 15
s_cselect_b32 s28, s28, 0
v_add_u32 v88, s28, v15
v_cndmask_b32 v50, v15, v88, vcc
s_addk_i32 m0, 4096
s_add_u32 s11, s11, 24896
buffer_load_dwordx4 v50, s[60:63], s11 offen offset:0 lds
s_add_u32 s11, s11, s28
s_bitcmp1_b32 s12, 15
s_cbranch_scc0 L_o2_%=
buffer_load_dwordx4 v15, s[60:63], s11 offen offset:1024 lds
s_mov_b64 exec, 0xffff
buffer_load_dwordx4 v15, s[60:63], s11 offen offset:2048 lds
s_mov_b64 exec, -1
L_o2_%=:
s_bfe_u32 s13, s12, 0x80010
s_mul_i32 s11, s13, 14
s_sub_u32 s23, s23, s11
s_lshl_b32 s23, s23, 4
s_add_u32 s21, s21, s13
s_add_i32 s26, s23, -3
s_cmp_lt_u32 s10, s15
s_cselect_b32 s24, 8, 12
s_sub_u32 s15, s13, s15
s_add_u32 s22, s13, 12
s_mov_b32 s25, 0
s_bitcmp1_b32 s12, 15
s_cselect_b32 s24, 13, s24
s_addc_u32 s15, s15, 14
s_and_b32 s11, s12, 0x6000000
s_cbranch_scc0 L_o3_%=
L_sq_%=:
s_bitcmp1_b32 s12, 26
s_cbranch_scc0 L_sq1_%=
s_lshr_b32 s11, s10, 1
s_mul_i32 s25, s11, 9
s_mul_i32 s28, s11, 6
s_sub_u32 s21, s21, s28
s_lshl4_add_u32 s23, s11, s23
s_branch L_o3_%=
L_sq1_%=:
s_add_i32 s15, s15, -2
s_cmp_eq_u32 s10, 3
s_cselect_b32 s24, 0, s24
s_branch L_o3_%=
L_sp_%=:
s_lshr_b32 s11, s10, 1
s_mul_i32 s11, s11, 132800
s_bitcmp1_b32 s12, 26
s_cselect_b32 s11, s11, 0
s_sub_u32 s13, s13, s11
s_lshr_b32 s27, s12, 27
s_lshl2_add_u32 s27, s27, s10
s_add_i32 s27, s27, -4
s_cmp_lt_u32 s27, 75
s_cbranch_scc0 L_spr_%=
s_mov_b32 s66, 460800
s_mov_b32 s67, s63
s_mov_b32 s38, 465152
s_mov_b32 s39, s63
s_mul_i32 s41, s10, 6144
s_add_u32 s41, s41, 87552
v_lshrrev_b32 v93, 5, v6
L_p1_%=:
s_mov_b32 s78, s27
s_lshl_b32 s11, s78, 3
s_add_u32 s28, s11, 1440
s_cmp_lt_u32 s78, 60
s_cselect_b32 s28, s11, s28
s_cselect_b32 s79, 20, 4
s_add_u32 s11, s11, 1728
s_cmp_lt_u32 s78, 72
s_cselect_b32 s28, s28, s11
s_cselect_b32 s79, s79, 1
s_mul_i32 s79, s79, 4608
s_mul_i32 s28, s28, 192
s_sub_u32 s79, s79, 1536
v_mad_u32_u24 v92, v93, s79, v15
s_mov_b32 m0, s41
s_nop 0
buffer_load_dwordx4 v15, s[64:67], s28 offen offset:0 lds
buffer_load_dwordx4 v92, s[64:67], s28 offen offset:1024 lds
s_add_u32 s28, s28, s79
buffer_load_dwordx4 v15, s[64:67], s28 offen offset:2048 lds
s_add_u32 s28, s28, s79
buffer_load_dwordx4 v15, s[64:67], s28 offen offset:3072 lds
s_add_u32 m0, m0, 4096
s_add_u32 s28, s28, 4096
buffer_load_dwordx4 v92, s[64:67], s28 offen offset:0 lds
s_add_u32 s28, s28, s79
buffer_load_dwordx4 v15, s[64:67], s28 offen offset:1024 lds
s_cmp_lt_u32 s27, 8
s_cselect_b32 s11, 68, -4
s_cmp_lt_u32 s27, 4
s_cselect_b32 s11, 0, s11
s_add_i32 s79, s27, s11
s_lshl_b32 s79, s79, 4
buffer_load_dwordx2 v[80:81], off, s[36:39], s79 sc1
s_mov_b32 m0, s29
s_cmp_lt_u32 s79, 1152
s_cbranch_scc1 L_spr_%=
L_o3_%=:
s_mov_b32 s66, 460800
s_mov_b32 s67, s63
s_mul_i32 s41, s10, 6144
s_add_u32 s41, s41, 87552
v_lshrrev_b32 v93, 5, v6
s_mul_i32 s40, s10, 1536
v_mul_u32_u24 v230, 90624, v93
v_add_u32 v230, v230, v15
s_mov_b32 s38, 465152
s_mov_b32 s39, s63
s_mov_b32 s82, 9633792
s_mov_b32 s83, s63
s_mov_b32 s70, 9633792
s_mov_b32 s71, s63
v_add_u32 v24, 0, v15
v_add_u32 v25, 1024, v15
v_add_u32 v26, 2048, v15
v_add_u32 v27, 3072, v15
v_lshlrev_b32 v28, 4, v0
v_mov_b32 v5, 0
v_add_u32 v45, 4096, v15
v_and_b32 v7, 15, v6
v_lshrrev_b32 v8, 4, v6
v_and_b32 v90, 3, v6
v_mul_u32_u24 v90, 192, v90
v_lshl_add_u32 v90, v8, 4, v90
v_lshrrev_b32 v91, 2, v7
v_mul_u32_u24 v91, 1536, v91
v_add3_u32 v231, v91, v90, s41
v_mov_b32 v90, 16640
v_and_b32 v94, 1, v6
v_lshlrev_b32 v94, 4, v94
v_lshlrev_b32 v90, v94, v90
v_lshrrev_b32 v91, 1, v7
v_mad_i32_i24 v91, v8, -2, v91
v_cmp_eq_u32 vcc, 0, v91
v_cmp_eq_u32 s[30:31], 1, v91
v_mov_b64 v[224:225], 0
v_mov_b64 v[228:229], 0
v_cndmask_b32 v226, 0, v90, vcc
v_cndmask_b32 v227, 0, v90, s[30:31]
v_mul_u32_u24 v52, 171, v24
v_lshrrev_b32 v52, 15, v52
v_lshl_add_u32 v96, v52, 4, v24
v_mul_u32_u24 v53, 171, v25
v_lshrrev_b32 v53, 15, v53
v_lshl_add_u32 v97, v53, 4, v25
v_mul_u32_u24 v54, 171, v26
v_lshrrev_b32 v54, 15, v54
v_lshl_add_u32 v98, v54, 4, v26
v_mul_u32_u24 v55, 171, v27
v_lshrrev_b32 v55, 15, v55
v_lshl_add_u32 v99, v55, 4, v27
v_mul_u32_u24 v56, 171, v28
v_lshrrev_b32 v56, 15, v56
v_lshl_add_u32 v100, v56, 4, v28
s_cmp_lt_u32 s27, 75
s_cbranch_scc0 L_p2r_%=
L_p2_%=:
s_mul_i32 s11, s78, 6144
s_and_b32 s28, s78, -4
s_lshl_b32 s28, s28, 5
s_add_u32 s11, s11, s28
s_add_u32 s11, s11, 2048
v_add_u32 v88, s11, v15
v_add_u32 v89, s11, v45
v_mov_b32 v82, 0
s_cmp_lt_u32 s79, 1152
s_waitcnt vmcnt(6)
s_cbranch_scc1 L_p2a_%=
s_waitcnt vmcnt(0)
L_p2a_%=:
RA 0,231,0
RA 4,231,768
RA 8,231,64
RA 12,231,832
RA 16,231,128
RA 20,231,896
s_waitcnt lgkmcnt(0)
v_mfma_f32_16x16x32_bf16 v[112:115], a[0:3], v[226:229], 0
v_mfma_f32_16x16x32_bf16 v[116:119], a[4:7], v[226:229], 0
v_mfma_f32_16x16x32_bf16 v[120:123], a[0:3], v[224:227], 0
v_mfma_f32_16x16x32_bf16 v[124:127], a[4:7], v[224:227], 0
s_nop 3
v_mfma_f32_16x16x32_bf16 v[128:131], a[8:11], v[226:229], 0
v_mfma_f32_16x16x32_bf16 v[132:135], a[12:15], v[226:229], 0
v_cvt_pk_f16_f32 v136, v112, v113
v_cvt_pk_f16_f32 v137, v114, v115
v_cvt_pk_f16_f32 v138, v116, v117
v_cvt_pk_f16_f32 v139, v118, v119
v_max3_f32 v82, v82, |v112|, |v113|
v_max3_f32 v82, v82, |v114|, |v115|
v_max3_f32 v82, v82, |v116|, |v117|
v_max3_f32 v82, v82, |v118|, |v119|
v_mfma_f32_16x16x32_bf16 v[112:115], a[8:11], v[224:227], 0
v_mfma_f32_16x16x32_bf16 v[116:119], a[12:15], v[224:227], 0
buffer_store_dwordx4 v[136:139], v88, s[36:39], 0 offen offset:0 sc1
v_cvt_pk_f16_f32 v140, v120, v121
v_cvt_pk_f16_f32 v141, v122, v123
v_cvt_pk_f16_f32 v142, v124, v125
v_cvt_pk_f16_f32 v143, v126, v127
v_max3_f32 v82, v82, |v120|, |v121|
v_max3_f32 v82, v82, |v122|, |v123|
v_max3_f32 v82, v82, |v124|, |v125|
v_max3_f32 v82, v82, |v126|, |v127|
v_mfma_f32_16x16x32_bf16 v[120:123], a[16:19], v[226:229], 0
v_mfma_f32_16x16x32_bf16 v[124:127], a[20:23], v[226:229], 0
buffer_store_dwordx4 v[140:143], v88, s[36:39], 0 offen offset:1024 sc1
v_cvt_pk_f16_f32 v136, v128, v129
v_cvt_pk_f16_f32 v137, v130, v131
v_cvt_pk_f16_f32 v138, v132, v133
v_cvt_pk_f16_f32 v139, v134, v135
v_max3_f32 v82, v82, |v128|, |v129|
v_max3_f32 v82, v82, |v130|, |v131|
v_max3_f32 v82, v82, |v132|, |v133|
v_max3_f32 v82, v82, |v134|, |v135|
v_mfma_f32_16x16x32_bf16 v[128:131], a[16:19], v[224:227], 0
v_mfma_f32_16x16x32_bf16 v[132:135], a[20:23], v[224:227], 0
buffer_store_dwordx4 v[136:139], v88, s[36:39], 0 offen offset:2048 sc1
v_cvt_pk_f16_f32 v140, v112, v113
v_cvt_pk_f16_f32 v141, v114, v115
v_cvt_pk_f16_f32 v142, v116, v117
v_cvt_pk_f16_f32 v143, v118, v119
v_max3_f32 v82, v82, |v112|, |v113|
v_max3_f32 v82, v82, |v114|, |v115|
v_max3_f32 v82, v82, |v116|, |v117|
v_max3_f32 v82, v82, |v118|, |v119|
s_nop 1
buffer_store_dwordx4 v[140:143], v88, s[36:39], 0 offen offset:3072 sc1
v_cvt_pk_f16_f32 v136, v120, v121
v_cvt_pk_f16_f32 v137, v122, v123
v_cvt_pk_f16_f32 v138, v124, v125
v_cvt_pk_f16_f32 v139, v126, v127
v_max3_f32 v82, v82, |v120|, |v121|
v_max3_f32 v82, v82, |v122|, |v123|
v_max3_f32 v82, v82, |v124|, |v125|
v_max3_f32 v82, v82, |v126|, |v127|
s_nop 1
buffer_store_dwordx4 v[136:139], v89, s[36:39], 0 offen offset:0 sc1
v_cvt_pk_f16_f32 v140, v128, v129
v_cvt_pk_f16_f32 v141, v130, v131
v_cvt_pk_f16_f32 v142, v132, v133
v_cvt_pk_f16_f32 v143, v134, v135
v_max3_f32 v82, v82, |v128|, |v129|
v_max3_f32 v82, v82, |v130|, |v131|
v_max3_f32 v82, v82, |v132|, |v133|
v_max3_f32 v82, v82, |v134|, |v135|
s_nop 1
buffer_store_dwordx4 v[140:143], v89, s[36:39], 0 offen offset:1024 sc1
s_waitcnt vmcnt(11)
v_readfirstlane_b32 s76, v80
v_readfirstlane_b32 s77, v81
s_mov_b32 s11, 1191182336
v_cmp_ngt_f32 vcc, s11, v82
s_mov_b32 s11, 973078528
v_cmp_le_f32 s[30:31], s11, v82
v_cmp_lt_f32 s[58:59], 0, v82
s_mov_b32 s28, 0
s_cmp_lg_u64 s[58:59], 0
s_addc_u32 s28, s28, s28
s_cmp_lg_u64 s[30:31], 0
s_addc_u32 s28, s28, s28
s_cmp_lg_u64 vcc, 0
s_addc_u32 s28, s28, s28
s_xor_b32 s11, s76, 1779033703
s_xor_b32 s28, s28, 1013904242
s_xor_b32 s28, s28, s77
v_mov_b32 v114, s11
v_mov_b32 v115, s28
s_mov_b32 s78, s79
v_add_u32 v31, 1, v80
v_writelane_b32 v30, s78, 0
s_cmp_lt_u32 s79, 1152
s_cbranch_scc0 L_pu_%=
L_p2r_%=:
s_mov_b32 m0, s41
s_nop 0
buffer_load_dwordx4 v15, s[64:67], s40 offen offset:0 lds
s_add_i32 s13, s13, -3
buffer_load_dwordx4 v230, s[64:67], s40 offen offset:1024 lds
s_movk_i32 s50, 4096
s_add_u32 s40, s40, 90624
buffer_load_dwordx4 v15, s[64:67], s40 offen offset:2048 lds
s_max_i32 s53, s26, 0
s_mul_i32 s53, s53, 192
s_min_u32 s54, s15, 223
s_mul_i32 s54, s54, 43008
s_mul_i32 s14, s13, 43008
s_add_u32 s54, s54, s53
s_add_u32 s40, s40, 90624
buffer_load_dwordx4 v15, s[64:67], s40 offen offset:3072 lds
s_add_i32 s14, s14, s53
s_mul_i32 s48, s10, 43008
s_add_i32 s48, s48, s14
s_max_i32 s16, s48, s53
s_add_i32 s17, s48, 172032
s_add_i32 s18, s48, 344064
s_min_i32 s18, s18, s54
s_add_i32 s19, s48, 516096
s_min_i32 s19, s19, s54
s_add_i32 s20, s14, 688128
s_min_i32 s20, s20, s54
s_add_i32 s51, s14, 731136
s_min_i32 s51, s51, s54
s_add_i32 s52, s14, 774144
s_min_i32 s52, s52, s54
s_and_b32 s11, s12, 0x5000000
s_cbranch_scc1 L_fx_%=
L_fxr_%=:
s_add_u32 s11, s41, 49152
v_add_u32 v1, s11, v96
v_add_u32 v2, s11, v97
v_add_u32 v3, s11, v98
v_mul_u32_u24 v4, 13, v7
v_add_u32 v4, v4, v8
v_lshl_add_u32 v4, v4, 4, s11
s_mul_i32 s28, s10, 4608
v_add_u32 v96, s28, v96
v_add_u32 v97, s28, v97
v_add_u32 v98, s28, v98
v_add_u32 v99, s28, v99
v_add_u32 v100, 73728, v100
v_mad_i32_i24 v57, v56, -12, v0
v_min_u32 v90, 10, v57
v_mul_u32_u24 v91, 288, v56
v_add_u32 v90, v90, v91
v_min_u32 v90, 5194, v90
v_lshlrev_b32 v101, 4, v90
v_cmp_gt_u32 vcc, 8, v57
v_cmp_gt_u32 s[30:31], 19, v56
v_lshlrev_b32 v57, 4, v57
v_mov_b32 v90, 2147483392
s_and_b64 vcc, vcc, s[30:31]
v_cndmask_b32 v57, v90, v57, vcc
v_mov_b32 v90, 15872
v_lshlrev_b32 v90, v94, v90
v_lshrrev_b32 v91, 3, v6
v_xor_b32 v91, v91, v6
v_and_b32 v91, 4, v91
v_and_or_b32 v91, v6, 16, v91
v_bfe_u32 v94, v6, 1, 1
v_or_b32 v91, v91, v94
v_bfe_u32 v94, v6, 3, 1
v_lshl_or_b32 v91, v94, 1, v91
v_cmp_eq_u32 vcc, 0, v91
v_mul_u32_u24 v11, 100, v7
v_add_u32 v11, s29, v11
v_cndmask_b32 v220, 0, v90, vcc
v_cmp_eq_u32 vcc, 1, v91
v_mad_u32_u24 v9, v8, 20, v11
v_lshl_add_u32 v10, v8, 2, v11
v_cndmask_b32 v221, 0, v90, vcc
v_cmp_eq_u32 vcc, 2, v91
v_add_u32 v105, 1600, v9
v_add_u32 v106, 3200, v9
v_cndmask_b32 v222, 0, v90, vcc
v_cmp_eq_u32 vcc, 3, v91
v_add_u32 v107, 4800, v9
v_mov_b64 v[12:13], 0
v_cndmask_b32 v223, 0, v90, vcc
v_min_u32 v47, 160, v15
v_add_u32 v90, 16, v47
v_and_b32 v90, 128, v90
v_lshrrev_b32 v90, 3, v90
v_add_u32 v47, v47, v90
v_add_u32 v14, 87552, v15
v_add_u32 v85, -4, v8
v_cvt_f32_i32 v85, v85
s_and_b32 s45, s10, 1
s_and_b32 s46, s10, 2
s_mul_i32 s45, s45, -32
s_mul_i32 s46, s46, -32
s_mul_i32 s47, s10, -32
s_lshl4_add_u32 s44, s10, 3036676096
v_add_u32 v16, s41, v15
s_mul_i32 s28, s45, 384
v_add_u32 v17, s28, v16
s_mul_i32 s28, s46, 384
v_add_u32 v18, s28, v16
s_mul_i32 s28, s47, 384
v_add_u32 v19, s28, v16
v_add_u32 v20, 49152, v16
v_add_u32 v21, 49152, v17
v_add_u32 v22, 49152, v18
v_add_u32 v23, 49152, v19
v_mul_u32_u24 v46, 12, v7
v_add_lshl_u32 v46, v46, v8, 4
s_mov_b32 s43, 0
s_mov_b32 s72, 1167065088
s_mov_b32 s73, 1129316352
s_mov_b32 s74, 4294901760
s_mov_b32 s42, 67108864
s_mov_b32 s75, 4096
s_mov_b32 s49, 1130316639
v_mul_u32_u24 v90, 43008, v56
v_add_u32 v90, s14, v90
v_max_i32 v90, s53, v90
v_min_i32 v90, s54, v90
v_add3_u32 v90, v90, v5, s50
v_add_u32 v29, v90, v57
s_sub_i32 s11, s25, s13
s_mul_i32 s11, s11, 4608
s_mul_i32 s28, s23, 208
s_sub_i32 s11, s11, s28
s_add_i32 s11, s11, 8389232
v_cvt_f32_i32 v86, s11
v_mad_u32_u24 v87, v8, 48, s11
v_cvt_f32_i32 v87, v87
v_cvt_f32_i32 v80, s21
v_cvt_f32_i32 v83, s22
v_add3_u32 v84, s23, v7, 2
v_cvt_f32_i32 v84, v84
v_add_f32 v80, 2.0, v80
v_add_f32 v83, 2.0, v83
v_add_f32 v81, 1.0, v80
v_add_f32 v82, 2.0, v80
s_cmp_lt_u32 s23, 48
s_cselect_b32 s11, 8, 4
s_cmp_lt_u32 s21, 36
s_cselect_b32 s11, 8, s11
s_cmp_eq_u32 s24, 8
s_cselect_b32 s11, 12, s11
s_cmp_eq_u32 s24, 13
s_cselect_b32 s11, 0, s11
s_add_u32 s11, s11, s10
s_lshl4_add_u32 s11, s11, 12
s_getpc_b64 s[30:31]
s_add_u32 s30, s30, s11
s_addc_u32 s31, s31, 0
s_setpc_b64 s[30:31]
s_call_b64 s[58:59], T_s1_%=
s_call_b64 s[58:59], T_t130_%=
s_branch L_pd_%=
s_nop 0
s_call_b64 s[58:59], T_ef_%=
s_branch L_pd_%=
s_nop 0
s_nop 0
s_call_b64 s[58:59], T_e130_%=
s_branch L_pd_%=
s_nop 0
s_nop 0
s_call_b64 s[58:59], T_b130_%=
s_branch L_pd_%=
s_nop 0
s_nop 0
s_call_b64 s[58:59], T_ef_%=
s_branch L_pd_%=
s_nop 0
s_nop 0
s_call_b64 s[58:59], T_e120_%=
s_branch L_pd_%=
s_nop 0
s_nop 0
s_call_b64 s[58:59], T_b120_%=
s_branch L_pd_%=
s_nop 0
s_nop 0
s_call_b64 s[58:59], T_t120_%=
s_branch L_pd_%=
s_nop 0
s_nop 0
s_call_b64 s[58:59], T_ef_%=
s_call_b64 s[58:59], T_t121_%=
s_branch L_pd_%=
s_nop 0
s_call_b64 s[58:59], T_e121_%=
s_call_b64 s[58:59], T_b120_%=
s_branch L_pd_%=
s_nop 0
s_call_b64 s[58:59], T_b121_%=
s_call_b64 s[58:59], T_t120_%=
s_branch L_pd_%=
s_nop 0
s_call_b64 s[58:59], T_a121_%=
s_call_b64 s[58:59], T_e120_%=
s_branch L_pd_%=
s_nop 0
s_call_b64 s[58:59], T_ef_%=
s_call_b64 s[58:59], T_b82_%=
s_call_b64 s[58:59], T_e81_%=
s_branch L_pd_%=
s_call_b64 s[58:59], T_md_%=
s_call_b64 s[58:59], T_a82_%=
s_call_b64 s[58:59], T_b81_%=
s_branch L_pd_%=
s_call_b64 s[58:59], T_e82_%=
s_call_b64 s[58:59], T_a82x_%=
s_call_b64 s[58:59], T_a81_%=
s_branch L_pd_%=
s_call_b64 s[58:59], T_e82x_%=
s_call_b64 s[58:59], T_t82_%=
s_call_b64 s[58:59], T_t81_%=
s_branch L_pd_%=
L_fx_%=:
s_bitcmp1_b32 s12, 26
s_cbranch_scc1 L_fd_%=
s_movk_i32 s11, 223
s_max_i32 s28, s26, 0
v_add_u32 v88, s26, v52
v_med3_i32 v88, v88, 0, s11
v_add_u32 v89, s28, v52
v_sub_u32 v88, v88, v89
v_mul_i32_i24 v88, 192, v88
v_add_u32 v24, v24, v88
v_add_u32 v88, s26, v53
v_med3_i32 v88, v88, 0, s11
v_add_u32 v89, s28, v53
v_sub_u32 v88, v88, v89
v_mul_i32_i24 v88, 192, v88
v_add_u32 v25, v25, v88
v_add_u32 v88, s26, v54
v_med3_i32 v88, v88, 0, s11
v_add_u32 v89, s28, v54
v_sub_u32 v88, v88, v89
v_mul_i32_i24 v88, 192, v88
v_add_u32 v26, v26, v88
v_add_u32 v88, s26, v55
v_med3_i32 v88, v88, 0, s11
v_add_u32 v89, s28, v55
v_sub_u32 v88, v88, v89
v_mul_i32_i24 v88, 192, v88
v_add_u32 v27, v27, v88
v_add_u32 v88, s26, v56
v_med3_i32 v88, v88, 0, s11
v_add_u32 v89, s28, v56
v_sub_u32 v88, v88, v89
v_mul_i32_i24 v88, 192, v88
v_add_u32 v28, v28, v88
s_addk_i32 s50, -576
s_branch L_fxr_%=
L_fd_%=:
s_cmp_eq_u32 s10, 0
s_cselect_b32 s11, 0, -384000
s_add_i32 s18, s48, 344064
s_add_i32 s18, s18, s11
s_add_i32 s19, s48, 132096
s_add_i32 s20, s14, 304128
s_add_i32 s51, s14, 347136
s_mov_b32 s52, s51
v_mov_b32 v88, -384000
v_cmp_le_u32 vcc, 9, v56
s_add_i32 s54, s14, 731136
s_nop 0
v_cndmask_b32 v5, 0, v88, vcc
s_branch L_fxr_%=
L_p3_%=:
s_waitcnt vmcnt(9)
s_mov_b64 exec, 1
buffer_store_dwordx2 v[114:115], off, s[36:39], s78 offset:8 sc1
s_mov_b64 exec, -1
s_branch L_p3r_%=
L_pu_%=:
s_mov_b64 exec, 1
s_mov_b32 s27, -1
s_waitcnt vmcnt(0)
buffer_store_dwordx2 v[114:115], off, s[36:39], s78 offset:8 sc1
s_mov_b64 exec, -1
s_branch L_spr_%=
.p2align 12
L_pd_%=:
s_waitcnt vmcnt(7)
buffer_load_dwordx4 a[48:51], v24, s[80:83], s16 offen
s_waitcnt vmcnt(7)
buffer_load_dwordx4 a[52:55], v25, s[80:83], s16 offen
s_waitcnt vmcnt(7)
s_addk_i32 m0, 4096
s_addk_i32 s40, 4096
buffer_load_dwordx4 v230, s[64:67], s40 offen offset:0 lds
s_cmp_lt_u32 s27, 75
s_waitcnt vmcnt(13)
s_cbranch_scc1 L_pw_%=
s_waitcnt vmcnt(7)
L_pw_%=:
ds_read_b32 v54, v11 offset:96
ds_read_b32 v53, v10 offset:80
ds_read_b32 v62, v11 offset:1696
ds_read_b32 v61, v10 offset:1680
ds_read_b32 v52, v9 offset:16
ds_read2_b32 v[50:51], v9 offset0:2 offset1:3
ds_read2_b32 v[48:49], v9 offset0:0 offset1:1
ds_read_b32 v60, v9 offset:1616
ds_read2_b32 v[58:59], v105 offset0:2 offset1:3
ds_read2_b32 v[56:57], v105 offset0:0 offset1:1
s_add_u32 s40, s40, 90624
buffer_load_dwordx4 v15, s[64:67], s40 offen offset:1024 lds
s_waitcnt lgkmcnt(6)
v_lshlrev_b32 v88, 16, v54
v_lshlrev_b32 v89, 16, v62
v_and_b32 v90, s74, v54
v_and_b32_e64 v91, s74, v62
v_max3_f32 v12, v12, |v88|, |v89|
v_pk_add_f32 v[88:89], v[88:89], v[80:81]
v_max3_f32 v12, v12, |v90|, |v91|
v_pk_add_f32 v[90:91], v[90:91], v[84:85] op_sel_hi:[1,0]
v_cvt_pk_bf16_f32 v89, v88, v89
v_cvt_pk_bf16_f32 v91, v90, v91
v_pk_min_i16 v89, v89, s49
v_lshlrev_b32 v90, 16, v91
v_and_b32 v91, s74, v91
v_lshlrev_b32 v88, 16, v89
v_and_b32 v89, s74, v89
v_rndne_f32 v94, v90
v_rndne_f32 v95, v91
v_rndne_f32 v92, v88
v_rndne_f32 v93, v89
v_pk_add_f32 v[90:91], v[90:91], v[94:95] neg_lo:[0,1] neg_hi:[0,1]
v_pk_add_f32 v[88:89], v[88:89], v[92:93] neg_lo:[0,1] neg_hi:[0,1]
v_pk_fma_f32 v[94:95], v[94:95], s[72:73], v[86:87] op_sel:[0,1,1] op_sel_hi:[1,1,1]
v_cvt_pk_f16_f32 v156, v88, v90
v_cvt_pk_f16_f32 v157, v89, v91
v_pk_fma_f32 v[152:153], v[92:93], s[72:73], v[94:95] op_sel_hi:[1,0,1]
v_or_b32 v232, v156, v157
v_add_f32 v84, v84, v85
buffer_load_dwordx4 a[56:59], v26, s[80:83], s16 offen
ds_read_b32 v70, v11 offset:3296
ds_read_b32 v69, v10 offset:3280
ds_read_b32 v68, v9 offset:3216
ds_read2_b32 v[66:67], v106 offset0:2 offset1:3
ds_read2_b32 v[64:65], v106 offset0:0 offset1:1
v_lshlrev_b32 v88, 16, v53
v_lshlrev_b32 v89, 16, v61
v_and_b32 v90, s74, v53
v_and_b32 v91, s74, v61
v_max3_f32 v12, v12, |v88|, |v89|
v_pk_add_f32 v[88:89], v[88:89], v[80:81]
v_max3_f32 v12, v12, |v90|, |v91|
v_pk_add_f32 v[90:91], v[90:91], v[84:85] op_sel_hi:[1,0]
v_cvt_pk_bf16_f32 v89, v88, v89
v_cvt_pk_bf16_f32 v91, v90, v91
v_pk_min_i16 v89, v89, s49
v_lshlrev_b32 v90, 16, v91
v_and_b32 v91, s74, v91
v_lshlrev_b32 v88, 16, v89
v_and_b32 v89, s74, v89
v_rndne_f32 v94, v90
v_rndne_f32 v95, v91
v_rndne_f32 v92, v88
v_rndne_f32 v93, v89
v_pk_add_f32 v[90:91], v[90:91], v[94:95] neg_lo:[0,1] neg_hi:[0,1]
v_pk_add_f32 v[88:89], v[88:89], v[92:93] neg_lo:[0,1] neg_hi:[0,1]
v_pk_fma_f32 v[94:95], v[94:95], s[72:73], v[86:87] op_sel:[0,1,0] op_sel_hi:[1,1,0]
v_cvt_pk_f16_f32 v148, v88, v90
v_cvt_pk_f16_f32 v149, v89, v91
v_pk_fma_f32 v[144:145], v[92:93], s[72:73], v[94:95] op_sel_hi:[1,0,1]
v_or3_b32 v232, v232, v148, v149
v_sub_f32 v84, v84, v85
v_pk_add_f32 v[80:81], v[80:81], v[84:85] op_sel:[0,1]
s_cmp_lt_u32 s27, 75
s_cbranch_scc1 L_p3_%=
L_p3r_%=:
buffer_load_dwordx4 a[60:63], v27, s[80:83], s16 offen
ds_read_b32 v78, v11 offset:4896
ds_read_b32 v77, v10 offset:4880
ds_read_b32 v76, v9 offset:4816
ds_read2_b32 v[74:75], v107 offset0:2 offset1:3
ds_read2_b32 v[72:73], v107 offset0:0 offset1:1
s_waitcnt lgkmcnt(10)
v_lshlrev_b32 v88, 16, v52
v_lshlrev_b32 v89, 16, v60
v_and_b32 v90, s74, v52
v_and_b32_e64 v91, s74, v60
v_max3_f32 v12, v12, |v88|, |v89|
v_pk_add_f32 v[88:89], v[88:89], v[80:81]
v_max3_f32 v12, v12, |v90|, |v91|
v_pk_add_f32 v[90:91], v[90:91], v[84:85] op_sel_hi:[1,0]
v_cvt_pk_bf16_f32 v89, v88, v89
v_cvt_pk_bf16_f32 v91, v90, v91
v_pk_min_i16 v89, v89, s49
v_lshlrev_b32 v90, 16, v91
v_and_b32 v91, s74, v91
v_lshlrev_b32 v88, 16, v89
v_and_b32 v89, s74, v89
v_rndne_f32 v94, v90
v_rndne_f32 v95, v91
v_rndne_f32 v92, v88
v_rndne_f32 v93, v89
v_pk_add_f32 v[90:91], v[90:91], v[94:95] neg_lo:[0,1] neg_hi:[0,1]
v_pk_add_f32 v[88:89], v[88:89], v[92:93] neg_lo:[0,1] neg_hi:[0,1]
v_pk_fma_f32 v[94:95], v[94:95], s[72:73], v[86:87] op_sel:[0,1,0] op_sel_hi:[1,1,0]
v_cvt_pk_f16_f32 v140, v88, v90
v_cvt_pk_f16_f32 v141, v89, v91
v_pk_fma_f32 v[136:137], v[92:93], s[72:73], v[94:95] op_sel_hi:[1,0,1]
v_or3_b32 v232, v232, v140, v141
v_add_f32_e64 v84, -1.0, v84
buffer_load_dwordx4 a[64:67], v24, s[80:83], s17 offen
v_lshlrev_b32 v88, 16, v51
v_lshlrev_b32 v89, 16, v59
v_and_b32 v90, s74, v51
v_and_b32 v91, s74, v59
v_max3_f32 v12, v12, |v88|, |v89|
v_pk_add_f32 v[88:89], v[88:89], v[80:81]
v_max3_f32 v12, v12, |v90|, |v91|
v_pk_add_f32 v[90:91], v[90:91], v[84:85] op_sel_hi:[1,0]
v_cvt_pk_bf16_f32 v89, v88, v89
v_cvt_pk_bf16_f32 v91, v90, v91
v_pk_min_i16 v89, v89, s49
v_lshlrev_b32 v90, 16, v91
v_and_b32 v91, s74, v91
v_lshlrev_b32 v88, 16, v89
v_and_b32 v89, s74, v89
v_rndne_f32 v94, v90
v_rndne_f32 v95, v91
v_rndne_f32 v92, v88
v_rndne_f32 v93, v89
v_pk_add_f32 v[90:91], v[90:91], v[94:95] neg_lo:[0,1] neg_hi:[0,1]
v_pk_add_f32 v[88:89], v[88:89], v[92:93] neg_lo:[0,1] neg_hi:[0,1]
v_pk_fma_f32 v[94:95], v[94:95], s[72:73], v[86:87] op_sel:[0,1,0] op_sel_hi:[1,1,0]
v_cvt_pk_f16_f32 v132, v88, v90
v_cvt_pk_f16_f32 v133, v89, v91
v_pk_fma_f32 v[128:129], v[92:93], s[72:73], v[94:95] op_sel_hi:[1,0,1]
v_or3_b32 v232, v232, v132, v133
v_add_f32_e64 v84, -1.0, v84
buffer_load_dwordx4 a[68:71], v25, s[80:83], s17 offen
v_lshlrev_b32 v88, 16, v50
v_lshlrev_b32 v89, 16, v58
v_and_b32 v90, s74, v50
v_and_b32 v91, s74, v58
v_max3_f32 v12, v12, |v88|, |v89|
v_pk_add_f32 v[88:89], v[88:89], v[80:81]
v_max3_f32 v12, v12, |v90|, |v91|
v_pk_add_f32 v[90:91], v[90:91], v[84:85] op_sel_hi:[1,0]
v_cvt_pk_bf16_f32 v89, v88, v89
v_cvt_pk_bf16_f32 v91, v90, v91
v_pk_min_i16 v89, v89, s49
v_lshlrev_b32 v90, 16, v91
v_and_b32 v91, s74, v91
v_lshlrev_b32 v88, 16, v89
v_and_b32 v89, s74, v89
v_rndne_f32 v94, v90
v_rndne_f32 v95, v91
v_rndne_f32 v92, v88
v_rndne_f32 v93, v89
v_pk_add_f32 v[90:91], v[90:91], v[94:95] neg_lo:[0,1] neg_hi:[0,1]
v_pk_add_f32 v[88:89], v[88:89], v[92:93] neg_lo:[0,1] neg_hi:[0,1]
v_pk_fma_f32 v[94:95], v[94:95], s[72:73], v[86:87] op_sel:[0,1,0] op_sel_hi:[1,1,0]
v_cvt_pk_f16_f32 v124, v88, v90
v_cvt_pk_f16_f32 v125, v89, v91
v_pk_fma_f32 v[120:121], v[92:93], s[72:73], v[94:95] op_sel_hi:[1,0,1]
v_or3_b32 v232, v232, v124, v125
v_add_f32_e64 v84, -1.0, v84
buffer_load_dwordx4 a[72:75], v26, s[80:83], s17 offen
v_lshlrev_b32 v88, 16, v49
v_lshlrev_b32 v89, 16, v57
v_and_b32 v90, s74, v49
v_and_b32 v91, s74, v57
v_max3_f32 v12, v12, |v88|, |v89|
v_pk_add_f32 v[88:89], v[88:89], v[80:81]
v_max3_f32 v12, v12, |v90|, |v91|
v_pk_add_f32 v[90:91], v[90:91], v[84:85] op_sel_hi:[1,0]
v_cvt_pk_bf16_f32 v89, v88, v89
v_cvt_pk_bf16_f32 v91, v90, v91
v_pk_min_i16 v89, v89, s49
v_lshlrev_b32 v90, 16, v91
v_and_b32 v91, s74, v91
v_lshlrev_b32 v88, 16, v89
v_and_b32 v89, s74, v89
v_rndne_f32 v94, v90
v_rndne_f32 v95, v91
v_rndne_f32 v92, v88
v_rndne_f32 v93, v89
v_pk_add_f32 v[90:91], v[90:91], v[94:95] neg_lo:[0,1] neg_hi:[0,1]
v_pk_add_f32 v[88:89], v[88:89], v[92:93] neg_lo:[0,1] neg_hi:[0,1]
v_pk_fma_f32 v[94:95], v[94:95], s[72:73], v[86:87] op_sel:[0,1,0] op_sel_hi:[1,1,0]
v_cvt_pk_f16_f32 v116, v88, v90
v_cvt_pk_f16_f32 v117, v89, v91
v_pk_fma_f32 v[112:113], v[92:93], s[72:73], v[94:95] op_sel_hi:[1,0,1]
v_or3_b32 v232, v232, v116, v117
v_add_f32_e64 v84, -1.0, v84
buffer_load_dwordx4 a[76:79], v27, s[80:83], s17 offen
v_lshlrev_b32 v88, 16, v48
v_lshlrev_b32 v89, 16, v56
v_and_b32 v90, s74, v48
v_and_b32 v91, s74, v56
v_max3_f32 v12, v12, |v88|, |v89|
v_pk_add_f32 v[88:89], v[88:89], v[80:81]
v_max3_f32 v12, v12, |v90|, |v91|
v_pk_add_f32 v[90:91], v[90:91], v[84:85] op_sel_hi:[1,0]
v_cvt_pk_bf16_f32 v89, v88, v89
v_cvt_pk_bf16_f32 v91, v90, v91
v_pk_min_i16 v89, v89, s49
v_lshlrev_b32 v90, 16, v91
v_and_b32 v91, s74, v91
v_lshlrev_b32 v88, 16, v89
v_and_b32 v89, s74, v89
v_rndne_f32 v94, v90
v_rndne_f32 v95, v91
v_rndne_f32 v92, v88
v_rndne_f32 v93, v89
v_pk_add_f32 v[90:91], v[90:91], v[94:95] neg_lo:[0,1] neg_hi:[0,1]
v_pk_add_f32 v[88:89], v[88:89], v[92:93] neg_lo:[0,1] neg_hi:[0,1]
v_pk_fma_f32 v[94:95], v[94:95], s[72:73], v[86:87] op_sel:[0,1,0] op_sel_hi:[1,1,0]
v_cvt_pk_f16_f32 v108, v88, v90
v_cvt_pk_f16_f32 v109, v89, v91
v_pk_fma_f32 v[104:105], v[92:93], s[72:73], v[94:95] op_sel_hi:[1,0,1]
v_or3_b32 v232, v232, v108, v109
buffer_load_dwordx4 a[80:83], v24, s[80:83], s18 offen
s_waitcnt lgkmcnt(0)
s_cmp_eq_u32 s24, 13
s_cbranch_scc1 L_s1_%=
L_s2_%=:
v_mov_b32 v16, v14
v_add_u32 v20, 49152, v14
s_mov_b32 s44, 3036676096
s_cmp_lt_u32 s24, 12
s_cbranch_scc1 L_s3_%=
v_add_f32 v83, v82, v85
v_mov_b32 v80, v84
v_add_f32 v81, 1.0, v84
v_lshlrev_b32 v88, 16, v64
v_lshlrev_b32 v89, 16, v65
v_and_b32 v90, s74, v64
v_and_b32_e64 v91, s74, v65
v_max3_f32 v12, v12, |v88|, |v89|
v_pk_add_f32 v[88:89], v[88:89], v[82:83] op_sel:[0,1]
v_max3_f32 v12, v12, |v90|, |v91|
v_pk_add_f32 v[90:91], v[90:91], v[80:81]
v_cvt_pk_bf16_f32 v89, v88, v89
v_cvt_pk_bf16_f32 v91, v90, v91
v_pk_min_i16 v89, v89, s49
v_lshlrev_b32 v90, 16, v91
v_and_b32 v91, s74, v91
v_lshlrev_b32 v88, 16, v89
v_and_b32 v89, s74, v89
v_rndne_f32 v94, v90
v_rndne_f32 v95, v91
v_rndne_f32 v92, v88
v_rndne_f32 v93, v89
v_pk_add_f32 v[90:91], v[90:91], v[94:95] neg_lo:[0,1] neg_hi:[0,1]
v_pk_add_f32 v[88:89], v[88:89], v[92:93] neg_lo:[0,1] neg_hi:[0,1]
v_pk_fma_f32 v[94:95], v[94:95], s[72:73], v[86:87] op_sel:[0,1,0] op_sel_hi:[1,1,0]
v_cvt_pk_f16_f32 v110, v88, v90
v_cvt_pk_f16_f32 v118, v89, v91
v_fma_f32 v106, v92, s72, v94
v_fma_f32 v114, v93, s72, v95
v_or3_b32 v232, v232, v110, v118
v_pk_add_f32 v[80:81], v[80:81], 2.0 op_sel_hi:[1,0]
v_lshlrev_b32 v88, 16, v66
v_lshlrev_b32 v89, 16, v67
v_and_b32 v90, s74, v66
v_and_b32 v91, s74, v67
v_max3_f32 v12, v12, |v88|, |v89|
v_pk_add_f32 v[88:89], v[88:89], v[82:83] op_sel:[0,1]
v_max3_f32 v12, v12, |v90|, |v91|
v_pk_add_f32 v[90:91], v[90:91], v[80:81]
v_cvt_pk_bf16_f32 v89, v88, v89
v_cvt_pk_bf16_f32 v91, v90, v91
v_pk_min_i16 v89, v89, s49
v_lshlrev_b32 v90, 16, v91
v_and_b32 v91, s74, v91
v_lshlrev_b32 v88, 16, v89
v_and_b32 v89, s74, v89
v_rndne_f32 v94, v90
v_rndne_f32 v95, v91
v_rndne_f32 v92, v88
v_rndne_f32 v93, v89
v_pk_add_f32 v[90:91], v[90:91], v[94:95] neg_lo:[0,1] neg_hi:[0,1]
v_pk_add_f32 v[88:89], v[88:89], v[92:93] neg_lo:[0,1] neg_hi:[0,1]
v_pk_fma_f32 v[94:95], v[94:95], s[72:73], v[86:87] op_sel:[0,1,0] op_sel_hi:[1,1,0]
v_cvt_pk_f16_f32 v126, v88, v90
v_cvt_pk_f16_f32 v134, v89, v91
v_fma_f32 v122, v92, s72, v94
v_fma_f32 v130, v93, s72, v95
v_or3_b32 v232, v232, v126, v134
v_pk_add_f32 v[80:81], v[80:81], 2.0 op_sel_hi:[1,0]
buffer_load_dwordx4 a[84:87], v25, s[80:83], s18 offen
v_lshlrev_b32 v88, 16, v68
v_lshlrev_b32 v89, 16, v70
v_and_b32 v90, s74, v68
v_and_b32 v91, s74, v70
v_max3_f32 v12, v12, |v88|, |v89|
v_pk_add_f32 v[88:89], v[88:89], v[82:83] op_sel:[0,1] op_sel_hi:[1,0]
v_max3_f32 v12, v12, |v90|, |v91|
v_pk_add_f32 v[90:91], v[90:91], v[80:81] op_sel_hi:[1,0]
v_cvt_pk_bf16_f32 v89, v88, v89
v_cvt_pk_bf16_f32 v91, v90, v91
v_pk_min_i16 v89, v89, s49
v_lshlrev_b32 v90, 16, v91
v_and_b32 v91, s74, v91
v_lshlrev_b32 v88, 16, v89
v_and_b32 v89, s74, v89
v_rndne_f32 v94, v90
v_rndne_f32 v95, v91
v_rndne_f32 v92, v88
v_rndne_f32 v93, v89
v_pk_add_f32 v[90:91], v[90:91], v[94:95] neg_lo:[0,1] neg_hi:[0,1]
v_pk_add_f32 v[88:89], v[88:89], v[92:93] neg_lo:[0,1] neg_hi:[0,1]
v_pk_fma_f32 v[94:95], v[94:95], s[72:73], v[86:87] op_sel:[0,1,0] op_sel_hi:[1,1,1]
v_cvt_pk_f16_f32 v142, v88, v90
v_cvt_pk_f16_f32 v158, v89, v91
v_fma_f32 v138, v92, s72, v94
v_fma_f32 v154, v93, s72, v95
v_or3_b32 v232, v232, v142, v158
v_add_f32_e64 v80, v80, v85
buffer_load_dwordx4 a[88:91], v26, s[80:83], s18 offen
v_lshlrev_b32 v88, 16, v69
v_lshlrev_b32 v89, 16, v69
v_and_b32 v90, s74, v69
v_and_b32 v91, s74, v69
v_max3_f32 v12, v12, |v88|, |v89|
v_pk_add_f32 v[88:89], v[88:89], v[82:83] op_sel_hi:[1,0]
v_max3_f32 v12, v12, |v90|, |v91|
v_pk_add_f32 v[90:91], v[90:91], v[80:81] op_sel_hi:[1,0]
v_cvt_pk_bf16_f32 v89, v88, v89
v_cvt_pk_bf16_f32 v91, v90, v91
v_pk_min_i16 v89, v89, s49
v_lshlrev_b32 v90, 16, v91
v_and_b32 v91, s74, v91
v_lshlrev_b32 v88, 16, v89
v_and_b32 v89, s74, v89
v_rndne_f32 v94, v90
v_rndne_f32 v95, v91
v_rndne_f32 v92, v88
v_rndne_f32 v93, v89
v_pk_add_f32 v[90:91], v[90:91], v[94:95] neg_lo:[0,1] neg_hi:[0,1]
v_pk_add_f32 v[88:89], v[88:89], v[92:93] neg_lo:[0,1] neg_hi:[0,1]
v_pk_fma_f32 v[94:95], v[94:95], s[72:73], v[86:87] op_sel:[0,1,0] op_sel_hi:[1,1,0]
v_cvt_pk_f16_f32 v150, v88, v90
v_cvt_pk_f16_f32 v150, v89, v91
v_fma_f32 v146, v92, s72, v94
v_fma_f32 v146, v93, s72, v95
v_or3_b32 v232, v232, v150, v150
buffer_load_dwordx4 a[92:95], v27, s[80:83], s18 offen
v_cmp_ne_u32 vcc, 0, v232
s_cbranch_vccz L_s2r_%=
s_mov_b32 s42, 2
v_or3_b32 v92, v156, v157, v158
v_mad_u32_u16 v232, v156, v156, 0 op_sel:[0,1,0,0] clamp
v_cmp_ne_u32 vcc, 0, v92
v_mad_u32_u16 v232, v157, v157, v232 op_sel:[0,1,0,0] clamp
v_mad_u32_u16 v232, v158, v158, v232 op_sel:[0,1,0,0] clamp
s_cmp_lg_u64 vcc, 0
s_addc_u32 s42, s42, s42
v_or3_b32 v92, v148, v149, v150
v_mad_u32_u16 v232, v148, v148, v232 op_sel:[0,1,0,0] clamp
v_cmp_ne_u32 vcc, 0, v92
v_mad_u32_u16 v232, v149, v149, v232 op_sel:[0,1,0,0] clamp
v_mad_u32_u16 v232, v150, v150, v232 op_sel:[0,1,0,0] clamp
s_lshl_b32 s42, s42, 3
s_cmp_lg_u64 vcc, 0
s_addc_u32 s42, s42, s42
v_or3_b32 v92, v140, v141, v142
v_mad_u32_u16 v232, v140, v140, v232 op_sel:[0,1,0,0] clamp
v_cmp_ne_u32 vcc, 0, v92
v_mad_u32_u16 v232, v141, v141, v232 op_sel:[0,1,0,0] clamp
v_mad_u32_u16 v232, v142, v142, v232 op_sel:[0,1,0,0] clamp
s_lshl_b32 s42, s42, 3
s_cmp_lg_u64 vcc, 0
s_addc_u32 s42, s42, s42
v_or3_b32 v92, v132, v133, v134
v_mad_u32_u16 v232, v132, v132, v232 op_sel:[0,1,0,0] clamp
v_cmp_ne_u32 vcc, 0, v92
v_mad_u32_u16 v232, v133, v133, v232 op_sel:[0,1,0,0] clamp
v_mad_u32_u16 v232, v134, v134, v232 op_sel:[0,1,0,0] clamp
s_lshl_b32 s42, s42, 3
s_cmp_lg_u64 vcc, 0
s_addc_u32 s42, s42, s42
v_or3_b32 v92, v124, v125, v126
v_mad_u32_u16 v232, v124, v124, v232 op_sel:[0,1,0,0] clamp
v_cmp_ne_u32 vcc, 0, v92
v_mad_u32_u16 v232, v125, v125, v232 op_sel:[0,1,0,0] clamp
v_mad_u32_u16 v232, v126, v126, v232 op_sel:[0,1,0,0] clamp
s_lshl_b32 s42, s42, 3
s_cmp_lg_u64 vcc, 0
s_addc_u32 s42, s42, s42
v_or3_b32 v92, v116, v117, v118
v_mad_u32_u16 v232, v116, v116, v232 op_sel:[0,1,0,0] clamp
v_cmp_ne_u32 vcc, 0, v92
v_mad_u32_u16 v232, v117, v117, v232 op_sel:[0,1,0,0] clamp
v_mad_u32_u16 v232, v118, v118, v232 op_sel:[0,1,0,0] clamp
s_lshl_b32 s42, s42, 3
s_cmp_lg_u64 vcc, 0
s_addc_u32 s42, s42, s42
v_or3_b32 v92, v108, v109, v110
v_mad_u32_u16 v232, v108, v108, v232 op_sel:[0,1,0,0] clamp
v_cmp_ne_u32 vcc, 0, v92
v_mad_u32_u16 v232, v109, v109, v232 op_sel:[0,1,0,0] clamp
v_mad_u32_u16 v232, v110, v110, v232 op_sel:[0,1,0,0] clamp
s_lshl_b32 s42, s42, 3
s_cmp_lg_u64 vcc, 0
s_addc_u32 s42, s42, s42
s_branch L_s2r_%=
L_s3_%=:
buffer_load_dwordx4 a[84:87], v25, s[80:83], s18 offen
buffer_load_dwordx4 a[88:91], v26, s[80:83], s18 offen
buffer_load_dwordx4 a[92:95], v27, s[80:83], s18 offen
s_branch L_s2r_%=
.p2align 6
s_nop 0
T_s1_%=:
s_setpc_b64 s[58:59]
L_s1_%=:
v_pk_add_f32 v[80:81], v[82:83], v[84:85] op_sel:[0,1]
v_lshlrev_b32 v88, 16, v64
v_lshlrev_b32 v89, 16, v72
v_and_b32 v90, s74, v64
v_and_b32 v91, s74, v72
v_max3_f32 v12, v12, |v88|, |v89|
v_pk_add_f32 v[88:89], v[88:89], v[80:81]
v_max3_f32 v12, v12, |v90|, |v91|
v_pk_add_f32 v[90:91], v[90:91], v[84:85] op_sel_hi:[1,0]
v_cvt_pk_bf16_f32 v89, v88, v89
v_cvt_pk_bf16_f32 v91, v90, v91
v_pk_min_i16 v89, v89, s49
v_lshlrev_b32 v90, 16, v91
v_and_b32 v91, s74, v91
v_lshlrev_b32 v88, 16, v89
v_and_b32 v89, s74, v89
v_rndne_f32 v94, v90
v_rndne_f32 v95, v91
v_rndne_f32 v92, v88
v_rndne_f32 v93, v89
v_pk_add_f32 v[90:91], v[90:91], v[94:95] neg_lo:[0,1] neg_hi:[0,1]
v_pk_add_f32 v[88:89], v[88:89], v[92:93] neg_lo:[0,1] neg_hi:[0,1]
v_pk_fma_f32 v[94:95], v[94:95], s[72:73], v[86:87] op_sel:[0,1,0] op_sel_hi:[1,1,0]
v_cvt_pk_f16_f32 v110, v88, v90
v_cvt_pk_f16_f32 v111, v89, v91
v_pk_fma_f32 v[106:107], v[92:93], s[72:73], v[94:95] op_sel_hi:[1,0,1]
v_or3_b32 v232, v232, v110, v111
v_add_f32 v84, 1.0, v84
v_lshlrev_b32 v88, 16, v65
v_lshlrev_b32 v89, 16, v73
v_and_b32 v90, s74, v65
v_and_b32_e64 v91, s74, v73
v_max3_f32 v12, v12, |v88|, |v89|
v_pk_add_f32 v[88:89], v[88:89], v[80:81]
v_max3_f32 v12, v12, |v90|, |v91|
v_pk_add_f32 v[90:91], v[90:91], v[84:85] op_sel_hi:[1,0]
v_cvt_pk_bf16_f32 v89, v88, v89
v_cvt_pk_bf16_f32 v91, v90, v91
v_pk_min_i16 v89, v89, s49
v_lshlrev_b32 v90, 16, v91
v_and_b32 v91, s74, v91
v_lshlrev_b32 v88, 16, v89
v_and_b32 v89, s74, v89
v_rndne_f32 v94, v90
v_rndne_f32 v95, v91
v_rndne_f32 v92, v88
v_rndne_f32 v93, v89
v_pk_add_f32 v[90:91], v[90:91], v[94:95] neg_lo:[0,1] neg_hi:[0,1]
v_pk_add_f32 v[88:89], v[88:89], v[92:93] neg_lo:[0,1] neg_hi:[0,1]
v_pk_fma_f32 v[94:95], v[94:95], s[72:73], v[86:87] op_sel:[0,1,0] op_sel_hi:[1,1,0]
v_cvt_pk_f16_f32 v118, v88, v90
v_cvt_pk_f16_f32 v119, v89, v91
v_pk_fma_f32 v[114:115], v[92:93], s[72:73], v[94:95] op_sel_hi:[1,0,1]
v_or3_b32 v232, v232, v118, v119
v_add_f32 v84, 1.0, v84
v_lshlrev_b32 v88, 16, v66
v_lshlrev_b32 v89, 16, v74
v_and_b32 v90, s74, v66
v_and_b32_e64 v91, s74, v74
v_max3_f32 v12, v12, |v88|, |v89|
v_pk_add_f32 v[88:89], v[88:89], v[80:81]
v_max3_f32 v12, v12, |v90|, |v91|
v_pk_add_f32 v[90:91], v[90:91], v[84:85] op_sel_hi:[1,0]
v_cvt_pk_bf16_f32 v89, v88, v89
v_cvt_pk_bf16_f32 v91, v90, v91
v_pk_min_i16 v89, v89, s49
v_lshlrev_b32 v90, 16, v91
v_and_b32 v91, s74, v91
v_lshlrev_b32 v88, 16, v89
v_and_b32 v89, s74, v89
v_rndne_f32 v94, v90
v_rndne_f32 v95, v91
v_rndne_f32 v92, v88
v_rndne_f32 v93, v89
v_pk_add_f32 v[90:91], v[90:91], v[94:95] neg_lo:[0,1] neg_hi:[0,1]
v_pk_add_f32 v[88:89], v[88:89], v[92:93] neg_lo:[0,1] neg_hi:[0,1]
v_pk_fma_f32 v[94:95], v[94:95], s[72:73], v[86:87] op_sel:[0,1,0] op_sel_hi:[1,1,0]
v_cvt_pk_f16_f32 v126, v88, v90
v_cvt_pk_f16_f32 v127, v89, v91
v_pk_fma_f32 v[122:123], v[92:93], s[72:73], v[94:95] op_sel_hi:[1,0,1]
v_or3_b32 v232, v232, v126, v127
v_add_f32_e64 v84, 1.0, v84
buffer_load_dwordx4 a[84:87], v25, s[80:83], s18 offen
v_lshlrev_b32 v88, 16, v67
v_lshlrev_b32 v89, 16, v75
v_and_b32 v90, s74, v67
v_and_b32 v91, s74, v75
v_max3_f32 v12, v12, |v88|, |v89|
v_pk_add_f32 v[88:89], v[88:89], v[80:81]
v_max3_f32 v12, v12, |v90|, |v91|
v_pk_add_f32 v[90:91], v[90:91], v[84:85] op_sel_hi:[1,0]
v_cvt_pk_bf16_f32 v89, v88, v89
v_cvt_pk_bf16_f32 v91, v90, v91
v_pk_min_i16 v89, v89, s49
v_lshlrev_b32 v90, 16, v91
v_and_b32 v91, s74, v91
v_lshlrev_b32 v88, 16, v89
v_and_b32 v89, s74, v89
v_rndne_f32 v94, v90
v_rndne_f32 v95, v91
v_rndne_f32 v92, v88
v_rndne_f32 v93, v89
v_pk_add_f32 v[90:91], v[90:91], v[94:95] neg_lo:[0,1] neg_hi:[0,1]
v_pk_add_f32 v[88:89], v[88:89], v[92:93] neg_lo:[0,1] neg_hi:[0,1]
v_pk_fma_f32 v[94:95], v[94:95], s[72:73], v[86:87] op_sel:[0,1,0] op_sel_hi:[1,1,0]
v_cvt_pk_f16_f32 v134, v88, v90
v_cvt_pk_f16_f32 v135, v89, v91
v_pk_fma_f32 v[130:131], v[92:93], s[72:73], v[94:95] op_sel_hi:[1,0,1]
v_or3_b32 v232, v232, v134, v135
v_add_f32 v84, 1.0, v84
v_lshlrev_b32 v88, 16, v68
v_lshlrev_b32 v89, 16, v76
v_and_b32 v90, s74, v68
v_and_b32_e64 v91, s74, v76
v_max3_f32 v12, v12, |v88|, |v89|
v_pk_add_f32 v[88:89], v[88:89], v[80:81]
v_max3_f32 v12, v12, |v90|, |v91|
v_pk_add_f32 v[90:91], v[90:91], v[84:85] op_sel_hi:[1,0]
v_cvt_pk_bf16_f32 v89, v88, v89
v_cvt_pk_bf16_f32 v91, v90, v91
v_pk_min_i16 v89, v89, s49
v_lshlrev_b32 v90, 16, v91
v_and_b32 v91, s74, v91
v_lshlrev_b32 v88, 16, v89
v_and_b32 v89, s74, v89
v_rndne_f32 v94, v90
v_rndne_f32 v95, v91
v_rndne_f32 v92, v88
v_rndne_f32 v93, v89
v_pk_add_f32 v[90:91], v[90:91], v[94:95] neg_lo:[0,1] neg_hi:[0,1]
v_pk_add_f32 v[88:89], v[88:89], v[92:93] neg_lo:[0,1] neg_hi:[0,1]
v_pk_fma_f32 v[94:95], v[94:95], s[72:73], v[86:87] op_sel:[0,1,0] op_sel_hi:[1,1,0]
v_cvt_pk_f16_f32 v142, v88, v90
v_cvt_pk_f16_f32 v143, v89, v91
v_pk_fma_f32 v[138:139], v[92:93], s[72:73], v[94:95] op_sel_hi:[1,0,1]
v_or3_b32 v232, v232, v142, v143
v_add_f32_e64 v84, v84, v85
buffer_load_dwordx4 a[88:91], v26, s[80:83], s18 offen
v_lshlrev_b32 v88, 16, v69
v_lshlrev_b32 v89, 16, v77
v_and_b32 v90, s74, v69
v_and_b32 v91, s74, v77
v_max3_f32 v12, v12, |v88|, |v89|
v_pk_add_f32 v[88:89], v[88:89], v[82:83]
v_max3_f32 v12, v12, |v90|, |v91|
v_pk_add_f32 v[90:91], v[90:91], v[84:85] op_sel_hi:[1,0]
v_cvt_pk_bf16_f32 v89, v88, v89
v_cvt_pk_bf16_f32 v91, v90, v91
v_pk_min_i16 v89, v89, s49
v_lshlrev_b32 v90, 16, v91
v_and_b32 v91, s74, v91
v_lshlrev_b32 v88, 16, v89
v_and_b32 v89, s74, v89
v_rndne_f32 v94, v90
v_rndne_f32 v95, v91
v_rndne_f32 v92, v88
v_rndne_f32 v93, v89
v_pk_add_f32 v[90:91], v[90:91], v[94:95] neg_lo:[0,1] neg_hi:[0,1]
v_pk_add_f32 v[88:89], v[88:89], v[92:93] neg_lo:[0,1] neg_hi:[0,1]
v_pk_fma_f32 v[94:95], v[94:95], s[72:73], v[86:87] op_sel:[0,1,0] op_sel_hi:[1,1,0]
v_cvt_pk_f16_f32 v150, v88, v90
v_cvt_pk_f16_f32 v151, v89, v91
v_pk_fma_f32 v[146:147], v[92:93], s[72:73], v[94:95] op_sel_hi:[1,0,1]
v_or3_b32 v232, v232, v150, v151
v_sub_f32 v84, v84, v85
v_lshlrev_b32 v88, 16, v70
v_lshlrev_b32 v89, 16, v78
v_and_b32 v90, s74, v70
v_and_b32_e64 v91, s74, v78
v_max3_f32 v12, v12, |v88|, |v89|
v_pk_add_f32 v[88:89], v[88:89], v[82:83]
v_max3_f32 v12, v12, |v90|, |v91|
v_pk_add_f32 v[90:91], v[90:91], v[84:85] op_sel_hi:[1,0]
v_cvt_pk_bf16_f32 v89, v88, v89
v_cvt_pk_bf16_f32 v91, v90, v91
v_pk_min_i16 v89, v89, s49
v_lshlrev_b32 v90, 16, v91
v_and_b32 v91, s74, v91
v_lshlrev_b32 v88, 16, v89
v_and_b32 v89, s74, v89
v_rndne_f32 v94, v90
v_rndne_f32 v95, v91
v_rndne_f32 v92, v88
v_rndne_f32 v93, v89
v_pk_add_f32 v[90:91], v[90:91], v[94:95] neg_lo:[0,1] neg_hi:[0,1]
v_pk_add_f32 v[88:89], v[88:89], v[92:93] neg_lo:[0,1] neg_hi:[0,1]
v_pk_fma_f32 v[94:95], v[94:95], s[72:73], v[86:87] op_sel:[0,1,1] op_sel_hi:[1,1,1]
v_cvt_pk_f16_f32 v158, v88, v90
v_cvt_pk_f16_f32 v159, v89, v91
v_pk_fma_f32 v[154:155], v[92:93], s[72:73], v[94:95] op_sel_hi:[1,0,1]
v_or3_b32 v232, v232, v158, v159
buffer_load_dwordx4 a[92:95], v27, s[80:83], s18 offen
s_cmp_eq_u32 s10, 3
s_cselect_b64 vcc, -1, 0
s_cselect_b32 s25, 0, s10
v_mov_b32 v10, 1258295776
v_or_b32 v11, 0x7fff0000, v46
s_cmp_eq_u32 s25, 2
s_cselect_b32 s61, -48, 0
v_cndmask_b32 v155, v155, v10, vcc
s_cmp_lg_u32 s25, 0
s_cselect_b32 s62, -48, 0
v_cndmask_b32 v159, v159, 0, vcc
s_lshl4_add_u32 s60, s25, 3036676096
v_cndmask_b32 v46, v46, v11, vcc
v_add_u32 v32, s44, v104
v_add_u32 v36, s44, v105
v_add_u32 v40, s44, v106
v_add_u32 v44, s44, v107
v_add_u32 v33, s45, v32
v_add_u32 v37, s45, v36
v_add_u32 v41, s45, v40
v_add_u32 v34, s46, v32
v_add_u32 v38, s46, v36
v_add_u32 v42, s46, v40
v_add_u32 v35, s47, v32
v_add_u32 v39, s47, v36
v_add_u32 v43, s47, v40
s_branch L_s2r_%=
.p2align 12
.p2align 6
s_nop 0
T_e121_%=:
s_setpc_b64 s[58:59]
L_ent12_1_%=:
s_set_gpr_idx_on s43, gpr_idx(SRC1)
v_add_u32 v32, s44, v104
v_add_u32 v36, s44, v105
v_add_u32 v40, s44, v106
v_add_u32 v6, 0, v108
v_add_u32 v7, 0, v109
v_add_u32 v8, 0, v110
s_set_gpr_idx_off
v_pk_max_i16 v25, v6, s50
v_pk_min_i16 v25, v25, s52
v_mad_i32_i16 v33, v25, s53, v32
v_mad_i32_i16 v33, v25, s54, v33 op_sel:[1,0,0,0]
v_pk_max_i16 v27, v7, s50
v_pk_min_i16 v27, v27, s52
v_mad_i32_i16 v37, v27, s53, v36
v_mad_i32_i16 v37, v27, s54, v37 op_sel:[1,0,0,0]
v_pk_max_i16 v29, v8, s50
v_pk_min_i16 v29, v29, s52
v_mad_i32_i16 v41, v29, s53, v40
v_mad_i32_i16 v41, v29, s54, v41 op_sel:[1,0,0,0]
s_waitcnt lgkmcnt(0)
s_barrier
RV 48,32,0
v_and_b32_e64 v6, s56, v6
RV 52,33,0
v_pk_add_f16 v25, v6, v6 op_sel:[0,1] op_sel_hi:[1,0]
RV 56,36,0
v_pk_add_f16 v24, v25, s51 neg_lo:[1,0]
RV 60,37,0
v_and_b32_e64 v7, s56, v7
RV 64,40,0
v_pk_add_f16 v27, v7, v7 op_sel:[0,1] op_sel_hi:[1,0]
RV 68,41,0
v_pk_add_f16 v26, v27, s51 neg_lo:[1,0]
RA 0,16,0
v_and_b32_e64 v8, s56, v8
RA 4,16,1024
v_pk_add_f16 v29, v8, v8 op_sel:[0,1] op_sel_hi:[1,0]
RA 8,16,2048
v_pk_add_f16 v28, v29, s51 neg_lo:[1,0]
RA 12,16,3072
RA 16,16,4096
RA 20,16,5120
s_waitcnt lgkmcnt(6)
RV 232,32,16
RV 236,33,16
PM 72,48,24
PM 73,49,24
PM 74,50,24
PM 75,51,24
v_pk_fmac_f16 v72, v52, v25
PH 73,53,24
RV 240,36,16
RV 244,37,16
v_pk_fmac_f16 v74, v54, v25
v_pk_fmac_f16 v75, v55, v25
PM 76,56,26
PM 77,57,26
PM 78,58,26
PM 79,59,26
RV 248,40,16
RV 252,41,16
v_pk_fmac_f16 v76, v60, v27
v_pk_fmac_f16 v77, v61, v27
v_pk_fmac_f16 v78, v62, v27
v_pk_fmac_f16 v79, v63, v27
PM 80,64,28
PM 81,65,28
RV 48,32,32
RV 52,33,32
RV 56,36,32
RV 60,37,32
PM 82,66,28
PM 83,67,28
v_pk_fmac_f16 v80, v68, v29
v_pk_fmac_f16 v81, v69, v29
PH 82,70,28
v_pk_fmac_f16 v83, v71, v29
s_waitcnt lgkmcnt(4)
s_nop 0
v_mfma_f32_16x16x32_f16 v[160:163], a[0:3], v[72:75], 0
s_add_u32 m0, s41, 24576
RA 24,16,6144
RA 28,16,7168
v_mfma_f32_16x16x32_f16 v[164:167], a[4:7], v[72:75], 0
buffer_load_dwordx4 v15, s[36:39], s40 offen sc1 lds
s_waitcnt lgkmcnt(4)
PM 88,232,24
PM 89,233,24
v_mfma_f32_16x16x32_f16 v[168:171], a[8:11], v[72:75], 0
PM 90,234,24
PM 91,235,24
v_mfma_f32_16x16x32_f16 v[172:175], a[12:15], v[72:75], 0
buffer_load_dwordx4 v15, s[36:39], s40 offen offset:1024 sc1 lds
RA 32,16,8192
RA 36,16,9216
v_mfma_f32_16x16x32_f16 v[176:179], a[16:19], v[72:75], 0
v_pk_fmac_f16 v88, v236, v25
v_pk_fmac_f16 v89, v237, v25
v_mfma_f32_16x16x32_f16 v[180:183], a[20:23], v[72:75], 0
buffer_load_dwordx4 v15, s[36:39], s40 offen offset:2048 sc1 lds
v_pk_fmac_f16 v90, v238, v25
v_pk_fmac_f16 v91, v239, v25
v_mfma_f32_16x16x32_f16 v[204:207], a[20:23], v[76:79], 0
RA 40,16,10240
RA 44,16,11264
v_mfma_f32_16x16x32_f16 v[200:203], a[16:19], v[76:79], 0
PM 92,240,26
PM 93,241,26
v_mfma_f32_16x16x32_f16 v[196:199], a[12:15], v[76:79], 0
buffer_load_dwordx4 v15, s[36:39], s40 offen offset:3072 sc1 lds
PM 94,242,26
PM 95,243,26
v_mfma_f32_16x16x32_f16 v[192:195], a[8:11], v[76:79], 0
s_add_u32 m0, m0, 4096
RV 64,40,32
RV 68,41,32
v_mfma_f32_16x16x32_f16 v[188:191], a[4:7], v[76:79], 0
buffer_load_dwordx4 v45, s[36:39], s40 offen sc1 lds
v_pk_fmac_f16 v92, v244, v27
v_pk_fmac_f16 v93, v245, v27
v_mfma_f32_16x16x32_f16 v[184:187], a[0:3], v[76:79], 0
v_pk_fmac_f16 v94, v246, v27
v_pk_fmac_f16 v95, v247, v27
v_mfma_f32_16x16x32_f16 v[208:211], a[0:3], v[80:83], 0
buffer_load_dwordx4 v45, s[36:39], s40 offen offset:1024 sc1 lds
RV 232,32,48
RV 236,33,48
v_mfma_f32_16x16x32_f16 v[212:215], a[4:7], v[80:83], 0
s_add_u32 s40, s40, 24704
PM 96,248,28
PM 97,249,28
v_mfma_f32_16x16x32_f16 v[216:219], a[8:11], v[80:83], 0
PM 98,250,28
PM 99,251,28
v_mfma_f32_16x16x32_f16 v[220:223], a[12:15], v[80:83], 0
RV 240,36,48
RV 244,37,48
v_mfma_f32_16x16x32_f16 v[224:227], a[16:19], v[80:83], 0
v_pk_fmac_f16 v96, v252, v29
v_pk_fmac_f16 v97, v253, v29
v_mfma_f32_16x16x32_f16 v[228:231], a[20:23], v[80:83], 0
PH 98,254,28
v_pk_fmac_f16 v99, v255, v29
s_waitcnt lgkmcnt(2)
M 160,24,88
RA 0,16,12288
RA 4,16,13312
M 164,28,88
PM 72,48,24
PM 73,49,24
M 168,32,88
PM 74,50,24
PM 75,51,24
M 172,36,88
RA 8,16,14336
RA 12,16,15360
M 176,40,88
v_pk_fmac_f16 v72, v52, v25
v_pk_fmac_f16 v73, v53, v25
M 180,44,88
v_pk_fmac_f16 v74, v54, v25
v_pk_fmac_f16 v75, v55, v25
M 204,44,92
RA 16,16,16384
RA 20,16,17408
M 200,40,92
PM 76,56,26
PM 77,57,26
M 196,36,92
PM 78,58,26
PM 79,59,26
M 192,32,92
RV 248,40,48
RV 252,41,48
M 188,28,92
v_pk_fmac_f16 v76, v60, v27
v_pk_fmac_f16 v77, v61, v27
M 184,24,92
v_pk_fmac_f16 v78, v62, v27
v_pk_fmac_f16 v79, v63, v27
M 208,24,96
RV 48,32,64
RV 52,33,64
M 212,28,96
PM 80,64,28
PM 81,65,28
M 216,32,96
PM 82,66,28
PM 83,67,28
M 220,36,96
RV 56,36,64
RV 60,37,64
M 224,40,96
v_pk_fmac_f16 v80, v68, v29
v_pk_fmac_f16 v81, v69, v29
M 228,44,96
PH 82,70,28
v_pk_fmac_f16 v83, v71, v29
s_waitcnt lgkmcnt(2)
M 160,0,72
RA 24,16,18432
RA 28,16,19456
M 164,4,72
PM 88,232,24
PM 89,233,24
M 168,8,72
PM 90,234,24
PM 91,235,24
M 172,12,72
RA 32,16,20480
RA 36,16,21504
M 176,16,72
v_pk_fmac_f16 v88, v236, v25
v_pk_fmac_f16 v89, v237, v25
M 180,20,72
v_pk_fmac_f16 v90, v238, v25
v_pk_fmac_f16 v91, v239, v25
M 204,20,76
RA 40,16,22528
RA 44,16,23552
M 200,16,76
PM 92,240,26
PM 93,241,26
M 196,12,76
PM 94,242,26
PM 95,243,26
M 192,8,76
RV 64,40,64
RV 68,41,64
M 188,4,76
PH 92,244,26
v_pk_fmac_f16 v93, v245, v27
v_cmp_nge_f32 vcc, 1.0, v12
M 184,0,76
v_pk_fmac_f16 v94, v246, v27
v_pk_fmac_f16 v95, v247, v27
v_and_b32 v13, 0x7ff87ff8, v13
M 208,0,80
RV 232,32,80
RV 236,33,80
s_lshl2_add_u32 s11, s10, 162304
M 212,4,80
PM 96,248,28
PM 97,249,28
v_addc_co_u32 v13, vcc, 0, v13, vcc
M 216,8,80
buffer_load_dwordx4 v[6:9], v15, s[36:39], 0 offen sc1
PM 98,250,28
PM 99,251,28
v_add_u32 v12, s11, v15
M 220,12,80
s_waitcnt vmcnt(1) lgkmcnt(2)
v_pk_fmac_f16 v96, v252, v29
PH 97,253,28
M 224,16,80
buffer_load_dwordx4 v[100:103], v47, s[36:39], 0 offen offset:1024 sc1
s_barrier
v_pk_fmac_f16 v98, v254, v29
PH 99,255,28
M 228,20,80
RV 240,36,80
RV 244,37,80
M 160,24,88
RA 0,16,24576
RA 4,16,25600
M 164,28,88
ds_write_b32 v12, v13
PM 72,48,24
PM 73,49,24
M 168,32,88
PM 74,50,24
PM 75,51,24
M 172,36,88
RA 8,16,26624
RA 12,16,27648
M 176,40,88
v_pk_fmac_f16 v72, v52, v25
v_pk_fmac_f16 v73, v53, v25
M 180,44,88
v_pk_fmac_f16 v74, v54, v25
v_pk_fmac_f16 v75, v55, v25
M 204,44,92
RA 16,16,28672
RA 20,16,29696
M 200,40,92
PM 76,56,26
PM 77,57,26
M 196,36,92
PM 78,58,26
PM 79,59,26
M 192,32,92
RV 248,40,80
RV 252,41,80
M 188,28,92
v_pk_fmac_f16 v76, v60, v27
v_pk_fmac_f16 v77, v61, v27
M 184,24,92
v_pk_fmac_f16 v78, v62, v27
v_pk_fmac_f16 v79, v63, v27
M 208,24,96
RV 48,32,96
RV 52,33,96
M 212,28,96
PM 80,64,28
PM 81,65,28
M 216,32,96
PM 82,66,28
PM 83,67,28
M 220,36,96
RV 56,36,96
RV 60,37,96
M 224,40,96
v_pk_fmac_f16 v80, v68, v29
v_pk_fmac_f16 v81, v69, v29
M 228,44,96
v_pk_fmac_f16 v82, v70, v29
v_pk_fmac_f16 v83, v71, v29
s_waitcnt lgkmcnt(2)
M 160,0,72
RA 24,16,30720
RA 28,16,31744
s_waitcnt vmcnt(0)
v_bitop3_b32 v6, v6, v8, s66 bitop3:0x96
M 164,4,72
PM 88,232,24
PM 89,233,24
v_bitop3_b32 v100, v100, v102, s66 bitop3:0x96
v_bitop3_b32 v7, v7, v9, s67 bitop3:0x96
M 168,8,72
PM 90,234,24
PM 91,235,24
v_bitop3_b32 v101, v101, v103, s67 bitop3:0x96
v_or_b32_e64 v6, v6, v100
M 172,12,72
RA 32,16,32768
RA 36,16,33792
v_or_b32 v5, v7, v101
v_cmp_ne_u32 vcc, 0, v6
M 176,16,72
v_pk_fmac_f16 v88, v236, v25
v_pk_fmac_f16 v89, v237, v25
v_cmp_lt_u32 s[30:31], 7, v5
s_or_b64 vcc, vcc, s[30:31]
M 180,20,72
v_pk_fmac_f16 v90, v238, v25
v_pk_fmac_f16 v91, v239, v25
s_cbranch_scc1 L_ag12_1_%=
L_ok12_1_%=:
M 204,20,76
s_add_u32 m0, s41, 49152
RA 40,16,34816
RA 44,16,35840
M 200,16,76
buffer_load_dwordx4 v15, s[36:39], s40 offen sc1 lds
PM 92,240,26
PM 93,241,26
M 196,12,76
PM 94,242,26
PM 95,243,26
M 192,8,76
buffer_load_dwordx4 v15, s[36:39], s40 offen offset:1024 sc1 lds
RV 64,40,96
RV 68,41,96
M 188,4,76
v_pk_fmac_f16 v92, v244, v27
v_pk_fmac_f16 v93, v245, v27
M 184,0,76
buffer_load_dwordx4 v15, s[36:39], s40 offen offset:2048 sc1 lds
v_pk_fmac_f16 v94, v246, v27
v_pk_fmac_f16 v95, v247, v27
M 208,0,80
RV 232,32,112
RV 236,33,112
M 212,4,80
PM 96,248,28
PM 97,249,28
M 216,8,80
buffer_load_dwordx4 v15, s[36:39], s40 offen offset:3072 sc1 lds
PM 98,250,28
PM 99,251,28
M 220,12,80
s_add_u32 m0, m0, 4096
RV 240,36,112
RV 244,37,112
M 224,16,80
buffer_load_dwordx4 v45, s[36:39], s40 offen sc1 lds
v_pk_fmac_f16 v96, v252, v29
v_pk_fmac_f16 v97, v253, v29
M 228,20,80
PH 98,254,28
v_pk_fmac_f16 v99, v255, v29
s_waitcnt lgkmcnt(2)
M 160,24,88
buffer_load_dwordx4 v45, s[36:39], s40 offen offset:1024 sc1 lds
RA 0,16,36864
RA 4,16,37888
M 164,28,88
s_add_u32 s40, s40, 24704
PM 72,48,24
PM 73,49,24
M 168,32,88
PM 74,50,24
PM 75,51,24
M 172,36,88
RA 8,16,38912
RA 12,16,39936
M 176,40,88
s_add_u32 m0, s41, 0
v_pk_fmac_f16 v72, v52, v25
PH 73,53,24
M 180,44,88
buffer_load_dwordx4 v15, s[36:39], s40 offen sc1 lds
v_pk_fmac_f16 v74, v54, v25
v_pk_fmac_f16 v75, v55, v25
M 204,44,92
RA 16,16,40960
RA 20,16,41984
M 200,40,92
PM 76,56,26
PM 77,57,26
M 196,36,92
PM 78,58,26
PM 79,59,26
M 192,32,92
buffer_load_dwordx4 v15, s[36:39], s40 offen offset:1024 sc1 lds
RV 248,40,112
RV 252,41,112
M 188,28,92
v_pk_fmac_f16 v76, v60, v27
v_pk_fmac_f16 v77, v61, v27
M 184,24,92
v_pk_fmac_f16 v78, v62, v27
v_pk_fmac_f16 v79, v63, v27
M 208,24,96
RV 48,32,128
RV 52,33,128
M 212,28,96
PM 80,64,28
PM 81,65,28
M 216,32,96
buffer_load_dwordx4 v15, s[36:39], s40 offen offset:2048 sc1 lds
PM 82,66,28
PM 83,67,28
M 220,36,96
RV 56,36,128
RV 60,37,128
M 224,40,96
v_pk_fmac_f16 v80, v68, v29
v_pk_fmac_f16 v81, v69, v29
M 228,44,96
PH 82,70,28
v_pk_fmac_f16 v83, v71, v29
s_waitcnt lgkmcnt(2)
M 160,0,72
buffer_load_dwordx4 v15, s[36:39], s40 offen offset:3072 sc1 lds
RA 24,16,43008
RA 28,16,44032
M 164,4,72
s_add_u32 m0, m0, 4096
PM 88,232,24
PM 89,233,24
M 168,8,72
PM 90,234,24
PM 91,235,24
M 172,12,72
RA 32,16,45056
RA 36,16,46080
M 176,16,72
v_pk_fmac_f16 v88, v236, v25
v_pk_fmac_f16 v89, v237, v25
M 180,20,72
buffer_load_dwordx4 v45, s[36:39], s40 offen sc1 lds
v_pk_fmac_f16 v90, v238, v25
v_pk_fmac_f16 v91, v239, v25
M 204,20,76
RA 40,16,47104
RA 44,16,48128
M 200,16,76
PM 92,240,26
PM 93,241,26
M 196,12,76
PM 94,242,26
PM 95,243,26
M 192,8,76
buffer_load_dwordx4 v45, s[36:39], s40 offen offset:1024 sc1 lds
RV 64,40,128
RV 68,41,128
M 188,4,76
s_addk_i32 s40, 24704
v_pk_fmac_f16 v92, v244, v27
PH 93,245,26
M 184,0,76
v_pk_fmac_f16 v94, v246, v27
v_pk_fmac_f16 v95, v247, v27
M 208,0,80
RV 232,32,144
RV 236,33,144
M 212,4,80
PM 96,248,28
PM 97,249,28
M 216,8,80
PM 98,250,28
PM 99,251,28
M 220,12,80
s_waitcnt vmcnt(6) lgkmcnt(2)
v_pk_fmac_f16 v96, v252, v29
PH 97,253,28
M 224,16,80
s_barrier
v_pk_fmac_f16 v98, v254, v29
PH 99,255,28
M 228,20,80
RV 240,36,144
RV 244,37,144
M 160,24,88
RA 0,20,0
RA 4,20,1024
M 164,28,88
s_branch L_in12_1_%=
L_ag12_1_%=:
s_call_b64 s[72:73], L_retry_%=
s_branch L_ok12_1_%=
.p2align 12
.p2align 6
s_nop 0
T_a121_%=:
s_setpc_b64 s[58:59]
L_body12_1_%=:
M 160,24,88
L_bodz12_1_%=:
RA 0,16,0
RA 4,16,1024
M 164,28,88
s_set_gpr_idx_on s43, gpr_idx(SRC1)
v_add_u32 v32, s44, v104
v_add_u32 v36, s44, v105
v_add_u32 v40, s44, v106
v_add_u32 v6, 0, v108
v_add_u32 v7, 0, v109
v_add_u32 v8, 0, v110
s_set_gpr_idx_off
M 168,32,88
RA 8,16,2048
v_pk_max_i16 v25, v6, s50
v_pk_min_i16 v25, v25, s52
v_mad_i32_i16 v33, v25, s53, v32
v_mad_i32_i16 v33, v25, s54, v33 op_sel:[1,0,0,0]
M 172,36,88
RA 12,16,3072
v_pk_max_i16 v27, v7, s50
v_pk_min_i16 v27, v27, s52
v_mad_i32_i16 v37, v27, s53, v36
v_mad_i32_i16 v37, v27, s54, v37 op_sel:[1,0,0,0]
M 176,40,88
RA 16,16,4096
v_pk_max_i16 v29, v8, s50
v_pk_min_i16 v29, v29, s52
v_mad_i32_i16 v41, v29, s53, v40
v_mad_i32_i16 v41, v29, s54, v41 op_sel:[1,0,0,0]
M 180,44,88
RV 48,32,0
RV 52,33,0
v_and_b32_e64 v6, s56, v6
v_pk_add_f16 v25, v6, v6 op_sel:[0,1] op_sel_hi:[1,0]
M 204,44,92
RV 56,36,0
RV 60,37,0
v_pk_add_f16 v24, v25, s51 neg_lo:[1,0]
v_and_b32_e64 v7, s56, v7
M 200,40,92
RV 64,40,0
RV 68,41,0
v_pk_add_f16 v27, v7, v7 op_sel:[0,1] op_sel_hi:[1,0]
v_pk_add_f16 v26, v27, s51 neg_lo:[1,0]
M 196,36,92
RA 20,16,5120
v_and_b32_e64 v8, s56, v8
v_pk_add_f16 v29, v8, v8 op_sel:[0,1] op_sel_hi:[1,0]
v_pk_add_f16 v28, v29, s51 neg_lo:[1,0]
M 192,32,92
RV 232,32,16
RV 236,33,16
RV 240,36,16
RV 244,37,16
M 188,28,92
s_add_u32 m0, s41, 49152
s_waitcnt lgkmcnt(5)
PM 72,48,24
PM 73,49,24
PM 74,50,24
M 184,24,92
RV 248,40,16
RV 252,41,16
PM 75,51,24
v_pk_fmac_f16 v72, v52, v25
PH 73,53,24
M 208,24,96
buffer_load_dwordx4 v15, s[36:39], s40 offen sc1 lds
PH 74,54,24
M 212,28,96
PH 75,55,24
PM 76,56,26
PM 77,57,26
M 216,32,96
RV 48,32,32
RV 52,33,32
PM 78,58,26
PM 79,59,26
M 220,36,96
v_pk_fmac_f16 v76, v60, v27
v_pk_fmac_f16 v77, v61, v27
v_pk_fmac_f16 v78, v62, v27
v_pk_fmac_f16 v79, v63, v27
PM 80,64,28
M 224,40,96
RV 56,36,32
RV 60,37,32
PM 81,65,28
PM 82,66,28
PM 83,67,28
M 228,44,96
v_pk_fmac_f16 v80, v68, v29
v_pk_fmac_f16 v81, v69, v29
PH 82,70,28
v_pk_fmac_f16 v83, v71, v29
M 160,0,72
RA 24,16,6144
RA 28,16,7168
M 164,4,72
s_waitcnt lgkmcnt(4)
PM 88,232,24
PM 89,233,24
M 168,8,72
buffer_load_dwordx4 v15, s[36:39], s40 offen offset:1024 sc1 lds
PM 90,234,24
PM 91,235,24
M 172,12,72
RA 32,16,8192
RA 36,16,9216
M 176,16,72
v_pk_fmac_f16 v88, v236, v25
v_pk_fmac_f16 v89, v237, v25
M 180,20,72
v_pk_fmac_f16 v90, v238, v25
v_pk_fmac_f16 v91, v239, v25
M 204,20,76
RA 40,16,10240
RA 44,16,11264
M 200,16,76
PM 92,240,26
PM 93,241,26
M 196,12,76
PM 94,242,26
PM 95,243,26
M 192,8,76
RV 64,40,32
RV 68,41,32
M 188,4,76
v_pk_fmac_f16 v92, v244, v27
v_pk_fmac_f16 v93, v245, v27
M 184,0,76
buffer_load_dwordx4 v15, s[36:39], s40 offen offset:2048 sc1 lds
v_pk_fmac_f16 v94, v246, v27
v_pk_fmac_f16 v95, v247, v27
M 208,0,80
RV 232,32,48
RV 236,33,48
M 212,4,80
PM 96,248,28
PM 97,249,28
M 216,8,80
PM 98,250,28
PM 99,251,28
M 220,12,80
RV 240,36,48
RV 244,37,48
M 224,16,80
v_pk_fmac_f16 v96, v252, v29
v_pk_fmac_f16 v97, v253, v29
M 228,20,80
PH 98,254,28
v_pk_fmac_f16 v99, v255, v29
s_waitcnt lgkmcnt(2)
M 160,24,88
RA 0,16,12288
RA 4,16,13312
M 164,28,88
PM 72,48,24
PM 73,49,24
M 168,32,88
buffer_load_dwordx4 v15, s[36:39], s40 offen offset:3072 sc1 lds
PM 74,50,24
PM 75,51,24
M 172,36,88
RA 8,16,14336
RA 12,16,15360
M 176,40,88
v_pk_fmac_f16 v72, v52, v25
v_pk_fmac_f16 v73, v53, v25
M 180,44,88
v_pk_fmac_f16 v74, v54, v25
v_pk_fmac_f16 v75, v55, v25
M 204,44,92
RA 16,16,16384
RA 20,16,17408
M 200,40,92
PM 76,56,26
PM 77,57,26
M 196,36,92
s_add_u32 m0, m0, 4096
PM 78,58,26
PM 79,59,26
M 192,32,92
RV 248,40,48
RV 252,41,48
M 188,28,92
v_pk_fmac_f16 v76, v60, v27
v_pk_fmac_f16 v77, v61, v27
M 184,24,92
buffer_load_dwordx4 v45, s[36:39], s40 offen sc1 lds
v_pk_fmac_f16 v78, v62, v27
v_pk_fmac_f16 v79, v63, v27
M 208,24,96
RV 48,32,64
RV 52,33,64
M 212,28,96
PM 80,64,28
PM 81,65,28
M 216,32,96
)ASM" R"ASM(
PM 82,66,28
PM 83,67,28
M 220,36,96
RV 56,36,64
RV 60,37,64
M 224,40,96
v_pk_fmac_f16 v80, v68, v29
v_pk_fmac_f16 v81, v69, v29
M 228,44,96
PH 82,70,28
v_pk_fmac_f16 v83, v71, v29
s_waitcnt lgkmcnt(2)
M 160,0,72
RA 24,16,18432
RA 28,16,19456
M 164,4,72
PM 88,232,24
PM 89,233,24
M 168,8,72
buffer_load_dwordx4 v45, s[36:39], s40 offen offset:1024 sc1 lds
PM 90,234,24
PM 91,235,24
M 172,12,72
RA 32,16,20480
RA 36,16,21504
M 176,16,72
v_pk_fmac_f16 v88, v236, v25
v_pk_fmac_f16 v89, v237, v25
M 180,20,72
v_pk_fmac_f16 v90, v238, v25
v_pk_fmac_f16 v91, v239, v25
M 204,20,76
RA 40,16,22528
RA 44,16,23552
M 200,16,76
PM 92,240,26
PM 93,241,26
M 196,12,76
PM 94,242,26
PM 95,243,26
M 192,8,76
RV 64,40,64
RV 68,41,64
M 188,4,76
v_pk_fmac_f16 v92, v244, v27
v_pk_fmac_f16 v93, v245, v27
M 184,0,76
s_addk_i32 s40, 24704
v_pk_fmac_f16 v94, v246, v27
PH 95,247,26
M 208,0,80
RV 232,32,80
RV 236,33,80
M 212,4,80
PM 96,248,28
PM 97,249,28
M 216,8,80
PM 98,250,28
PM 99,251,28
M 220,12,80
s_waitcnt vmcnt(6) lgkmcnt(2)
v_pk_fmac_f16 v96, v252, v29
PH 97,253,28
M 224,16,80
s_barrier
v_pk_fmac_f16 v98, v254, v29
PH 99,255,28
M 228,20,80
RV 240,36,80
RV 244,37,80
M 160,24,88
RA 0,16,24576
RA 4,16,25600
M 164,28,88
PM 72,48,24
PM 73,49,24
M 168,32,88
PM 74,50,24
PM 75,51,24
M 172,36,88
RA 8,16,26624
RA 12,16,27648
M 176,40,88
v_pk_fmac_f16 v72, v52, v25
v_pk_fmac_f16 v73, v53, v25
M 180,44,88
s_mov_b32 m0, s41
v_pk_fmac_f16 v74, v54, v25
PH 75,55,24
M 204,44,92
RA 16,16,28672
RA 20,16,29696
M 200,40,92
PM 76,56,26
PM 77,57,26
M 196,36,92
buffer_load_dwordx4 v15, s[36:39], s40 offen sc1 lds
PM 78,58,26
PM 79,59,26
M 192,32,92
RV 248,40,80
RV 252,41,80
M 188,28,92
v_pk_fmac_f16 v76, v60, v27
v_pk_fmac_f16 v77, v61, v27
M 184,24,92
v_pk_fmac_f16 v78, v62, v27
v_pk_fmac_f16 v79, v63, v27
M 208,24,96
RV 48,32,96
RV 52,33,96
M 212,28,96
PM 80,64,28
PM 81,65,28
M 216,32,96
PM 82,66,28
PM 83,67,28
M 220,36,96
RV 56,36,96
RV 60,37,96
M 224,40,96
v_pk_fmac_f16 v80, v68, v29
v_pk_fmac_f16 v81, v69, v29
M 228,44,96
PH 82,70,28
v_pk_fmac_f16 v83, v71, v29
s_waitcnt lgkmcnt(2)
M 160,0,72
RA 24,16,30720
RA 28,16,31744
M 164,4,72
PM 88,232,24
PM 89,233,24
M 168,8,72
buffer_load_dwordx4 v15, s[36:39], s40 offen offset:1024 sc1 lds
PM 90,234,24
PM 91,235,24
M 172,12,72
RA 32,16,32768
RA 36,16,33792
M 176,16,72
v_pk_fmac_f16 v88, v236, v25
v_pk_fmac_f16 v89, v237, v25
M 180,20,72
v_pk_fmac_f16 v90, v238, v25
v_pk_fmac_f16 v91, v239, v25
M 204,20,76
RA 40,16,34816
RA 44,16,35840
M 200,16,76
PM 92,240,26
PM 93,241,26
M 196,12,76
PM 94,242,26
PM 95,243,26
M 192,8,76
RV 64,40,96
RV 68,41,96
M 188,4,76
v_pk_fmac_f16 v92, v244, v27
v_pk_fmac_f16 v93, v245, v27
M 184,0,76
buffer_load_dwordx4 v15, s[36:39], s40 offen offset:2048 sc1 lds
v_pk_fmac_f16 v94, v246, v27
v_pk_fmac_f16 v95, v247, v27
M 208,0,80
RV 232,32,112
RV 236,33,112
M 212,4,80
PM 96,248,28
PM 97,249,28
M 216,8,80
PM 98,250,28
PM 99,251,28
M 220,12,80
RV 240,36,112
RV 244,37,112
M 224,16,80
v_pk_fmac_f16 v96, v252, v29
v_pk_fmac_f16 v97, v253, v29
M 228,20,80
PH 98,254,28
v_pk_fmac_f16 v99, v255, v29
s_waitcnt lgkmcnt(2)
M 160,24,88
RA 0,16,36864
RA 4,16,37888
M 164,28,88
PM 72,48,24
PM 73,49,24
M 168,32,88
buffer_load_dwordx4 v15, s[36:39], s40 offen offset:3072 sc1 lds
PM 74,50,24
PM 75,51,24
M 172,36,88
RA 8,16,38912
RA 12,16,39936
M 176,40,88
v_pk_fmac_f16 v72, v52, v25
v_pk_fmac_f16 v73, v53, v25
M 180,44,88
v_pk_fmac_f16 v74, v54, v25
v_pk_fmac_f16 v75, v55, v25
M 204,44,92
RA 16,16,40960
RA 20,16,41984
M 200,40,92
PM 76,56,26
PM 77,57,26
M 196,36,92
s_add_u32 m0, m0, 4096
PM 78,58,26
PM 79,59,26
M 192,32,92
RV 248,40,112
RV 252,41,112
M 188,28,92
v_pk_fmac_f16 v76, v60, v27
v_pk_fmac_f16 v77, v61, v27
M 184,24,92
buffer_load_dwordx4 v45, s[36:39], s40 offen sc1 lds
v_pk_fmac_f16 v78, v62, v27
v_pk_fmac_f16 v79, v63, v27
M 208,24,96
RV 48,32,128
RV 52,33,128
M 212,28,96
PM 80,64,28
PM 81,65,28
M 216,32,96
PM 82,66,28
PM 83,67,28
M 220,36,96
RV 56,36,128
RV 60,37,128
M 224,40,96
v_pk_fmac_f16 v80, v68, v29
v_pk_fmac_f16 v81, v69, v29
M 228,44,96
PH 82,70,28
v_pk_fmac_f16 v83, v71, v29
s_waitcnt lgkmcnt(2)
M 160,0,72
RA 24,16,43008
RA 28,16,44032
M 164,4,72
PM 88,232,24
PM 89,233,24
M 168,8,72
buffer_load_dwordx4 v45, s[36:39], s40 offen offset:1024 sc1 lds
PM 90,234,24
PM 91,235,24
M 172,12,72
RA 32,16,45056
RA 36,16,46080
M 176,16,72
v_pk_fmac_f16 v88, v236, v25
v_pk_fmac_f16 v89, v237, v25
M 180,20,72
v_pk_fmac_f16 v90, v238, v25
v_pk_fmac_f16 v91, v239, v25
M 204,20,76
RA 40,16,47104
RA 44,16,48128
M 200,16,76
PM 92,240,26
PM 93,241,26
M 196,12,76
PM 94,242,26
PM 95,243,26
M 192,8,76
RV 64,40,128
RV 68,41,128
M 188,4,76
v_pk_fmac_f16 v92, v244, v27
v_pk_fmac_f16 v93, v245, v27
M 184,0,76
s_addk_i32 s40, 24704
v_pk_fmac_f16 v94, v246, v27
PH 95,247,26
M 208,0,80
RV 232,32,144
RV 236,33,144
M 212,4,80
PM 96,248,28
PM 97,249,28
M 216,8,80
PM 98,250,28
PM 99,251,28
M 220,12,80
s_waitcnt vmcnt(6) lgkmcnt(2)
v_pk_fmac_f16 v96, v252, v29
PH 97,253,28
M 224,16,80
s_barrier
v_pk_fmac_f16 v98, v254, v29
PH 99,255,28
M 228,20,80
RV 240,36,144
RV 244,37,144
M 160,24,88
RA 0,20,0
RA 4,20,1024
M 164,28,88
s_branch L_in12_1_%=
.p2align 12
.p2align 6
s_nop 0
T_b130_%=:
s_setpc_b64 s[58:59]
L_in13_0_%=:
M 184,24,92
M 208,24,96
M 212,28,96
M 216,32,96
s_add_u32 m0, s41, 24576
M 220,36,96
buffer_load_dwordx4 v15, s[36:39], s40 offen sc1 lds
M 224,40,96
M 228,44,96
s_waitcnt lgkmcnt(0)
M 160,0,72
RV 88,33,144
M 164,4,72
RV 92,37,144
M 168,8,72
RV 96,41,144
M 172,12,72
RA 24,21,6144
M 176,16,72
RA 28,21,7168
M 180,20,72
RA 32,21,8192
M 204,20,76
RA 36,21,9216
M 200,16,76
RA 40,21,10240
M 196,12,76
RA 44,21,11264
M 192,8,76
M 188,4,76
buffer_load_dwordx4 v15, s[36:39], s40 offen offset:1024 sc1 lds
M 184,0,76
M 208,0,80
M 212,4,80
M 216,8,80
M 220,12,80
M 224,16,80
M 228,20,80
M 252,20,84
M 248,16,84
M 244,12,84
M 240,8,84
buffer_load_dwordx4 v15, s[36:39], s40 offen offset:2048 sc1 lds
M 236,4,84
M 232,0,84
s_waitcnt lgkmcnt(0)
M 160,24,88
RV 72,34,160
M 164,28,88
RV 76,38,160
M 168,32,88
RV 80,42,160
M 172,36,88
RA 0,22,12288
M 176,40,88
RA 4,22,13312
M 180,44,88
RA 8,22,14336
M 204,44,92
RA 12,22,15360
M 200,40,92
RA 16,22,16384
M 196,36,92
RA 20,22,17408
M 192,32,92
M 188,28,92
buffer_load_dwordx4 v15, s[36:39], s40 offen offset:3072 sc1 lds
M 184,24,92
M 208,24,96
M 212,28,96
M 216,32,96
s_addk_i32 m0, 4096
M 220,36,96
buffer_load_dwordx4 v45, s[36:39], s40 offen sc1 lds
M 224,40,96
M 228,44,96
s_waitcnt lgkmcnt(0)
M 160,0,72
RV 88,35,176
M 164,4,72
RV 92,39,176
M 168,8,72
RV 96,43,176
M 172,12,72
RA 24,23,18432
M 176,16,72
RA 28,23,19456
M 180,20,72
RA 32,23,20480
M 204,20,76
RA 36,23,21504
M 200,16,76
RA 40,23,22528
M 196,12,76
RA 44,23,23552
M 192,8,76
buffer_load_dwordx4 v45, s[36:39], s40 offen offset:1024 sc1 lds
M 188,4,76
M 184,0,76
s_addk_i32 s40, 24704
M 208,0,80
s_lshr_b32 s42, s42, 4
s_add_u32 s43, s43, 8
M 212,4,80
s_and_b32 s55, s42, 15
s_cmp_eq_u32 s55, 0
M 216,8,80
M 220,12,80
s_waitcnt vmcnt(6) lgkmcnt(0)
M 224,16,80
s_barrier
M 228,20,80
s_cbranch_scc0 L_disp13_0_%=
L_body13_0_%=:
M 160,24,88
L_bodz13_0_%=:
s_set_gpr_idx_on s43, gpr_idx(SRC1)
v_add_u32 v32, s44, v104
v_add_u32 v36, s44, v105
v_add_u32 v40, s44, v106
v_add_u32 v44, s44, v107
s_set_gpr_idx_off
M 164,28,88
RA 0,16,0
M 168,32,88
RA 4,16,1024
v_add_u32_e64 v33, s45, v32
M 172,36,88
RV 72,32,0
v_add_u32_e64 v37, s45, v36
M 176,40,88
RV 76,36,0
v_add_u32_e64 v41, s45, v40
M 180,44,88
RV 80,40,0
M 204,44,92
RV 84,44,0
M 200,40,92
RA 8,16,2048
M 196,36,92
RA 12,16,3072
M 192,32,92
RA 16,16,4096
M 188,28,92
RA 20,16,5120
M 184,24,92
M 208,24,96
M 212,28,96
M 216,32,96
s_add_u32 m0, s41, 49152
M 220,36,96
buffer_load_dwordx4 v15, s[36:39], s40 offen sc1 lds
M 224,40,96
M 228,44,96
s_waitcnt lgkmcnt(0)
M 160,0,72
RV 88,33,16
M 164,4,72
RV 92,37,16
v_add_u32 v34, s46, v32
M 168,8,72
RV 96,41,16
v_add_u32_e64 v38, s46, v36
M 172,12,72
RA 24,17,6144
v_add_u32_e64 v42, s46, v40
M 176,16,72
RA 28,17,7168
M 180,20,72
RA 32,17,8192
M 204,20,76
RA 36,17,9216
M 200,16,76
RA 40,17,10240
M 196,12,76
RA 44,17,11264
M 192,8,76
M 188,4,76
buffer_load_dwordx4 v15, s[36:39], s40 offen offset:1024 sc1 lds
M 184,0,76
M 208,0,80
M 212,4,80
M 216,8,80
M 220,12,80
M 224,16,80
M 228,20,80
M 252,20,84
M 248,16,84
M 244,12,84
M 240,8,84
buffer_load_dwordx4 v15, s[36:39], s40 offen offset:2048 sc1 lds
M 236,4,84
M 232,0,84
s_waitcnt lgkmcnt(0)
M 160,24,88
RV 72,34,32
M 164,28,88
RV 76,38,32
v_add_u32 v35, s47, v32
M 168,32,88
RV 80,42,32
v_add_u32_e64 v39, s47, v36
M 172,36,88
RA 0,18,12288
v_add_u32_e64 v43, s47, v40
M 176,40,88
RA 4,18,13312
M 180,44,88
RA 8,18,14336
M 204,44,92
RA 12,18,15360
M 200,40,92
RA 16,18,16384
M 196,36,92
RA 20,18,17408
M 192,32,92
M 188,28,92
buffer_load_dwordx4 v15, s[36:39], s40 offen offset:3072 sc1 lds
M 184,24,92
M 208,24,96
M 212,28,96
M 216,32,96
s_addk_i32 m0, 4096
M 220,36,96
buffer_load_dwordx4 v45, s[36:39], s40 offen sc1 lds
M 224,40,96
M 228,44,96
s_waitcnt lgkmcnt(0)
M 160,0,72
RV 88,35,48
M 164,4,72
RV 92,39,48
M 168,8,72
RV 96,43,48
M 172,12,72
RA 24,19,18432
M 176,16,72
RA 28,19,19456
M 180,20,72
RA 32,19,20480
M 204,20,76
RA 36,19,21504
M 200,16,76
RA 40,19,22528
M 196,12,76
RA 44,19,23552
M 192,8,76
M 188,4,76
buffer_load_dwordx4 v45, s[36:39], s40 offen offset:1024 sc1 lds
M 184,0,76
M 208,0,80
M 212,4,80
M 216,8,80
s_add_u32 s40, s40, 24704
M 220,12,80
s_waitcnt vmcnt(6) lgkmcnt(0)
M 224,16,80
s_barrier
M 228,20,80
M 160,24,88
RV 72,32,64
M 164,28,88
RV 76,36,64
M 168,32,88
RV 80,40,64
M 172,36,88
RV 84,44,64
M 176,40,88
RA 0,16,24576
M 180,44,88
RA 4,16,25600
M 204,44,92
RA 8,16,26624
M 200,40,92
RA 12,16,27648
M 196,36,92
RA 16,16,28672
M 192,32,92
RA 20,16,29696
M 188,28,92
M 184,24,92
M 208,24,96
M 212,28,96
M 216,32,96
s_mov_b32 m0, s41
M 220,36,96
buffer_load_dwordx4 v15, s[36:39], s40 offen sc1 lds
M 224,40,96
M 228,44,96
s_waitcnt lgkmcnt(0)
M 160,0,72
RV 88,33,80
M 164,4,72
RV 92,37,80
M 168,8,72
RV 96,41,80
M 172,12,72
RA 24,17,30720
M 176,16,72
RA 28,17,31744
M 180,20,72
RA 32,17,32768
M 204,20,76
RA 36,17,33792
M 200,16,76
RA 40,17,34816
M 196,12,76
RA 44,17,35840
M 192,8,76
M 188,4,76
buffer_load_dwordx4 v15, s[36:39], s40 offen offset:1024 sc1 lds
M 184,0,76
M 208,0,80
M 212,4,80
M 216,8,80
M 220,12,80
M 224,16,80
M 228,20,80
M 252,20,84
M 248,16,84
M 244,12,84
M 240,8,84
buffer_load_dwordx4 v15, s[36:39], s40 offen offset:2048 sc1 lds
M 236,4,84
M 232,0,84
s_waitcnt lgkmcnt(0)
M 160,24,88
RV 72,34,96
M 164,28,88
RV 76,38,96
M 168,32,88
RV 80,42,96
M 172,36,88
RA 0,18,36864
M 176,40,88
RA 4,18,37888
M 180,44,88
RA 8,18,38912
M 204,44,92
RA 12,18,39936
M 200,40,92
RA 16,18,40960
M 196,36,92
RA 20,18,41984
M 192,32,92
M 188,28,92
buffer_load_dwordx4 v15, s[36:39], s40 offen offset:3072 sc1 lds
M 184,24,92
M 208,24,96
M 212,28,96
M 216,32,96
s_add_u32 m0, m0, 4096
M 220,36,96
buffer_load_dwordx4 v45, s[36:39], s40 offen sc1 lds
M 224,40,96
M 228,44,96
s_waitcnt lgkmcnt(0)
M 160,0,72
RV 88,35,112
M 164,4,72
RV 92,39,112
M 168,8,72
RV 96,43,112
M 172,12,72
RA 24,19,43008
M 176,16,72
RA 28,19,44032
M 180,20,72
RA 32,19,45056
M 204,20,76
RA 36,19,46080
M 200,16,76
RA 40,19,47104
M 196,12,76
RA 44,19,48128
M 192,8,76
M 188,4,76
buffer_load_dwordx4 v45, s[36:39], s40 offen offset:1024 sc1 lds
M 184,0,76
M 208,0,80
M 212,4,80
M 216,8,80
s_add_u32 s40, s40, 24704
M 220,12,80
s_waitcnt vmcnt(6) lgkmcnt(0)
M 224,16,80
s_barrier
M 228,20,80
M 160,24,88
RV 72,32,128
M 164,28,88
RV 76,36,128
M 168,32,88
RV 80,40,128
M 172,36,88
RV 84,44,128
M 176,40,88
RA 0,20,0
M 180,44,88
RA 4,20,1024
M 204,44,92
RA 8,20,2048
M 200,40,92
RA 12,20,3072
M 196,36,92
RA 16,20,4096
M 192,32,92
RA 20,20,5120
M 188,28,92
s_branch L_in13_0_%=
.p2align 6
s_nop 0
T_md_%=:
s_setpc_b64 s[58:59]
L_modes_%=:
s_cmp_eq_u32 s24, 12
s_cbranch_scc1 L_mz_%=
s_cmp_eq_u32 s24, 13
s_cbranch_scc1 L_mall_%=
v_mov_b32 v110, 0
v_mov_b32 v118, 0
v_mov_b32 v126, 0
v_mov_b32 v134, 0
v_mov_b32 v142, 0
v_mov_b32 v150, 0
v_mov_b32 v158, 0
L_mz_%=:
v_mov_b32 v111, 0
v_mov_b32 v119, 0
v_mov_b32 v127, 0
v_mov_b32 v135, 0
v_mov_b32 v143, 0
v_mov_b32_e64 v151, 0
v_mov_b32 v159, 0
L_mall_%=:
s_mov_b32 s42, 1
v_mad_u32_u16 v88, v156, v156, 0 op_sel:[0,1,0,0] clamp
v_mad_u32_u16 v88, v157, v157, v88 op_sel:[0,1,0,0] clamp
v_mad_u32_u16 v88, v158, v158, v88 op_sel:[0,1,0,0] clamp
v_mad_u32_u16 v88, v159, v159, v88 op_sel:[0,1,0,0] clamp
v_or3_b32 v92, v156, v157, v158
v_cmp_ne_u32 s[30:31], 0, v88
v_or_b32 v92, v92, v159
v_cmp_ne_u32 vcc, 0, v92
s_cmp_lg_u64 s[30:31], 0
s_addc_u32 s42, s42, s42
s_cmp_lg_u64 vcc, 0
s_addc_u32 s42, s42, s42
v_mad_u32_u16 v88, v148, v148, 0 op_sel:[0,1,0,0] clamp
v_mad_u32_u16 v88, v149, v149, v88 op_sel:[0,1,0,0] clamp
v_mad_u32_u16 v88, v150, v150, v88 op_sel:[0,1,0,0] clamp
v_mad_u32_u16 v88, v151, v151, v88 op_sel:[0,1,0,0] clamp
v_or3_b32 v92, v148, v149, v150
v_cmp_ne_u32 s[30:31], 0, v88
v_or_b32_e64 v92, v92, v151
v_cmp_ne_u32 vcc, 0, v92
s_lshl_b32 s42, s42, 2
s_cmp_lg_u64 s[30:31], 0
s_addc_u32 s42, s42, s42
s_cmp_lg_u64 vcc, 0
s_addc_u32 s42, s42, s42
v_mad_u32_u16 v88, v140, v140, 0 op_sel:[0,1,0,0] clamp
v_mad_u32_u16 v88, v141, v141, v88 op_sel:[0,1,0,0] clamp
v_mad_u32_u16 v88, v142, v142, v88 op_sel:[0,1,0,0] clamp
v_mad_u32_u16 v88, v143, v143, v88 op_sel:[0,1,0,0] clamp
v_or3_b32 v92, v140, v141, v142
v_cmp_ne_u32 s[30:31], 0, v88
v_or_b32_e64 v92, v92, v143
v_cmp_ne_u32 vcc, 0, v92
s_lshl_b32 s42, s42, 2
s_cmp_lg_u64 s[30:31], 0
s_addc_u32 s42, s42, s42
s_cmp_lg_u64 vcc, 0
s_addc_u32 s42, s42, s42
v_mad_u32_u16 v88, v132, v132, 0 op_sel:[0,1,0,0] clamp
v_mad_u32_u16 v88, v133, v133, v88 op_sel:[0,1,0,0] clamp
v_mad_u32_u16 v88, v134, v134, v88 op_sel:[0,1,0,0] clamp
v_mad_u32_u16 v88, v135, v135, v88 op_sel:[0,1,0,0] clamp
v_or3_b32 v92, v132, v133, v134
v_cmp_ne_u32 s[30:31], 0, v88
v_or_b32_e64 v92, v92, v135
v_cmp_ne_u32 vcc, 0, v92
s_lshl_b32 s42, s42, 2
s_cmp_lg_u64 s[30:31], 0
s_addc_u32 s42, s42, s42
s_cmp_lg_u64 vcc, 0
s_addc_u32 s42, s42, s42
v_mad_u32_u16 v88, v124, v124, 0 op_sel:[0,1,0,0] clamp
v_mad_u32_u16 v88, v125, v125, v88 op_sel:[0,1,0,0] clamp
v_mad_u32_u16 v88, v126, v126, v88 op_sel:[0,1,0,0] clamp
v_mad_u32_u16 v88, v127, v127, v88 op_sel:[0,1,0,0] clamp
v_or3_b32 v92, v124, v125, v126
v_cmp_ne_u32 s[30:31], 0, v88
v_or_b32_e64 v92, v92, v127
v_cmp_ne_u32 vcc, 0, v92
s_lshl_b32 s42, s42, 2
s_cmp_lg_u64 s[30:31], 0
s_addc_u32 s42, s42, s42
s_cmp_lg_u64 vcc, 0
s_addc_u32 s42, s42, s42
v_mad_u32_u16 v88, v116, v116, 0 op_sel:[0,1,0,0] clamp
v_mad_u32_u16 v88, v117, v117, v88 op_sel:[0,1,0,0] clamp
v_mad_u32_u16 v88, v118, v118, v88 op_sel:[0,1,0,0] clamp
v_mad_u32_u16 v88, v119, v119, v88 op_sel:[0,1,0,0] clamp
v_or3_b32 v92, v116, v117, v118
v_cmp_ne_u32 s[30:31], 0, v88
v_or_b32_e64 v92, v92, v119
v_cmp_ne_u32 vcc, 0, v92
s_lshl_b32 s42, s42, 2
s_cmp_lg_u64 s[30:31], 0
s_addc_u32 s42, s42, s42
s_cmp_lg_u64 vcc, 0
s_addc_u32 s42, s42, s42
v_mad_u32_u16 v88, v108, v108, 0 op_sel:[0,1,0,0] clamp
v_mad_u32_u16 v88, v109, v109, v88 op_sel:[0,1,0,0] clamp
v_mad_u32_u16 v88, v110, v110, v88 op_sel:[0,1,0,0] clamp
v_mad_u32_u16 v88, v111, v111, v88 op_sel:[0,1,0,0] clamp
v_or3_b32 v92, v108, v109, v110
v_cmp_ne_u32 s[30:31], 0, v88
v_or_b32 v92, v92, v111
v_cmp_ne_u32 vcc, 0, v92
s_lshl_b32 s42, s42, 2
s_cmp_lg_u64 s[30:31], 0
s_addc_u32 s42, s42, s42
s_cmp_lg_u64 vcc, 0
s_addc_u32 s42, s42, s42
s_branch L_modesr_%=
.p2align 12
.p2align 6
s_nop 0
T_e81_%=:
s_setpc_b64 s[58:59]
L_ent8_1_%=:
s_set_gpr_idx_on s43, gpr_idx(SRC1)
v_add_u32 v32, s44, v104
v_add_u32 v36, s44, v105
v_add_u32 v6, 0, v108
v_add_u32 v7, 0, v109
s_set_gpr_idx_off
v_pk_max_i16 v25, v6, s50
v_pk_min_i16 v25, v25, s52
v_mad_i32_i16 v33, v25, s53, v32
v_mad_i32_i16 v33, v25, s54, v33 op_sel:[1,0,0,0]
v_pk_max_i16 v27, v7, s50
v_pk_min_i16 v27, v27, s52
v_mad_i32_i16 v37, v27, s53, v36
v_mad_i32_i16 v37, v27, s54, v37 op_sel:[1,0,0,0]
s_waitcnt lgkmcnt(0)
s_barrier
RV 48,32,0
v_and_b32_e64 v6, s56, v6
RV 52,33,0
v_pk_add_f16 v25, v6, v6 op_sel:[0,1] op_sel_hi:[1,0]
RV 56,36,0
v_pk_add_f16 v24, v25, s51 neg_lo:[1,0]
RV 60,37,0
v_and_b32_e64 v7, s56, v7
RA 0,16,0
v_pk_add_f16 v27, v7, v7 op_sel:[0,1] op_sel_hi:[1,0]
RA 4,16,1024
v_pk_add_f16 v26, v27, s51 neg_lo:[1,0]
RA 8,16,2048
RA 12,16,3072
RA 16,16,4096
RA 20,16,5120
s_waitcnt lgkmcnt(6)
RV 232,32,16
RV 236,33,16
PM 72,48,24
PM 73,49,24
PM 74,50,24
PM 75,51,24
v_pk_fmac_f16 v72, v52, v25
RV 240,36,16
RV 244,37,16
v_pk_fmac_f16 v73, v53, v25
v_pk_fmac_f16 v74, v54, v25
PH 75,55,24
PM 76,56,26
PM 77,57,26
PM 78,58,26
PM 79,59,26
v_pk_fmac_f16 v76, v60, v27
v_pk_fmac_f16 v77, v61, v27
PH 78,62,26
v_pk_fmac_f16 v79, v63, v27
s_waitcnt lgkmcnt(0)
s_nop 0
v_mfma_f32_16x16x32_f16 v[160:163], a[0:3], v[72:75], 0
s_add_u32 m0, s41, 24576
RA 24,16,6144
s_waitcnt lgkmcnt(2)
PM 88,232,24
v_mfma_f32_16x16x32_f16 v[164:167], a[4:7], v[72:75], 0
buffer_load_dwordx4 v15, s[36:39], s40 offen sc1 lds
RA 28,16,7168
PM 89,233,24
v_mfma_f32_16x16x32_f16 v[168:171], a[8:11], v[72:75], 0
RA 32,16,8192
PM 90,234,24
v_mfma_f32_16x16x32_f16 v[172:175], a[12:15], v[72:75], 0
buffer_load_dwordx4 v15, s[36:39], s40 offen offset:1024 sc1 lds
RA 36,16,9216
RA 40,16,10240
PM 91,235,24
v_mfma_f32_16x16x32_f16 v[176:179], a[16:19], v[72:75], 0
RA 44,16,11264
PH 88,236,24
v_mfma_f32_16x16x32_f16 v[180:183], a[20:23], v[72:75], 0
buffer_load_dwordx4 v15, s[36:39], s40 offen offset:2048 sc1 lds
PH 89,237,24
v_mfma_f32_16x16x32_f16 v[204:207], a[20:23], v[76:79], 0
RV 48,32,32
RV 52,33,32
PH 90,238,24
v_mfma_f32_16x16x32_f16 v[200:203], a[16:19], v[76:79], 0
RV 56,36,32
PH 91,239,24
v_mfma_f32_16x16x32_f16 v[196:199], a[12:15], v[76:79], 0
buffer_load_dwordx4 v15, s[36:39], s40 offen offset:3072 sc1 lds
RV 60,37,32
s_waitcnt lgkmcnt(5)
PM 92,240,26
v_mfma_f32_16x16x32_f16 v[192:195], a[8:11], v[76:79], 0
s_addk_i32 m0, 4096
PM 93,241,26
PM 94,242,26
v_mfma_f32_16x16x32_f16 v[188:191], a[4:7], v[76:79], 0
buffer_load_dwordx4 v45, s[36:39], s40 offen sc1 lds
PM 95,243,26
v_pk_fmac_f16 v92, v244, v27
v_pk_fmac_f16 v93, v245, v27
v_mfma_f32_16x16x32_f16 v[184:187], a[0:3], v[76:79], 0
v_pk_fmac_f16 v94, v246, v27
v_pk_fmac_f16 v95, v247, v27
M 160,24,88
buffer_load_dwordx4 v45, s[36:39], s40 offen offset:1024 sc1 lds
RA 0,16,12288
s_waitcnt lgkmcnt(2)
PM 72,48,24
M 164,28,88
s_addk_i32 s40, 24704
RA 4,16,13312
PM 73,49,24
M 168,32,88
RA 8,16,14336
RA 12,16,15360
M 172,36,88
PM 74,50,24
PM 75,51,24
M 176,40,88
RA 16,16,16384
RA 20,16,17408
PH 72,52,24
M 180,44,88
RV 232,32,48
PH 73,53,24
M 204,44,92
M 200,40,92
RV 236,33,48
PH 74,54,24
M 196,36,92
RV 240,36,48
v_pk_fmac_f16 v75, v55, v25
M 192,32,92
RV 244,37,48
s_waitcnt lgkmcnt(4)
PM 76,56,26
PM 77,57,26
M 188,28,92
PM 78,58,26
PM 79,59,26
PH 76,60,26
M 184,24,92
v_pk_fmac_f16 v77, v61, v27
v_pk_fmac_f16 v78, v62, v27
v_pk_fmac_f16 v79, v63, v27
M 160,0,72
RA 24,16,18432
s_waitcnt lgkmcnt(3)
PM 88,232,24
M 164,4,72
RA 28,16,19456
PM 89,233,24
M 168,8,72
RA 32,16,20480
PM 90,234,24
PM 91,235,24
M 172,12,72
RA 36,16,21504
RA 40,16,22528
PH 88,236,24
M 176,16,72
RA 44,16,23552
v_pk_fmac_f16 v89, v237, v25
v_cmp_nge_f32 vcc, 1.0, v12
M 180,20,72
PH 90,238,24
v_and_b32 v13, 0x7ff87ff8, v13
M 204,20,76
buffer_load_dwordx4 v[6:9], v15, s[36:39], 0 offen sc1
RV 48,32,64
RV 52,33,64
PH 91,239,24
s_lshl2_add_u32 s11, s10, 162304
M 200,16,76
RV 56,36,64
s_waitcnt lgkmcnt(6)
PM 92,240,26
v_addc_co_u32 v13, vcc, 0, v13, vcc
M 196,12,76
buffer_load_dwordx4 v[100:103], v47, s[36:39], 0 offen offset:1024 sc1
RV 60,37,64
PM 93,241,26
v_add_u32 v12, s11, v15
M 192,8,76
s_waitcnt vmcnt(2) lgkmcnt(4)
PM 94,242,26
M 188,4,76
s_barrier
PM 95,243,26
v_pk_fmac_f16 v92, v244, v27
M 184,0,76
v_pk_fmac_f16 v93, v245, v27
v_pk_fmac_f16 v94, v246, v27
v_pk_fmac_f16 v95, v247, v27
M 160,24,88
RA 0,16,24576
s_waitcnt lgkmcnt(2)
PM 72,48,24
M 164,28,88
ds_write_b32 v12, v13
RA 4,16,25600
PM 73,49,24
M 168,32,88
RA 8,16,26624
PM 74,50,24
M 172,36,88
RA 12,16,27648
RA 16,16,28672
PM 75,51,24
M 176,40,88
RA 20,16,29696
PH 72,52,24
M 180,44,88
PH 73,53,24
M 204,44,92
RV 232,32,80
RV 236,33,80
v_pk_fmac_f16 v74, v54, v25
s_waitcnt vmcnt(0)
v_bitop3_b32 v6, v6, v8, s66 bitop3:0x96
M 200,40,92
RV 240,36,80
PH 75,55,24
v_bitop3_b32 v100, v100, v102, s66 bitop3:0x96
v_bitop3_b32 v7, v7, v9, s67 bitop3:0x96
M 196,36,92
RV 244,37,80
s_waitcnt lgkmcnt(5)
PM 76,56,26
v_bitop3_b32 v101, v101, v103, s67 bitop3:0x96
v_or_b32 v6, v6, v100
M 192,32,92
PM 77,57,26
PM 78,58,26
v_or_b32 v5, v7, v101
v_cmp_ne_u32 vcc, 0, v6
M 188,28,92
PM 79,59,26
v_pk_fmac_f16 v76, v60, v27
v_pk_fmac_f16 v77, v61, v27
v_cmp_lt_u32 s[30:31], 7, v5
s_or_b64 vcc, vcc, s[30:31]
M 184,24,92
v_pk_fmac_f16 v78, v62, v27
PH 79,63,26
s_cbranch_scc1 L_ag8_1_%=
L_ok8_1_%=:
M 160,0,72
s_add_u32 m0, s41, 49152
RA 24,16,30720
s_waitcnt lgkmcnt(2)
PM 88,232,24
M 164,4,72
buffer_load_dwordx4 v15, s[36:39], s40 offen sc1 lds
RA 28,16,31744
PM 89,233,24
M 168,8,72
RA 32,16,32768
PM 90,234,24
M 172,12,72
buffer_load_dwordx4 v15, s[36:39], s40 offen offset:1024 sc1 lds
RA 36,16,33792
RA 40,16,34816
PM 91,235,24
M 176,16,72
RA 44,16,35840
PH 88,236,24
M 180,20,72
buffer_load_dwordx4 v15, s[36:39], s40 offen offset:2048 sc1 lds
PH 89,237,24
M 204,20,76
RV 48,32,96
RV 52,33,96
PH 90,238,24
M 200,16,76
RV 56,36,96
PH 91,239,24
M 196,12,76
buffer_load_dwordx4 v15, s[36:39], s40 offen offset:3072 sc1 lds
RV 60,37,96
s_waitcnt lgkmcnt(5)
PM 92,240,26
M 192,8,76
s_addk_i32 m0, 4096
PM 93,241,26
PM 94,242,26
M 188,4,76
buffer_load_dwordx4 v45, s[36:39], s40 offen sc1 lds
PM 95,243,26
v_pk_fmac_f16 v92, v244, v27
v_pk_fmac_f16 v93, v245, v27
M 184,0,76
v_pk_fmac_f16 v94, v246, v27
v_pk_fmac_f16 v95, v247, v27
M 160,24,88
buffer_load_dwordx4 v45, s[36:39], s40 offen offset:1024 sc1 lds
RA 0,16,36864
s_waitcnt lgkmcnt(2)
PM 72,48,24
M 164,28,88
s_addk_i32 s40, 24704
RA 4,16,37888
PM 73,49,24
M 168,32,88
RA 8,16,38912
RA 12,16,39936
M 172,36,88
PM 74,50,24
PM 75,51,24
M 176,40,88
s_add_u32 m0, s41, 0
RA 16,16,40960
RA 20,16,41984
v_pk_fmac_f16 v72, v52, v25
M 180,44,88
buffer_load_dwordx4 v15, s[36:39], s40 offen sc1 lds
RV 232,32,112
PH 73,53,24
M 204,44,92
M 200,40,92
buffer_load_dwordx4 v15, s[36:39], s40 offen offset:1024 sc1 lds
RV 236,33,112
PH 74,54,24
M 196,36,92
RV 240,36,112
v_pk_fmac_f16 v75, v55, v25
M 192,32,92
RV 244,37,112
s_waitcnt lgkmcnt(4)
PM 76,56,26
PM 77,57,26
M 188,28,92
buffer_load_dwordx4 v15, s[36:39], s40 offen offset:2048 sc1 lds
PM 78,58,26
PM 79,59,26
PH 76,60,26
M 184,24,92
v_pk_fmac_f16 v77, v61, v27
v_pk_fmac_f16 v78, v62, v27
PH 79,63,26
M 160,0,72
buffer_load_dwordx4 v15, s[36:39], s40 offen offset:3072 sc1 lds
RA 24,16,43008
s_waitcnt lgkmcnt(3)
PM 88,232,24
M 164,4,72
s_addk_i32 m0, 4096
RA 28,16,44032
PM 89,233,24
M 168,8,72
RA 32,16,45056
PM 90,234,24
PM 91,235,24
M 172,12,72
buffer_load_dwordx4 v45, s[36:39], s40 offen sc1 lds
RA 36,16,46080
RA 40,16,47104
PH 88,236,24
M 176,16,72
RA 44,16,48128
PH 89,237,24
M 180,20,72
buffer_load_dwordx4 v45, s[36:39], s40 offen offset:1024 sc1 lds
PH 90,238,24
M 204,20,76
s_add_u32 s40, s40, 24704
RV 48,32,128
RV 52,33,128
v_pk_fmac_f16 v91, v239, v25
M 200,16,76
RV 56,36,128
s_waitcnt lgkmcnt(6)
PM 92,240,26
M 196,12,76
RV 60,37,128
PM 93,241,26
M 192,8,76
s_waitcnt vmcnt(6) lgkmcnt(4)
PM 94,242,26
M 188,4,76
s_barrier
PM 95,243,26
PH 92,244,26
M 184,0,76
v_pk_fmac_f16 v93, v245, v27
v_pk_fmac_f16 v94, v246, v27
v_pk_fmac_f16 v95, v247, v27
M 160,24,88
RA 0,20,0
s_waitcnt lgkmcnt(2)
PM 72,48,24
M 164,28,88
RA 4,20,1024
PM 73,49,24
M 168,32,88
RA 8,20,2048
PM 74,50,24
M 172,36,88
RA 12,20,3072
RA 16,20,4096
PM 75,51,24
M 176,40,88
s_add_u32 m0, s41, 24576
RA 20,20,5120
PH 72,52,24
M 180,44,88
buffer_load_dwordx4 v15, s[36:39], s40 offen sc1 lds
PH 73,53,24
M 204,44,92
RV 232,32,144
RV 236,33,144
PH 74,54,24
M 200,40,92
RV 240,36,144
v_pk_fmac_f16 v75, v55, v25
M 196,36,92
RV 244,37,144
s_waitcnt lgkmcnt(5)
PM 76,56,26
M 192,32,92
s_branch L_in8_1_%=
L_ag8_1_%=:
s_call_b64 s[72:73], L_retry_%=
s_branch L_ok8_1_%=
.p2align 6
s_nop 0
T_b81_%=:
s_setpc_b64 s[58:59]
L_in8_1_%=:
PM 77,57,26
PM 78,58,26
M 188,28,92
PM 79,59,26
v_pk_fmac_f16 v76, v60, v27
v_pk_fmac_f16 v77, v61, v27
M 184,24,92
PH 78,62,26
v_pk_fmac_f16 v79, v63, v27
M 160,0,72
RA 24,20,6144
buffer_load_dwordx4 v15, s[36:39], s40 offen offset:1024 sc1 lds
s_waitcnt lgkmcnt(2)
PM 88,232,24
M 164,4,72
RA 28,20,7168
PM 89,233,24
M 168,8,72
RA 32,20,8192
PM 90,234,24
M 172,12,72
RA 36,20,9216
RA 40,20,10240
PM 91,235,24
M 176,16,72
RA 44,20,11264
PH 88,236,24
M 180,20,72
buffer_load_dwordx4 v15, s[36:39], s40 offen offset:2048 sc1 lds
PH 89,237,24
M 204,20,76
RV 48,32,160
RV 52,33,160
PH 90,238,24
M 200,16,76
RV 56,36,160
v_pk_fmac_f16 v91, v239, v25
M 196,12,76
RV 60,37,160
s_waitcnt lgkmcnt(5)
PM 92,240,26
M 192,8,76
PM 93,241,26
PM 94,242,26
M 188,4,76
PM 95,243,26
v_pk_fmac_f16 v92, v244, v27
v_pk_fmac_f16 v93, v245, v27
M 184,0,76
PH 94,246,26
v_pk_fmac_f16 v95, v247, v27
M 160,24,88
RA 0,20,12288
buffer_load_dwordx4 v15, s[36:39], s40 offen offset:3072 sc1 lds
s_waitcnt lgkmcnt(2)
PM 72,48,24
M 164,28,88
RA 4,20,13312
PM 73,49,24
M 168,32,88
RA 8,20,14336
RA 12,20,15360
M 172,36,88
PM 74,50,24
PM 75,51,24
M 176,40,88
RA 16,20,16384
RA 20,20,17408
PH 72,52,24
M 180,44,88
s_add_u32 m0, m0, 4096
RV 232,32,176
PH 73,53,24
M 204,44,92
buffer_load_dwordx4 v45, s[36:39], s40 offen sc1 lds
M 200,40,92
RV 236,33,176
PH 74,54,24
M 196,36,92
RV 240,36,176
v_pk_fmac_f16 v75, v55, v25
M 192,32,92
RV 244,37,176
s_waitcnt lgkmcnt(4)
PM 76,56,26
PM 77,57,26
M 188,28,92
PM 78,58,26
PM 79,59,26
PH 76,60,26
M 184,24,92
v_pk_fmac_f16 v77, v61, v27
v_pk_fmac_f16 v78, v62, v27
v_pk_fmac_f16 v79, v63, v27
M 160,0,72
RA 24,20,18432
buffer_load_dwordx4 v45, s[36:39], s40 offen offset:1024 sc1 lds
s_waitcnt lgkmcnt(3)
PM 88,232,24
M 164,4,72
RA 28,20,19456
RA 32,20,20480
PM 89,233,24
M 168,8,72
RA 36,20,21504
PM 90,234,24
M 172,12,72
s_add_u32 s40, s40, 24704
RA 40,20,22528
RA 44,20,23552
M 176,16,72
PM 91,235,24
PH 88,236,24
M 180,20,72
v_pk_fmac_f16 v89, v237, v25
s_lshr_b32 s42, s42, 4
M 204,20,76
s_add_u32 s43, s43, 8
s_and_b32 s55, s42, 15
s_cmp_eq_u32 s55, 1
M 200,16,76
v_pk_fmac_f16 v90, v238, v25
v_pk_fmac_f16 v91, v239, v25
s_waitcnt lgkmcnt(2)
PM 92,240,26
M 196,12,76
PM 93,241,26
PM 94,242,26
PM 95,243,26
M 192,8,76
s_waitcnt vmcnt(6) lgkmcnt(0)
v_pk_fmac_f16 v92, v244, v27
PH 93,245,26
M 188,4,76
s_barrier
v_pk_fmac_f16 v94, v246, v27
PH 95,247,26
M 184,0,76
s_cbranch_scc1 L_body8_1_%=
s_branch L_disp8_1_%=
.p2align 12
.p2align 6
s_nop 0
T_b120_%=:
s_setpc_b64 s[58:59]
L_in12_0_%=:
M 188,28,92
M 184,24,92
M 208,24,96
M 212,28,96
M 216,32,96
s_add_u32 m0, s41, 24576
M 220,36,96
buffer_load_dwordx4 v15, s[36:39], s40 offen sc1 lds
M 224,40,96
M 228,44,96
s_waitcnt lgkmcnt(0)
M 160,0,72
RV 88,32,144
M 164,4,72
RV 92,36,144
M 168,8,72
RV 96,40,144
M 172,12,72
RA 24,20,6144
M 176,16,72
RA 28,20,7168
M 180,20,72
RA 32,20,8192
M 204,20,76
RA 36,20,9216
M 200,16,76
RA 40,20,10240
M 196,12,76
RA 44,20,11264
M 192,8,76
M 188,4,76
buffer_load_dwordx4 v15, s[36:39], s40 offen offset:1024 sc1 lds
M 184,0,76
M 208,0,80
M 212,4,80
M 216,8,80
M 220,12,80
buffer_load_dwordx4 v15, s[36:39], s40 offen offset:2048 sc1 lds
M 224,16,80
M 228,20,80
s_waitcnt lgkmcnt(0)
M 160,24,88
RV 72,32,160
M 164,28,88
RV 76,36,160
M 168,32,88
RV 80,40,160
M 172,36,88
RA 0,20,12288
M 176,40,88
RA 4,20,13312
M 180,44,88
RA 8,20,14336
M 204,44,92
RA 12,20,15360
M 200,40,92
RA 16,20,16384
M 196,36,92
RA 20,20,17408
M 192,32,92
M 188,28,92
buffer_load_dwordx4 v15, s[36:39], s40 offen offset:3072 sc1 lds
M 184,24,92
M 208,24,96
M 212,28,96
M 216,32,96
s_addk_i32 m0, 4096
M 220,36,96
buffer_load_dwordx4 v45, s[36:39], s40 offen sc1 lds
M 224,40,96
M 228,44,96
s_waitcnt lgkmcnt(0)
M 160,0,72
RV 88,32,176
M 164,4,72
RV 92,36,176
M 168,8,72
RV 96,40,176
M 172,12,72
RA 24,20,18432
M 176,16,72
RA 28,20,19456
M 180,20,72
RA 32,20,20480
M 204,20,76
RA 36,20,21504
M 200,16,76
RA 40,20,22528
M 196,12,76
RA 44,20,23552
M 192,8,76
buffer_load_dwordx4 v45, s[36:39], s40 offen offset:1024 sc1 lds
M 188,4,76
M 184,0,76
s_add_u32 s40, s40, 24704
M 208,0,80
s_lshr_b32 s42, s42, 4
s_add_u32 s43, s43, 8
M 212,4,80
s_and_b32 s55, s42, 15
s_cmp_eq_u32 s55, 0
M 216,8,80
M 220,12,80
s_waitcnt vmcnt(6) lgkmcnt(0)
M 224,16,80
s_barrier
M 228,20,80
s_cbranch_scc0 L_disp12_0_%=
L_body12_0_%=:
M 160,24,88
L_bodz12_0_%=:
s_set_gpr_idx_on s43, gpr_idx(SRC1)
v_add_u32 v32, s44, v104
v_add_u32 v36, s44, v105
v_add_u32 v40, s44, v106
s_set_gpr_idx_off
M 164,28,88
RA 0,16,0
M 168,32,88
RA 4,16,1024
M 172,36,88
RV 72,32,0
M 176,40,88
RV 76,36,0
M 180,44,88
RV 80,40,0
M 204,44,92
RA 8,16,2048
M 200,40,92
RA 12,16,3072
M 196,36,92
RA 16,16,4096
M 192,32,92
RA 20,16,5120
M 188,28,92
M 184,24,92
M 208,24,96
M 212,28,96
M 216,32,96
s_add_u32 m0, s41, 49152
M 220,36,96
buffer_load_dwordx4 v15, s[36:39], s40 offen sc1 lds
M 224,40,96
M 228,44,96
s_waitcnt lgkmcnt(0)
M 160,0,72
RV 88,32,16
M 164,4,72
RV 92,36,16
M 168,8,72
RV 96,40,16
M 172,12,72
RA 24,16,6144
M 176,16,72
RA 28,16,7168
M 180,20,72
RA 32,16,8192
M 204,20,76
RA 36,16,9216
M 200,16,76
RA 40,16,10240
M 196,12,76
RA 44,16,11264
M 192,8,76
M 188,4,76
buffer_load_dwordx4 v15, s[36:39], s40 offen offset:1024 sc1 lds
M 184,0,76
M 208,0,80
M 212,4,80
M 216,8,80
M 220,12,80
buffer_load_dwordx4 v15, s[36:39], s40 offen offset:2048 sc1 lds
M 224,16,80
M 228,20,80
s_waitcnt lgkmcnt(0)
M 160,24,88
RV 72,32,32
M 164,28,88
RV 76,36,32
M 168,32,88
RV 80,40,32
M 172,36,88
RA 0,16,12288
M 176,40,88
RA 4,16,13312
M 180,44,88
RA 8,16,14336
M 204,44,92
RA 12,16,15360
M 200,40,92
RA 16,16,16384
M 196,36,92
RA 20,16,17408
M 192,32,92
M 188,28,92
buffer_load_dwordx4 v15, s[36:39], s40 offen offset:3072 sc1 lds
M 184,24,92
M 208,24,96
M 212,28,96
M 216,32,96
s_addk_i32 m0, 4096
M 220,36,96
buffer_load_dwordx4 v45, s[36:39], s40 offen sc1 lds
M 224,40,96
M 228,44,96
s_waitcnt lgkmcnt(0)
M 160,0,72
RV 88,32,48
M 164,4,72
RV 92,36,48
M 168,8,72
RV 96,40,48
M 172,12,72
RA 24,16,18432
M 176,16,72
RA 28,16,19456
M 180,20,72
RA 32,16,20480
M 204,20,76
RA 36,16,21504
M 200,16,76
RA 40,16,22528
M 196,12,76
RA 44,16,23552
M 192,8,76
M 188,4,76
buffer_load_dwordx4 v45, s[36:39], s40 offen offset:1024 sc1 lds
M 184,0,76
M 208,0,80
M 212,4,80
M 216,8,80
s_add_u32 s40, s40, 24704
M 220,12,80
s_waitcnt vmcnt(6) lgkmcnt(0)
M 224,16,80
s_barrier
M 228,20,80
M 160,24,88
RV 72,32,64
M 164,28,88
RV 76,36,64
M 168,32,88
RV 80,40,64
M 172,36,88
RA 0,16,24576
M 176,40,88
RA 4,16,25600
M 180,44,88
RA 8,16,26624
M 204,44,92
RA 12,16,27648
M 200,40,92
RA 16,16,28672
M 196,36,92
RA 20,16,29696
M 192,32,92
M 188,28,92
M 184,24,92
M 208,24,96
M 212,28,96
M 216,32,96
s_mov_b32 m0, s41
M 220,36,96
buffer_load_dwordx4 v15, s[36:39], s40 offen sc1 lds
M 224,40,96
M 228,44,96
s_waitcnt lgkmcnt(0)
M 160,0,72
RV 88,32,80
M 164,4,72
RV 92,36,80
M 168,8,72
RV 96,40,80
M 172,12,72
RA 24,16,30720
M 176,16,72
RA 28,16,31744
M 180,20,72
RA 32,16,32768
M 204,20,76
RA 36,16,33792
M 200,16,76
RA 40,16,34816
M 196,12,76
RA 44,16,35840
M 192,8,76
M 188,4,76
buffer_load_dwordx4 v15, s[36:39], s40 offen offset:1024 sc1 lds
M 184,0,76
M 208,0,80
M 212,4,80
M 216,8,80
M 220,12,80
buffer_load_dwordx4 v15, s[36:39], s40 offen offset:2048 sc1 lds
M 224,16,80
M 228,20,80
s_waitcnt lgkmcnt(0)
M 160,24,88
RV 72,32,96
M 164,28,88
RV 76,36,96
M 168,32,88
RV 80,40,96
M 172,36,88
RA 0,16,36864
M 176,40,88
RA 4,16,37888
M 180,44,88
RA 8,16,38912
M 204,44,92
RA 12,16,39936
M 200,40,92
RA 16,16,40960
M 196,36,92
RA 20,16,41984
M 192,32,92
M 188,28,92
buffer_load_dwordx4 v15, s[36:39], s40 offen offset:3072 sc1 lds
M 184,24,92
M 208,24,96
M 212,28,96
M 216,32,96
s_add_u32 m0, m0, 4096
M 220,36,96
buffer_load_dwordx4 v45, s[36:39], s40 offen sc1 lds
M 224,40,96
M 228,44,96
s_waitcnt lgkmcnt(0)
M 160,0,72
RV 88,32,112
M 164,4,72
RV 92,36,112
M 168,8,72
RV 96,40,112
M 172,12,72
RA 24,16,43008
M 176,16,72
RA 28,16,44032
M 180,20,72
RA 32,16,45056
M 204,20,76
RA 36,16,46080
M 200,16,76
RA 40,16,47104
M 196,12,76
RA 44,16,48128
M 192,8,76
M 188,4,76
buffer_load_dwordx4 v45, s[36:39], s40 offen offset:1024 sc1 lds
M 184,0,76
M 208,0,80
M 212,4,80
M 216,8,80
s_add_u32 s40, s40, 24704
M 220,12,80
s_waitcnt vmcnt(6) lgkmcnt(0)
M 224,16,80
s_barrier
M 228,20,80
M 160,24,88
RV 72,32,128
M 164,28,88
RV 76,36,128
M 168,32,88
RV 80,40,128
M 172,36,88
RA 0,20,0
M 176,40,88
RA 4,20,1024
M 180,44,88
RA 8,20,2048
M 204,44,92
RA 12,20,3072
M 200,40,92
RA 16,20,4096
M 196,36,92
RA 20,20,5120
M 192,32,92
s_branch L_in12_0_%=
.p2align 6
s_nop 0
T_t80_%=:
s_setpc_b64 s[58:59]
L_sel8_0_%=:
s_cmp_eq_u32 s55, 0
s_cbranch_scc1 L_bodz8_0_%=
s_cmp_eq_u32 s55, 1
s_cbranch_scc1 L_bodz8_1_%=
s_cmp_eq_u32 s55, 4
s_cbranch_scc1 L_tail8_0_%=
s_cmp_eq_u32 s55, 5
s_cbranch_scc1 L_tail8_1_%=
s_cmp_lt_u32 s55, 4
s_cbranch_scc1 L_bodz8_2_%=
s_branch L_tail8_2_%=
L_disp8_0_%=:
M 160,24,88
s_cmp_lg_u32 s55, 4
s_cbranch_scc1 L_sel8_0_%=
L_tail8_0_%=:
RA 0,16,0
v_add_u32 v32, s44, v152
M 164,28,88
RA 4,16,1024
v_add_u32_e64 v36, s44, v153
M 168,32,88
RA 8,16,2048
s_mul_i32 s11, s23, 192
M 172,36,88
RA 12,16,3072
s_mul_i32 s16, s21, 43008
M 176,40,88
RA 16,16,4096
s_add_u32 s16, s16, s11
M 180,44,88
RA 20,16,5120
s_add_u32 s17, s16, 43008
M 204,44,92
RV 104,32,0
s_add_u32 s18, s16, 86016
M 200,40,92
RA 68,16,11264
v_and_b32 v47, 0xfff, v46
M 196,36,92
RA 64,16,10240
v_add_u32 v12, 162304, v15
M 192,32,92
RA 60,16,9216
s_waitcnt vmcnt(0)
M 188,28,92
buffer_store_dword v31, v30, s[36:39], 0 offen sc1
M 184,24,92
RA 56,16,8192
s_waitcnt lgkmcnt(4)
M 160,0,104
RA 52,16,7168
M 164,4,104
RA 48,16,6144
RV 108,32,16
M 168,8,104
RA 72,16,12288
M 172,12,104
RA 76,16,13312
M 176,16,104
RA 80,16,14336
M 180,20,104
RA 84,16,15360
s_waitcnt lgkmcnt(4)
M 180,68,108
RA 88,16,16384
M 176,64,108
RA 92,16,17408
RV 112,32,32
M 172,60,108
RV 116,36,0
M 168,56,108
RV 120,36,16
M 164,52,108
RV 124,36,32
M 160,48,108
RV 84,12,0
s_waitcnt lgkmcnt(4)
M 160,72,112
M 164,76,112
M 168,80,112
M 172,84,112
M 176,88,112
M 180,92,112
s_waitcnt lgkmcnt(0)
M 184,0,116
v_or3_b32 v84, v84, v85, v86
v_cvt_pk_bf16_f32 v88, v160, v161
M 188,4,116
v_cvt_pk_bf16_f32 v89, v162, v163
v_cvt_pk_bf16_f32 v90, v164, v165
M 192,8,116
v_cvt_pk_bf16_f32 v91, v166, v167
v_cvt_pk_bf16_f32 v92, v168, v169
M 196,12,116
buffer_store_dwordx4 v[88:91], v47, s[68:71], s16 offen offset:0 sc0 sc1 nt
M 200,16,116
v_cvt_pk_bf16_f32 v93, v170, v171
v_cvt_pk_bf16_f32 v94, v172, v173
M 204,20,116
v_cvt_pk_bf16_f32 v95, v174, v175
v_cvt_pk_bf16_f32 v96, v176, v177
M 204,68,120
v_cvt_pk_bf16_f32 v97, v178, v179
v_cvt_pk_bf16_f32 v98, v180, v181
M 200,64,120
v_cvt_pk_bf16_f32 v99, v182, v183
v_or3_b32 v84, v84, v87, v5
M 196,60,120
buffer_store_dwordx4 v[92:95], v47, s[68:71], s16 offen offset:64 sc0 sc1 nt
M 192,56,120
v_and_b32 v85, 0x40004001, v84
v_and_b32 v86, 0x70007000, v84
M 188,52,120
v_and_b32 v87, 2, v84
v_cmp_ne_u32 vcc, 0, v85
M 184,48,120
v_cmp_eq_u32 s[30:31], 0, v86
v_cmp_eq_u32 s[58:59], 0, v87
M 184,72,124
s_cmp_eq_u64 s[30:31], -1
s_cselect_b64 vcc, -1, vcc
M 188,76,124
buffer_store_dwordx4 v[96:99], v47, s[68:71], s16 offen offset:128 sc0 sc1 nt
M 192,80,124
s_cmp_eq_u64 s[58:59], -1
s_cselect_b64 vcc, -1, vcc
M 196,84,124
M 200,88,124
M 204,92,124
v_cvt_pk_bf16_f32 v88, v184, v185
v_cvt_pk_bf16_f32 v89, v186, v187
v_cvt_pk_bf16_f32 v90, v188, v189
v_cvt_pk_bf16_f32 v91, v190, v191
v_cvt_pk_bf16_f32 v92, v192, v193
v_cvt_pk_bf16_f32 v93, v194, v195
ds_write_b128 v4, v[88:91] offset:0
v_cvt_pk_bf16_f32 v94, v196, v197
v_cvt_pk_bf16_f32 v95, v198, v199
v_cvt_pk_bf16_f32 v96, v200, v201
v_cvt_pk_bf16_f32 v97, v202, v203
v_cvt_pk_bf16_f32 v98, v204, v205
v_cvt_pk_bf16_f32 v99, v206, v207
ds_write_b128 v4, v[92:95] offset:64
ds_write_b128 v4, v[96:99] offset:128
RV 144,1,0
RV 148,2,0
RV 100,3,0
s_waitcnt lgkmcnt(0)
buffer_store_dwordx4 v[144:147], v15, s[68:71], s17 offen offset:0 sc0 sc1 nt
buffer_store_dwordx4 v[148:151], v15, s[68:71], s17 offen offset:1024 sc0 sc1 nt
buffer_store_dwordx4 v[100:103], v15, s[68:71], s17 offen offset:2048 sc0 sc1 nt
s_cbranch_vccnz L_done_%=
s_endpgm
.p2align 12
.p2align 6
s_nop 0
T_e82_%=:
s_setpc_b64 s[58:59]
L_ent8_2_%=:
s_set_gpr_idx_on s43, gpr_idx(SRC1)
v_add_u32 v32, s44, v104
v_add_u32 v36, s44, v105
v_add_u32 v6, 0, v108
v_add_u32 v7, 0, v109
s_set_gpr_idx_off
v_pk_max_i16 v25, v6, s50
v_pk_min_i16 v25, v25, s52
v_mad_i32_i16 v33, v25, s54, v32 op_sel:[1,0,0,0]
v_mad_i32_i16 v34, v25, s53, v32
v_mad_i32_i16 v35, v25, s53, v33
v_pk_max_i16 v27, v7, s50
v_pk_min_i16 v27, v27, s52
v_mad_i32_i16 v37, v27, s54, v36 op_sel:[1,0,0,0]
v_mad_i32_i16 v38, v27, s53, v36
v_mad_i32_i16 v39, v27, s53, v37
s_waitcnt lgkmcnt(0)
s_barrier
RV 48,32,0
v_and_b32_e64 v6, s56, v6
RV 52,33,0
v_pk_add_f16 v25, v6, s51 op_sel:[1,0] neg_lo:[1,0]
RV 56,34,0
v_pk_add_f16 v6, v6, s51 op_sel_hi:[0,1] neg_lo:[1,0]
RV 60,35,0
PM 24,25,6
RV 208,36,0
v_pk_mul_f16 v25, v25, v6 op_sel:[0,1]
RV 212,37,0
v_and_b32 v7, s56, v7
RV 216,38,0
v_pk_add_f16 v27, v7, s51 op_sel:[1,0] neg_lo:[1,0]
RV 220,39,0
v_pk_add_f16 v7, v7, s51 op_sel_hi:[0,1] neg_lo:[1,0]
RA 0,16,0
PM 26,27,7
RA 4,16,1024
v_pk_mul_f16 v27, v27, v7 op_sel:[0,1]
)ASM" R"ASM(
RA 8,16,2048
RA 12,16,3072
RA 16,16,4096
RA 20,16,5120
s_waitcnt lgkmcnt(6)
RV 224,32,16
RV 228,33,16
PM 72,48,24
PM 73,49,24
PM 74,50,24
PM 75,51,24
PH 72,52,24
PH 73,53,24
RV 232,34,16
RV 236,35,16
PH 74,54,24
PH 75,55,24
PL 72,56,25
PL 73,57,25
PL 74,58,25
PL 75,59,25
RV 240,36,16
RV 244,37,16
PH 72,60,25
PH 73,61,25
PH 74,62,25
PH 75,63,25
PM 76,208,26
PM 77,209,26
RV 248,38,16
RV 252,39,16
PM 78,210,26
PM 79,211,26
PH 76,212,26
PH 77,213,26
PH 78,214,26
PH 79,215,26
PL 76,216,27
PL 77,217,27
PL 78,218,27
PL 79,219,27
PH 76,220,27
PH 77,221,27
PH 78,222,27
PH 79,223,27
s_waitcnt lgkmcnt(0)
s_nop 0
v_mfma_f32_16x16x32_f16 v[160:163], a[0:3], v[72:75], 0
s_add_u32 m0, s41, 24576
RA 24,16,6144
s_waitcnt lgkmcnt(4)
PM 88,224,24
PM 89,225,24
v_mfma_f32_16x16x32_f16 v[164:167], a[4:7], v[72:75], 0
buffer_load_dwordx4 v15, s[36:39], s40 offen sc1 lds
RA 28,16,7168
RA 32,16,8192
PM 90,226,24
PM 91,227,24
v_mfma_f32_16x16x32_f16 v[168:171], a[8:11], v[72:75], 0
RA 36,16,9216
RA 40,16,10240
PH 88,228,24
PH 89,229,24
v_mfma_f32_16x16x32_f16 v[172:175], a[12:15], v[72:75], 0
buffer_load_dwordx4 v15, s[36:39], s40 offen offset:1024 sc1 lds
RA 44,16,11264
RV 48,32,32
PH 90,230,24
PH 91,231,24
v_mfma_f32_16x16x32_f16 v[176:179], a[16:19], v[72:75], 0
RV 52,33,32
PL 88,232,25
PL 89,233,25
PL 90,234,25
v_mfma_f32_16x16x32_f16 v[180:183], a[20:23], v[72:75], 0
buffer_load_dwordx4 v15, s[36:39], s40 offen offset:2048 sc1 lds
RV 56,34,32
PL 91,235,25
PH 88,236,25
v_mfma_f32_16x16x32_f16 v[204:207], a[20:23], v[76:79], 0
RV 60,35,32
RV 208,36,32
PH 89,237,25
PH 90,238,25
PH 91,239,25
v_mfma_f32_16x16x32_f16 v[200:203], a[16:19], v[76:79], 0
RV 212,37,32
RV 216,38,32
s_waitcnt lgkmcnt(8)
PM 92,240,26
PM 93,241,26
v_mfma_f32_16x16x32_f16 v[196:199], a[12:15], v[76:79], 0
buffer_load_dwordx4 v15, s[36:39], s40 offen offset:3072 sc1 lds
RV 220,39,32
PM 94,242,26
PM 95,243,26
PH 92,244,26
v_mfma_f32_16x16x32_f16 v[192:195], a[8:11], v[76:79], 0
s_add_u32 m0, m0, 4096
PH 93,245,26
PH 94,246,26
PH 95,247,26
PL 92,248,27
v_mfma_f32_16x16x32_f16 v[188:191], a[4:7], v[76:79], 0
buffer_load_dwordx4 v45, s[36:39], s40 offen sc1 lds
PL 93,249,27
PL 94,250,27
PL 95,251,27
PH 92,252,27
v_mfma_f32_16x16x32_f16 v[184:187], a[0:3], v[76:79], 0
PH 93,253,27
PH 94,254,27
PH 95,255,27
M 160,24,88
buffer_load_dwordx4 v45, s[36:39], s40 offen offset:1024 sc1 lds
RA 0,16,12288
s_waitcnt lgkmcnt(2)
PM 72,48,24
PM 73,49,24
M 164,28,88
s_addk_i32 s40, 24704
RA 4,16,13312
RA 8,16,14336
PM 74,50,24
PM 75,51,24
M 168,32,88
RA 12,16,15360
RA 16,16,16384
PH 72,52,24
PH 73,53,24
M 172,36,88
RA 20,16,17408
PH 74,54,24
PH 75,55,24
PL 72,56,25
M 176,40,88
RV 224,32,48
RV 228,33,48
PL 73,57,25
PL 74,58,25
M 180,44,88
RV 232,34,48
PL 75,59,25
PH 72,60,25
M 204,44,92
RV 236,35,48
RV 240,36,48
PH 73,61,25
PH 74,62,25
M 200,40,92
RV 244,37,48
RV 248,38,48
PH 75,63,25
s_waitcnt lgkmcnt(8)
PM 76,208,26
M 196,36,92
RV 252,39,48
PM 77,209,26
PM 78,210,26
PM 79,211,26
M 192,32,92
PH 76,212,26
PH 77,213,26
PH 78,214,26
PH 79,215,26
M 188,28,92
PL 76,216,27
PL 77,217,27
PL 78,218,27
PL 79,219,27
M 184,24,92
PH 76,220,27
PH 77,221,27
PH 78,222,27
PH 79,223,27
M 160,0,72
RA 24,16,18432
s_waitcnt lgkmcnt(2)
PM 88,224,24
PM 89,225,24
M 164,4,72
RA 28,16,19456
RA 32,16,20480
PM 90,226,24
PM 91,227,24
M 168,8,72
RA 36,16,21504
RA 40,16,22528
PH 88,228,24
PH 89,229,24
M 172,12,72
RA 44,16,23552
RV 48,32,64
PH 90,230,24
PH 91,231,24
PL 88,232,25
M 176,16,72
RV 52,33,64
PL 89,233,25
PL 90,234,25
PL 91,235,25
v_cmp_nge_f32 vcc, 1.0, v12
M 180,20,72
RV 56,34,64
PH 88,236,25
PH 89,237,25
v_and_b32 v13, 0x7ff87ff8, v13
M 204,20,76
buffer_load_dwordx4 v[6:9], v15, s[36:39], 0 offen sc1
RV 60,35,64
RV 208,36,64
PH 90,238,25
PH 91,239,25
s_lshl2_add_u32 s11, s10, 162304
M 200,16,76
RV 212,37,64
RV 216,38,64
s_waitcnt lgkmcnt(8)
PM 92,240,26
PM 93,241,26
v_addc_co_u32 v13, vcc, 0, v13, vcc
M 196,12,76
buffer_load_dwordx4 v[100:103], v47, s[36:39], 0 offen offset:1024 sc1
RV 220,39,64
PM 94,242,26
PM 95,243,26
PH 92,244,26
v_add_u32_e64 v12, s11, v15
M 192,8,76
s_waitcnt vmcnt(2) lgkmcnt(6)
PH 93,245,26
PH 94,246,26
PH 95,247,26
M 188,4,76
s_barrier
PL 92,248,27
PL 93,249,27
PL 94,250,27
M 184,0,76
PL 95,251,27
PH 92,252,27
PH 93,253,27
PH 94,254,27
PH 95,255,27
M 160,24,88
RA 0,16,24576
s_waitcnt lgkmcnt(2)
PM 72,48,24
PM 73,49,24
M 164,28,88
ds_write_b32 v12, v13
RA 4,16,25600
RA 8,16,26624
PM 74,50,24
PM 75,51,24
M 168,32,88
RA 12,16,27648
RA 16,16,28672
PH 72,52,24
PH 73,53,24
M 172,36,88
RA 20,16,29696
RV 224,32,80
PH 74,54,24
PH 75,55,24
M 176,40,88
s_branch L_xe82_%=
.p2align 6
s_nop 0
T_e80_%=:
s_setpc_b64 s[58:59]
L_ent8_0_%=:
s_set_gpr_idx_on s43, gpr_idx(SRC1)
v_add_u32 v32, s44, v104
v_add_u32 v36, s44, v105
s_set_gpr_idx_off
s_waitcnt lgkmcnt(0)
s_barrier
RA 0,16,0
RA 4,16,1024
RV 72,32,0
RV 76,36,0
RA 8,16,2048
RA 12,16,3072
RA 16,16,4096
RA 20,16,5120
s_waitcnt lgkmcnt(0)
s_nop 0
s_waitcnt lgkmcnt(0)
v_mfma_f32_16x16x32_f16 v[160:163], a[0:3], v[72:75], 0
s_add_u32 m0, s41, 24576
RV 88,32,16
RV 92,36,16
v_mfma_f32_16x16x32_f16 v[164:167], a[4:7], v[72:75], 0
buffer_load_dwordx4 v15, s[36:39], s40 offen sc1 lds
RA 24,16,6144
RA 28,16,7168
v_mfma_f32_16x16x32_f16 v[168:171], a[8:11], v[72:75], 0
RA 32,16,8192
RA 36,16,9216
v_mfma_f32_16x16x32_f16 v[172:175], a[12:15], v[72:75], 0
buffer_load_dwordx4 v15, s[36:39], s40 offen offset:1024 sc1 lds
RA 40,16,10240
RA 44,16,11264
v_mfma_f32_16x16x32_f16 v[176:179], a[16:19], v[72:75], 0
v_mfma_f32_16x16x32_f16 v[180:183], a[20:23], v[72:75], 0
buffer_load_dwordx4 v15, s[36:39], s40 offen offset:2048 sc1 lds
v_mfma_f32_16x16x32_f16 v[204:207], a[20:23], v[76:79], 0
v_mfma_f32_16x16x32_f16 v[200:203], a[16:19], v[76:79], 0
v_mfma_f32_16x16x32_f16 v[196:199], a[12:15], v[76:79], 0
buffer_load_dwordx4 v15, s[36:39], s40 offen offset:3072 sc1 lds
v_mfma_f32_16x16x32_f16 v[192:195], a[8:11], v[76:79], 0
s_add_u32 m0, m0, 4096
v_mfma_f32_16x16x32_f16 v[188:191], a[4:7], v[76:79], 0
buffer_load_dwordx4 v45, s[36:39], s40 offen sc1 lds
v_mfma_f32_16x16x32_f16 v[184:187], a[0:3], v[76:79], 0
s_waitcnt lgkmcnt(0)
M 160,24,88
buffer_load_dwordx4 v45, s[36:39], s40 offen offset:1024 sc1 lds
RV 72,32,32
RV 76,36,32
M 164,28,88
s_add_u32 s40, s40, 24704
RA 0,16,12288
RA 4,16,13312
M 168,32,88
RA 8,16,14336
RA 12,16,15360
M 172,36,88
RA 16,16,16384
RA 20,16,17408
M 176,40,88
M 180,44,88
M 204,44,92
M 200,40,92
M 196,36,92
M 192,32,92
M 188,28,92
M 184,24,92
s_waitcnt lgkmcnt(0)
M 160,0,72
RV 88,32,48
RV 92,36,48
M 164,4,72
RA 24,16,18432
RA 28,16,19456
M 168,8,72
RA 32,16,20480
RA 36,16,21504
M 172,12,72
RA 40,16,22528
RA 44,16,23552
M 176,16,72
v_cmp_nge_f32 vcc, 1.0, v12
M 180,20,72
v_and_b32 v13, 0x7ff87ff8, v13
M 204,20,76
buffer_load_dwordx4 v[6:9], v15, s[36:39], 0 offen sc1
s_lshl2_add_u32 s11, s10, 162304
M 200,16,76
v_addc_co_u32 v13, vcc, 0, v13, vcc
M 196,12,76
buffer_load_dwordx4 v[100:103], v47, s[36:39], 0 offen offset:1024 sc1
v_add_u32 v12, s11, v15
M 192,8,76
s_waitcnt vmcnt(2) lgkmcnt(0)
M 188,4,76
s_barrier
M 184,0,76
M 160,24,88
RV 72,32,64
RV 76,36,64
M 164,28,88
ds_write_b32 v12, v13
RA 0,16,24576
RA 4,16,25600
M 168,32,88
RA 8,16,26624
RA 12,16,27648
M 172,36,88
RA 16,16,28672
RA 20,16,29696
M 176,40,88
M 180,44,88
M 204,44,92
s_waitcnt vmcnt(0)
v_bitop3_b32 v6, v6, v8, s66 bitop3:0x96
M 200,40,92
v_bitop3_b32 v100, v100, v102, s66 bitop3:0x96
v_bitop3_b32 v7, v7, v9, s67 bitop3:0x96
M 196,36,92
v_bitop3_b32 v101, v101, v103, s67 bitop3:0x96
v_or_b32 v6, v6, v100
M 192,32,92
v_or_b32_e64 v5, v7, v101
v_cmp_ne_u32 vcc, 0, v6
M 188,28,92
v_cmp_lt_u32 s[30:31], 7, v5
s_or_b64 vcc, vcc, s[30:31]
M 184,24,92
s_cbranch_scc1 L_ag8_0_%=
L_ok8_0_%=:
s_waitcnt lgkmcnt(0)
M 160,0,72
s_add_u32 m0, s41, 49152
RV 88,32,80
RV 92,36,80
M 164,4,72
buffer_load_dwordx4 v15, s[36:39], s40 offen sc1 lds
RA 24,16,30720
RA 28,16,31744
M 168,8,72
RA 32,16,32768
RA 36,16,33792
M 172,12,72
buffer_load_dwordx4 v15, s[36:39], s40 offen offset:1024 sc1 lds
RA 40,16,34816
RA 44,16,35840
M 176,16,72
M 180,20,72
buffer_load_dwordx4 v15, s[36:39], s40 offen offset:2048 sc1 lds
M 204,20,76
M 200,16,76
M 196,12,76
buffer_load_dwordx4 v15, s[36:39], s40 offen offset:3072 sc1 lds
M 192,8,76
s_addk_i32 m0, 4096
M 188,4,76
buffer_load_dwordx4 v45, s[36:39], s40 offen sc1 lds
M 184,0,76
s_waitcnt lgkmcnt(0)
M 160,24,88
buffer_load_dwordx4 v45, s[36:39], s40 offen offset:1024 sc1 lds
RV 72,32,96
RV 76,36,96
M 164,28,88
s_add_u32 s40, s40, 24704
RA 0,16,36864
RA 4,16,37888
M 168,32,88
RA 8,16,38912
RA 12,16,39936
M 172,36,88
RA 16,16,40960
RA 20,16,41984
M 176,40,88
s_add_u32 m0, s41, 0
M 180,44,88
buffer_load_dwordx4 v15, s[36:39], s40 offen sc1 lds
M 204,44,92
M 200,40,92
buffer_load_dwordx4 v15, s[36:39], s40 offen offset:1024 sc1 lds
M 196,36,92
M 192,32,92
M 188,28,92
buffer_load_dwordx4 v15, s[36:39], s40 offen offset:2048 sc1 lds
M 184,24,92
s_waitcnt lgkmcnt(0)
M 160,0,72
buffer_load_dwordx4 v15, s[36:39], s40 offen offset:3072 sc1 lds
RV 88,32,112
RV 92,36,112
M 164,4,72
s_add_u32 m0, m0, 4096
RA 24,16,43008
RA 28,16,44032
M 168,8,72
RA 32,16,45056
RA 36,16,46080
M 172,12,72
buffer_load_dwordx4 v45, s[36:39], s40 offen sc1 lds
RA 40,16,47104
RA 44,16,48128
M 176,16,72
M 180,20,72
buffer_load_dwordx4 v45, s[36:39], s40 offen offset:1024 sc1 lds
M 204,20,76
s_add_u32 s40, s40, 24704
M 200,16,76
M 196,12,76
M 192,8,76
s_waitcnt vmcnt(6) lgkmcnt(0)
M 188,4,76
s_barrier
M 184,0,76
M 160,24,88
RV 72,32,128
RV 76,36,128
M 164,28,88
RA 0,20,0
RA 4,20,1024
M 168,32,88
RA 8,20,2048
RA 12,20,3072
M 172,36,88
RA 16,20,4096
RA 20,20,5120
M 176,40,88
s_branch L_in8_0_%=
L_ag8_0_%=:
s_call_b64 s[72:73], L_retry_%=
s_branch L_ok8_0_%=
.p2align 12
.p2align 6
s_nop 0
T_e82x_%=:
s_setpc_b64 s[58:59]
L_xe82_%=:
RV 228,33,80
PL 72,56,25
PL 73,57,25
PL 74,58,25
M 180,44,88
RV 232,34,80
PL 75,59,25
PH 72,60,25
M 204,44,92
RV 236,35,80
RV 240,36,80
PH 73,61,25
PH 74,62,25
PH 75,63,25
s_waitcnt vmcnt(0)
v_bitop3_b32 v6, v6, v8, s66 bitop3:0x96
M 200,40,92
RV 244,37,80
RV 248,38,80
s_waitcnt lgkmcnt(8)
PM 76,208,26
PM 77,209,26
v_bitop3_b32 v100, v100, v102, s66 bitop3:0x96
v_bitop3_b32 v7, v7, v9, s67 bitop3:0x96
M 196,36,92
RV 252,39,80
PM 78,210,26
PM 79,211,26
PH 76,212,26
v_bitop3_b32 v101, v101, v103, s67 bitop3:0x96
v_or_b32_e64 v6, v6, v100
M 192,32,92
PH 77,213,26
PH 78,214,26
PH 79,215,26
PL 76,216,27
v_or_b32_e64 v5, v7, v101
v_cmp_ne_u32 vcc, 0, v6
M 188,28,92
PL 77,217,27
PL 78,218,27
PL 79,219,27
PH 76,220,27
v_cmp_lt_u32 s[30:31], 7, v5
s_or_b64 vcc, vcc, s[30:31]
M 184,24,92
PH 77,221,27
PH 78,222,27
PH 79,223,27
s_cbranch_scc1 L_ag8_2_%=
L_ok8_2_%=:
M 160,0,72
s_add_u32 m0, s41, 49152
RA 24,16,30720
s_waitcnt lgkmcnt(2)
PM 88,224,24
PM 89,225,24
M 164,4,72
buffer_load_dwordx4 v15, s[36:39], s40 offen sc1 lds
RA 28,16,31744
RA 32,16,32768
PM 90,226,24
PM 91,227,24
M 168,8,72
RA 36,16,33792
RA 40,16,34816
PH 88,228,24
PH 89,229,24
M 172,12,72
buffer_load_dwordx4 v15, s[36:39], s40 offen offset:1024 sc1 lds
RA 44,16,35840
RV 48,32,96
PH 90,230,24
PH 91,231,24
M 176,16,72
RV 52,33,96
PL 88,232,25
PL 89,233,25
PL 90,234,25
M 180,20,72
buffer_load_dwordx4 v15, s[36:39], s40 offen offset:2048 sc1 lds
RV 56,34,96
PL 91,235,25
PH 88,236,25
M 204,20,76
RV 60,35,96
RV 208,36,96
PH 89,237,25
PH 90,238,25
PH 91,239,25
M 200,16,76
RV 212,37,96
RV 216,38,96
s_waitcnt lgkmcnt(8)
PM 92,240,26
PM 93,241,26
M 196,12,76
buffer_load_dwordx4 v15, s[36:39], s40 offen offset:3072 sc1 lds
RV 220,39,96
PM 94,242,26
PM 95,243,26
PH 92,244,26
M 192,8,76
s_addk_i32 m0, 4096
PH 93,245,26
PH 94,246,26
PH 95,247,26
PL 92,248,27
M 188,4,76
buffer_load_dwordx4 v45, s[36:39], s40 offen sc1 lds
PL 93,249,27
PL 94,250,27
PL 95,251,27
PH 92,252,27
M 184,0,76
PH 93,253,27
PH 94,254,27
PH 95,255,27
M 160,24,88
buffer_load_dwordx4 v45, s[36:39], s40 offen offset:1024 sc1 lds
RA 0,16,36864
s_waitcnt lgkmcnt(2)
PM 72,48,24
PM 73,49,24
M 164,28,88
s_addk_i32 s40, 24704
RA 4,16,37888
RA 8,16,38912
PM 74,50,24
PM 75,51,24
M 168,32,88
RA 12,16,39936
RA 16,16,40960
PH 72,52,24
PH 73,53,24
M 172,36,88
RA 20,16,41984
PH 74,54,24
PH 75,55,24
PL 72,56,25
M 176,40,88
s_add_u32 m0, s41, 0
RV 224,32,112
RV 228,33,112
PL 73,57,25
PL 74,58,25
M 180,44,88
buffer_load_dwordx4 v15, s[36:39], s40 offen sc1 lds
RV 232,34,112
PL 75,59,25
PH 72,60,25
M 204,44,92
RV 236,35,112
RV 240,36,112
PH 73,61,25
PH 74,62,25
M 200,40,92
buffer_load_dwordx4 v15, s[36:39], s40 offen offset:1024 sc1 lds
RV 244,37,112
RV 248,38,112
PH 75,63,25
s_waitcnt lgkmcnt(8)
PM 76,208,26
M 196,36,92
RV 252,39,112
PM 77,209,26
PM 78,210,26
PM 79,211,26
M 192,32,92
PH 76,212,26
PH 77,213,26
PH 78,214,26
PH 79,215,26
M 188,28,92
buffer_load_dwordx4 v15, s[36:39], s40 offen offset:2048 sc1 lds
PL 76,216,27
PL 77,217,27
PL 78,218,27
PL 79,219,27
M 184,24,92
PH 76,220,27
PH 77,221,27
PH 78,222,27
PH 79,223,27
M 160,0,72
buffer_load_dwordx4 v15, s[36:39], s40 offen offset:3072 sc1 lds
RA 24,16,43008
s_waitcnt lgkmcnt(2)
PM 88,224,24
PM 89,225,24
M 164,4,72
s_addk_i32 m0, 4096
RA 28,16,44032
RA 32,16,45056
PM 90,226,24
PM 91,227,24
M 168,8,72
RA 36,16,46080
RA 40,16,47104
PH 88,228,24
PH 89,229,24
M 172,12,72
buffer_load_dwordx4 v45, s[36:39], s40 offen sc1 lds
RA 44,16,48128
RV 48,32,128
PH 90,230,24
PH 91,231,24
PL 88,232,25
M 176,16,72
RV 52,33,128
PL 89,233,25
PL 90,234,25
PL 91,235,25
M 180,20,72
buffer_load_dwordx4 v45, s[36:39], s40 offen offset:1024 sc1 lds
RV 56,34,128
PH 88,236,25
PH 89,237,25
M 204,20,76
s_add_u32 s40, s40, 24704
RV 60,35,128
RV 208,36,128
PH 90,238,25
PH 91,239,25
M 200,16,76
RV 212,37,128
RV 216,38,128
s_waitcnt lgkmcnt(8)
PM 92,240,26
PM 93,241,26
M 196,12,76
RV 220,39,128
PM 94,242,26
PM 95,243,26
PH 92,244,26
M 192,8,76
s_waitcnt vmcnt(6) lgkmcnt(6)
PH 93,245,26
PH 94,246,26
PH 95,247,26
M 188,4,76
s_barrier
PL 92,248,27
PL 93,249,27
PL 94,250,27
M 184,0,76
PL 95,251,27
PH 92,252,27
PH 93,253,27
PH 94,254,27
PH 95,255,27
M 160,24,88
RA 0,20,0
s_waitcnt lgkmcnt(2)
PM 72,48,24
PM 73,49,24
M 164,28,88
RA 4,20,1024
RA 8,20,2048
PM 74,50,24
PM 75,51,24
M 168,32,88
RA 12,20,3072
RA 16,20,4096
PH 72,52,24
PH 73,53,24
M 172,36,88
RA 20,20,5120
RV 224,32,144
PH 74,54,24
PH 75,55,24
M 176,40,88
s_add_u32 m0, s41, 24576
RV 228,33,144
PL 72,56,25
PL 73,57,25
PL 74,58,25
M 180,44,88
buffer_load_dwordx4 v15, s[36:39], s40 offen sc1 lds
RV 232,34,144
PL 75,59,25
PH 72,60,25
M 204,44,92
RV 236,35,144
RV 240,36,144
PH 73,61,25
PH 74,62,25
PH 75,63,25
M 200,40,92
RV 244,37,144
RV 248,38,144
s_waitcnt lgkmcnt(8)
PM 76,208,26
PM 77,209,26
M 196,36,92
RV 252,39,144
PM 78,210,26
PM 79,211,26
PH 76,212,26
M 192,32,92
s_branch L_in8_2_%=
L_ag8_2_%=:
s_call_b64 s[72:73], L_retry_%=
s_branch L_ok8_2_%=
.p2align 6
s_nop 0
T_b121_%=:
s_setpc_b64 s[58:59]
L_in12_1_%=:
PM 72,48,24
PM 73,49,24
M 168,32,88
PM 74,50,24
PM 75,51,24
M 172,36,88
RA 8,20,2048
RA 12,20,3072
M 176,40,88
v_pk_fmac_f16 v72, v52, v25
v_pk_fmac_f16 v73, v53, v25
M 180,44,88
s_add_u32 m0, s41, 24576
v_pk_fmac_f16 v74, v54, v25
v_pk_fmac_f16 v75, v55, v25
M 204,44,92
RA 16,20,4096
RA 20,20,5120
M 200,40,92
PM 76,56,26
PM 77,57,26
M 196,36,92
buffer_load_dwordx4 v15, s[36:39], s40 offen sc1 lds
PM 78,58,26
PM 79,59,26
M 192,32,92
RV 248,40,144
RV 252,41,144
M 188,28,92
v_pk_fmac_f16 v76, v60, v27
v_pk_fmac_f16 v77, v61, v27
M 184,24,92
v_pk_fmac_f16 v78, v62, v27
v_pk_fmac_f16 v79, v63, v27
M 208,24,96
RV 48,32,160
RV 52,33,160
M 212,28,96
PM 80,64,28
PM 81,65,28
M 216,32,96
PM 82,66,28
PM 83,67,28
M 220,36,96
RV 56,36,160
RV 60,37,160
M 224,40,96
v_pk_fmac_f16 v80, v68, v29
v_pk_fmac_f16 v81, v69, v29
M 228,44,96
PH 82,70,28
v_pk_fmac_f16 v83, v71, v29
s_waitcnt lgkmcnt(2)
M 160,0,72
RA 24,20,6144
RA 28,20,7168
M 164,4,72
PM 88,232,24
PM 89,233,24
M 168,8,72
buffer_load_dwordx4 v15, s[36:39], s40 offen offset:1024 sc1 lds
PM 90,234,24
PM 91,235,24
M 172,12,72
RA 32,20,8192
RA 36,20,9216
M 176,16,72
v_pk_fmac_f16 v88, v236, v25
v_pk_fmac_f16 v89, v237, v25
M 180,20,72
v_pk_fmac_f16 v90, v238, v25
v_pk_fmac_f16 v91, v239, v25
M 204,20,76
RA 40,20,10240
RA 44,20,11264
M 200,16,76
PM 92,240,26
PM 93,241,26
M 196,12,76
PM 94,242,26
PM 95,243,26
M 192,8,76
RV 64,40,160
RV 68,41,160
M 188,4,76
v_pk_fmac_f16 v92, v244, v27
v_pk_fmac_f16 v93, v245, v27
M 184,0,76
buffer_load_dwordx4 v15, s[36:39], s40 offen offset:2048 sc1 lds
v_pk_fmac_f16 v94, v246, v27
v_pk_fmac_f16 v95, v247, v27
M 208,0,80
RV 232,32,176
RV 236,33,176
M 212,4,80
PM 96,248,28
PM 97,249,28
M 216,8,80
PM 98,250,28
PM 99,251,28
M 220,12,80
RV 240,36,176
RV 244,37,176
M 224,16,80
v_pk_fmac_f16 v96, v252, v29
v_pk_fmac_f16 v97, v253, v29
M 228,20,80
PH 98,254,28
v_pk_fmac_f16 v99, v255, v29
s_waitcnt lgkmcnt(2)
M 160,24,88
RA 0,20,12288
RA 4,20,13312
M 164,28,88
PM 72,48,24
PM 73,49,24
M 168,32,88
buffer_load_dwordx4 v15, s[36:39], s40 offen offset:3072 sc1 lds
PM 74,50,24
M 172,36,88
RA 8,20,14336
RA 12,20,15360
M 176,40,88
PM 75,51,24
PH 72,52,24
M 180,44,88
v_pk_fmac_f16 v73, v53, v25
v_pk_fmac_f16 v74, v54, v25
M 204,44,92
RA 16,20,16384
RA 20,20,17408
M 200,40,92
PH 75,55,24
PM 76,56,26
M 196,36,92
s_add_u32 m0, m0, 4096
PM 77,57,26
M 192,32,92
RV 248,40,176
RV 252,41,176
M 188,28,92
PM 78,58,26
PM 79,59,26
M 184,24,92
buffer_load_dwordx4 v45, s[36:39], s40 offen sc1 lds
PH 76,60,26
M 208,24,96
v_pk_fmac_f16 v77, v61, v27
v_pk_fmac_f16 v78, v62, v27
M 212,28,96
PH 79,63,26
PM 80,64,28
M 216,32,96
PM 81,65,28
PM 82,66,28
M 220,36,96
PM 83,67,28
PH 80,68,28
M 224,40,96
v_pk_fmac_f16 v81, v69, v29
v_pk_fmac_f16 v82, v70, v29
M 228,44,96
v_pk_fmac_f16 v83, v71, v29
s_waitcnt lgkmcnt(0)
M 160,0,72
RA 24,20,18432
RA 28,20,19456
M 164,4,72
PM 88,232,24
PM 89,233,24
M 168,8,72
buffer_load_dwordx4 v45, s[36:39], s40 offen offset:1024 sc1 lds
PM 90,234,24
M 172,12,72
RA 32,20,20480
RA 36,20,21504
M 176,16,72
PM 91,235,24
PH 88,236,24
M 180,20,72
v_pk_fmac_f16 v89, v237, v25
v_pk_fmac_f16 v90, v238, v25
M 204,20,76
RA 40,20,22528
RA 44,20,23552
M 200,16,76
PH 91,239,24
PM 92,240,26
M 196,12,76
PM 93,241,26
PM 94,242,26
M 192,8,76
PM 95,243,26
PH 92,244,26
M 188,4,76
v_pk_fmac_f16 v93, v245, v27
v_pk_fmac_f16 v94, v246, v27
M 184,0,76
s_addk_i32 s40, 24704
v_pk_fmac_f16 v95, v247, v27
PM 96,248,28
M 208,0,80
s_lshr_b32 s42, s42, 4
s_add_u32 s43, s43, 8
PM 97,249,28
M 212,4,80
s_and_b32 s55, s42, 15
PM 98,250,28
PM 99,251,28
M 216,8,80
s_cmp_eq_u32 s55, 1
v_pk_fmac_f16 v96, v252, v29
v_pk_fmac_f16 v97, v253, v29
M 220,12,80
s_waitcnt vmcnt(6) lgkmcnt(0)
v_pk_fmac_f16 v98, v254, v29
M 224,16,80
s_barrier
v_pk_fmac_f16 v99, v255, v29
M 228,20,80
s_cbranch_scc1 L_body12_1_%=
s_branch L_disp12_1_%=
.p2align 12
.p2align 6
s_nop 0
T_a82x_%=:
s_setpc_b64 s[58:59]
L_xa82_%=:
RA 0,16,24576
s_waitcnt lgkmcnt(2)
PM 72,48,24
PM 73,49,24
M 164,28,88
RA 4,16,25600
RA 8,16,26624
PM 74,50,24
PM 75,51,24
M 168,32,88
RA 12,16,27648
RA 16,16,28672
PH 72,52,24
PH 73,53,24
M 172,36,88
RA 20,16,29696
RV 224,32,80
PH 74,54,24
PH 75,55,24
M 176,40,88
s_mov_b32 m0, s41
RV 228,33,80
PL 72,56,25
PL 73,57,25
PL 74,58,25
M 180,44,88
buffer_load_dwordx4 v15, s[36:39], s40 offen sc1 lds
RV 232,34,80
PL 75,59,25
PH 72,60,25
M 204,44,92
RV 236,35,80
RV 240,36,80
PH 73,61,25
PH 74,62,25
PH 75,63,25
M 200,40,92
RV 244,37,80
RV 248,38,80
s_waitcnt lgkmcnt(8)
PM 76,208,26
PM 77,209,26
M 196,36,92
RV 252,39,80
PM 78,210,26
PM 79,211,26
PH 76,212,26
M 192,32,92
PH 77,213,26
PH 78,214,26
PH 79,215,26
PL 76,216,27
M 188,28,92
PL 77,217,27
PL 78,218,27
PL 79,219,27
PH 76,220,27
M 184,24,92
PH 77,221,27
PH 78,222,27
PH 79,223,27
M 160,0,72
RA 24,16,30720
buffer_load_dwordx4 v15, s[36:39], s40 offen offset:1024 sc1 lds
s_waitcnt lgkmcnt(2)
PM 88,224,24
PM 89,225,24
M 164,4,72
RA 28,16,31744
RA 32,16,32768
PM 90,226,24
PM 91,227,24
M 168,8,72
RA 36,16,33792
RA 40,16,34816
PH 88,228,24
PH 89,229,24
M 172,12,72
RA 44,16,35840
RV 48,32,96
PH 90,230,24
PH 91,231,24
M 176,16,72
RV 52,33,96
PL 88,232,25
PL 89,233,25
PL 90,234,25
M 180,20,72
buffer_load_dwordx4 v15, s[36:39], s40 offen offset:2048 sc1 lds
RV 56,34,96
PL 91,235,25
PH 88,236,25
M 204,20,76
RV 60,35,96
RV 208,36,96
PH 89,237,25
PH 90,238,25
PH 91,239,25
M 200,16,76
RV 212,37,96
RV 216,38,96
s_waitcnt lgkmcnt(8)
PM 92,240,26
PM 93,241,26
M 196,12,76
RV 220,39,96
PM 94,242,26
PM 95,243,26
PH 92,244,26
M 192,8,76
PH 93,245,26
PH 94,246,26
PH 95,247,26
PL 92,248,27
M 188,4,76
PL 93,249,27
PL 94,250,27
PL 95,251,27
PH 92,252,27
M 184,0,76
PH 93,253,27
PH 94,254,27
PH 95,255,27
M 160,24,88
RA 0,16,36864
buffer_load_dwordx4 v15, s[36:39], s40 offen offset:3072 sc1 lds
s_waitcnt lgkmcnt(2)
PM 72,48,24
PM 73,49,24
M 164,28,88
RA 4,16,37888
RA 8,16,38912
PM 74,50,24
PM 75,51,24
M 168,32,88
RA 12,16,39936
RA 16,16,40960
PH 72,52,24
PH 73,53,24
M 172,36,88
RA 20,16,41984
PH 74,54,24
PH 75,55,24
PL 72,56,25
M 176,40,88
s_add_u32 m0, m0, 4096
RV 224,32,112
RV 228,33,112
PL 73,57,25
PL 74,58,25
M 180,44,88
RV 232,34,112
buffer_load_dwordx4 v45, s[36:39], s40 offen sc1 lds
PL 75,59,25
PH 72,60,25
M 204,44,92
RV 236,35,112
RV 240,36,112
PH 73,61,25
PH 74,62,25
M 200,40,92
RV 244,37,112
RV 248,38,112
PH 75,63,25
s_waitcnt lgkmcnt(8)
PM 76,208,26
M 196,36,92
RV 252,39,112
PM 77,209,26
PM 78,210,26
PM 79,211,26
M 192,32,92
PH 76,212,26
PH 77,213,26
PH 78,214,26
PH 79,215,26
M 188,28,92
PL 76,216,27
PL 77,217,27
PL 78,218,27
PL 79,219,27
M 184,24,92
PH 76,220,27
PH 77,221,27
PH 78,222,27
PH 79,223,27
M 160,0,72
RA 24,16,43008
buffer_load_dwordx4 v45, s[36:39], s40 offen offset:1024 sc1 lds
s_waitcnt lgkmcnt(2)
PM 88,224,24
PM 89,225,24
M 164,4,72
RA 28,16,44032
RA 32,16,45056
PM 90,226,24
PM 91,227,24
M 168,8,72
RA 36,16,46080
RA 40,16,47104
PH 88,228,24
PH 89,229,24
M 172,12,72
RA 44,16,48128
RV 48,32,128
PH 90,230,24
PH 91,231,24
PL 88,232,25
M 176,16,72
RV 52,33,128
PL 89,233,25
PL 90,234,25
PL 91,235,25
M 180,20,72
RV 56,34,128
PH 88,236,25
PH 89,237,25
M 204,20,76
RV 60,35,128
RV 208,36,128
PH 90,238,25
PH 91,239,25
M 200,16,76
RV 212,37,128
RV 216,38,128
s_waitcnt lgkmcnt(8)
PM 92,240,26
PM 93,241,26
M 196,12,76
s_add_u32 s40, s40, 24704
RV 220,39,128
PM 94,242,26
PM 95,243,26
PH 92,244,26
M 192,8,76
s_waitcnt vmcnt(6) lgkmcnt(6)
PH 93,245,26
PH 94,246,26
PH 95,247,26
M 188,4,76
s_barrier
PL 92,248,27
PL 93,249,27
PL 94,250,27
M 184,0,76
PL 95,251,27
PH 92,252,27
PH 93,253,27
PH 94,254,27
PH 95,255,27
M 160,24,88
RA 0,20,0
s_waitcnt lgkmcnt(2)
PM 72,48,24
PM 73,49,24
M 164,28,88
RA 4,20,1024
RA 8,20,2048
PM 74,50,24
PM 75,51,24
M 168,32,88
RA 12,20,3072
RA 16,20,4096
PH 72,52,24
PH 73,53,24
M 172,36,88
RA 20,20,5120
RV 224,32,144
PH 74,54,24
PH 75,55,24
M 176,40,88
s_add_u32 m0, s41, 24576
RV 228,33,144
PL 72,56,25
PL 73,57,25
PL 74,58,25
M 180,44,88
buffer_load_dwordx4 v15, s[36:39], s40 offen sc1 lds
RV 232,34,144
PL 75,59,25
PH 72,60,25
M 204,44,92
RV 236,35,144
RV 240,36,144
PH 73,61,25
PH 74,62,25
PH 75,63,25
M 200,40,92
RV 244,37,144
RV 248,38,144
s_waitcnt lgkmcnt(8)
PM 76,208,26
PM 77,209,26
M 196,36,92
RV 252,39,144
PM 78,210,26
PM 79,211,26
PH 76,212,26
M 192,32,92
s_branch L_in8_2_%=
.p2align 6
s_nop 0
T_t81_%=:
s_setpc_b64 s[58:59]
L_sel8_1_%=:
s_cmp_eq_u32 s55, 0
s_cbranch_scc1 L_bodz8_0_%=
s_cmp_eq_u32 s55, 1
s_cbranch_scc1 L_bodz8_1_%=
s_cmp_eq_u32 s55, 4
s_cbranch_scc1 L_tail8_0_%=
s_cmp_eq_u32 s55, 5
s_cbranch_scc1 L_tail8_1_%=
s_cmp_lt_u32 s55, 4
s_cbranch_scc1 L_bodz8_2_%=
s_branch L_tail8_2_%=
L_disp8_1_%=:
M 160,24,88
s_cmp_lg_u32 s55, 5
s_cbranch_scc1 L_sel8_1_%=
L_tail8_1_%=:
RA 0,16,0
RA 4,16,1024
v_add_u32 v32, s44, v152
v_mov_b32_e64 v6, v156
M 164,28,88
RA 8,16,2048
RA 12,16,3072
v_pk_max_i16 v25, v6, s50
v_pk_min_i16 v25, v25, s52
M 168,32,88
RA 16,16,4096
RA 20,16,5120
v_mad_i32_i16 v33, v25, s53, v32
v_mad_i32_i16 v33, v25, s54, v33 op_sel:[1,0,0,0]
M 172,36,88
RV 104,32,0
RV 48,33,0
v_add_u32 v36, s44, v153
v_mov_b32 v7, v157
M 176,40,88
RV 108,32,16
RV 52,33,16
v_pk_max_i16 v27, v7, s50
v_pk_min_i16 v27, v27, s52
M 180,44,88
RA 68,16,11264
RA 64,16,10240
v_mad_i32_i16 v37, v27, s53, v36
v_mad_i32_i16 v37, v27, s54, v37 op_sel:[1,0,0,0]
M 204,44,92
RA 60,16,9216
RA 56,16,8192
v_and_b32_e64 v6, s56, v6
v_pk_add_f16 v25, v6, v6 op_sel:[0,1] op_sel_hi:[1,0]
M 200,40,92
RA 52,16,7168
RA 48,16,6144
v_pk_add_f16 v24, v25, s51 neg_lo:[1,0]
v_and_b32 v7, s56, v7
M 196,36,92
RV 112,32,32
RV 72,33,32
v_pk_add_f16 v27, v7, v7 op_sel:[0,1] op_sel_hi:[1,0]
v_pk_add_f16 v26, v27, s51 neg_lo:[1,0]
s_waitcnt lgkmcnt(8)
PM 104,104,24
M 192,32,92
RA 72,16,12288
RA 76,16,13312
PM 105,105,24
PM 106,106,24
PM 107,107,24
v_pk_fmac_f16 v104, v48, v25
v_pk_fmac_f16 v105, v49, v25
v_pk_fmac_f16 v106, v50, v25
v_pk_fmac_f16 v107, v51, v25
M 188,28,92
M 184,24,92
RA 80,16,14336
RA 84,16,15360
PM 108,108,24
M 160,0,104
RA 88,16,16384
RA 92,16,17408
PM 109,109,24
M 164,4,104
RV 116,36,0
RV 76,37,0
M 168,8,104
RV 120,36,16
RV 48,37,16
PM 110,110,24
M 172,12,104
PM 111,111,24
v_pk_fmac_f16 v108, v52, v25
v_pk_fmac_f16 v109, v53, v25
PH 110,54,24
v_pk_fmac_f16 v111, v55, v25
M 176,16,104
M 180,20,104
RV 124,36,32
RV 52,37,32
s_waitcnt lgkmcnt(6)
PM 112,112,24
M 180,68,108
PM 113,113,24
PM 114,114,24
s_mul_i32 s11, s23, 192
s_mul_i32 s16, s21, 43008
s_add_u32 s16, s16, s11
s_add_u32 s17, s16, 43008
s_add_u32 s18, s16, 86016
v_and_b32 v47, 0xfff, v46
v_add_u32 v12, 162304, v15
s_waitcnt vmcnt(0)
M 176,64,108
M 172,60,108
M 168,56,108
PM 115,115,24
v_pk_fmac_f16 v112, v72, v25
v_pk_fmac_f16 v113, v73, v25
v_pk_fmac_f16 v114, v74, v25
v_pk_fmac_f16 v115, v75, v25
M 164,52,108
M 160,48,108
M 160,72,112
buffer_store_dword v31, v30, s[36:39], 0 offen sc1
RV 84,12,0
M 164,76,112
s_waitcnt lgkmcnt(1)
PM 116,116,26
PM 117,117,26
PM 118,118,26
M 168,80,112
PM 119,119,26
v_pk_fmac_f16 v116, v76, v27
M 172,84,112
v_pk_fmac_f16 v117, v77, v27
v_pk_fmac_f16 v118, v78, v27
PH 119,79,26
M 176,88,112
PM 120,120,26
PM 121,121,26
M 180,92,112
PM 122,122,26
PM 123,123,26
PH 120,48,26
M 184,0,116
v_pk_fmac_f16 v121, v49, v27
v_pk_fmac_f16 v122, v50, v27
PH 123,51,26
M 188,4,116
PM 124,124,26
PM 125,125,26
M 192,8,116
PM 126,126,26
PM 127,127,26
PH 124,52,26
M 196,12,116
v_pk_fmac_f16 v125, v53, v27
v_pk_fmac_f16 v126, v54, v27
M 200,16,116
PH 127,55,26
v_cvt_pk_bf16_f32 v88, v160, v161
v_cvt_pk_bf16_f32 v89, v162, v163
M 204,20,116
v_cvt_pk_bf16_f32 v90, v164, v165
v_cvt_pk_bf16_f32 v91, v166, v167
v_cvt_pk_bf16_f32 v92, v168, v169
M 204,68,120
buffer_store_dwordx4 v[88:91], v47, s[68:71], s16 offen offset:0 sc0 sc1 nt
M 200,64,120
v_cvt_pk_bf16_f32 v93, v170, v171
v_cvt_pk_bf16_f32 v94, v172, v173
v_cvt_pk_bf16_f32 v95, v174, v175
M 196,60,120
v_cvt_pk_bf16_f32 v96, v176, v177
v_cvt_pk_bf16_f32 v97, v178, v179
v_cvt_pk_bf16_f32 v98, v180, v181
M 192,56,120
v_cvt_pk_bf16_f32 v99, v182, v183
s_waitcnt lgkmcnt(0)
v_or3_b32 v84, v84, v85, v86
M 188,52,120
v_or3_b32 v84, v84, v87, v5
v_and_b32 v85, 0x40004001, v84
v_and_b32 v86, 0x70007000, v84
M 184,48,120
buffer_store_dwordx4 v[92:95], v47, s[68:71], s16 offen offset:64 sc0 sc1 nt
M 184,72,124
v_and_b32_e64 v87, 2, v84
v_cmp_ne_u32 vcc, 0, v85
v_cmp_eq_u32 s[30:31], 0, v86
M 188,76,124
v_cmp_eq_u32 s[58:59], 0, v87
s_cmp_eq_u64 s[30:31], -1
s_cselect_b64 vcc, -1, vcc
M 192,80,124
s_cmp_eq_u64 s[58:59], -1
s_cselect_b64 vcc, -1, vcc
M 196,84,124
v_cvt_pk_bf16_f32 v88, v184, v185
v_cvt_pk_bf16_f32 v89, v186, v187
M 200,88,124
buffer_store_dwordx4 v[96:99], v47, s[68:71], s16 offen offset:128 sc0 sc1 nt
v_cvt_pk_bf16_f32 v90, v188, v189
M 204,92,124
v_cvt_pk_bf16_f32 v91, v190, v191
v_cvt_pk_bf16_f32 v92, v192, v193
v_cvt_pk_bf16_f32 v93, v194, v195
ds_write_b128 v4, v[88:91] offset:0
v_cvt_pk_bf16_f32 v94, v196, v197
v_cvt_pk_bf16_f32 v95, v198, v199
v_cvt_pk_bf16_f32 v96, v200, v201
v_cvt_pk_bf16_f32 v97, v202, v203
v_cvt_pk_bf16_f32 v98, v204, v205
v_cvt_pk_bf16_f32 v99, v206, v207
ds_write_b128 v4, v[92:95] offset:64
ds_write_b128 v4, v[96:99] offset:128
RV 144,1,0
RV 148,2,0
RV 100,3,0
s_waitcnt lgkmcnt(0)
buffer_store_dwordx4 v[144:147], v15, s[68:71], s17 offen offset:0 sc0 sc1 nt
buffer_store_dwordx4 v[148:151], v15, s[68:71], s17 offen offset:1024 sc0 sc1 nt
buffer_store_dwordx4 v[100:103], v15, s[68:71], s17 offen offset:2048 sc0 sc1 nt
s_cbranch_vccnz L_done_%=
s_endpgm
.p2align 12
.p2align 6
s_nop 0
T_e130_%=:
s_setpc_b64 s[58:59]
L_ent13_0_%=:
RA 0,16,0
RA 4,16,1024
RA 8,16,2048
RA 12,16,3072
RA 16,16,4096
RA 20,16,5120
s_waitcnt lgkmcnt(6)
s_barrier
RV 72,32,0
RV 76,36,0
RV 80,40,0
RV 84,44,0
s_waitcnt lgkmcnt(0)
s_nop 0
s_waitcnt lgkmcnt(0)
v_mfma_f32_16x16x32_f16 v[160:163], a[0:3], v[72:75], 0
RV 88,33,16
v_mfma_f32_16x16x32_f16 v[164:167], a[4:7], v[72:75], 0
RV 92,37,16
v_mfma_f32_16x16x32_f16 v[168:171], a[8:11], v[72:75], 0
RV 96,41,16
v_mfma_f32_16x16x32_f16 v[172:175], a[12:15], v[72:75], 0
RA 24,17,6144
v_mfma_f32_16x16x32_f16 v[176:179], a[16:19], v[72:75], 0
RA 28,17,7168
v_mfma_f32_16x16x32_f16 v[180:183], a[20:23], v[72:75], 0
RA 32,17,8192
v_mfma_f32_16x16x32_f16 v[204:207], a[20:23], v[76:79], 0
RA 36,17,9216
v_mfma_f32_16x16x32_f16 v[200:203], a[16:19], v[76:79], 0
RA 40,17,10240
v_mfma_f32_16x16x32_f16 v[196:199], a[12:15], v[76:79], 0
RA 44,17,11264
v_mfma_f32_16x16x32_f16 v[192:195], a[8:11], v[76:79], 0
s_add_u32 m0, s41, 24576
v_mfma_f32_16x16x32_f16 v[188:191], a[4:7], v[76:79], 0
buffer_load_dwordx4 v15, s[36:39], s40 offen sc1 lds
v_mfma_f32_16x16x32_f16 v[184:187], a[0:3], v[76:79], 0
v_mfma_f32_16x16x32_f16 v[208:211], a[0:3], v[80:83], 0
buffer_load_dwordx4 v15, s[36:39], s40 offen offset:1024 sc1 lds
v_mfma_f32_16x16x32_f16 v[212:215], a[4:7], v[80:83], 0
v_mfma_f32_16x16x32_f16 v[216:219], a[8:11], v[80:83], 0
buffer_load_dwordx4 v15, s[36:39], s40 offen offset:2048 sc1 lds
v_mfma_f32_16x16x32_f16 v[220:223], a[12:15], v[80:83], 0
v_mfma_f32_16x16x32_f16 v[224:227], a[16:19], v[80:83], 0
v_mfma_f32_16x16x32_f16 v[228:231], a[20:23], v[80:83], 0
buffer_load_dwordx4 v15, s[36:39], s40 offen offset:3072 sc1 lds
v_mfma_f32_16x16x32_f16 v[252:255], a[20:23], v[84:87], 0
s_addk_i32 m0, 4096
v_mfma_f32_16x16x32_f16 v[248:251], a[16:19], v[84:87], 0
buffer_load_dwordx4 v45, s[36:39], s40 offen sc1 lds
v_mfma_f32_16x16x32_f16 v[244:247], a[12:15], v[84:87], 0
v_mfma_f32_16x16x32_f16 v[240:243], a[8:11], v[84:87], 0
buffer_load_dwordx4 v45, s[36:39], s40 offen offset:1024 sc1 lds
v_mfma_f32_16x16x32_f16 v[236:239], a[4:7], v[84:87], 0
s_addk_i32 s40, 24704
v_mfma_f32_16x16x32_f16 v[232:235], a[0:3], v[84:87], 0
s_waitcnt lgkmcnt(0)
M 160,24,88
RV 72,34,32
M 164,28,88
RV 76,38,32
M 168,32,88
RV 80,42,32
M 172,36,88
RA 0,18,12288
M 176,40,88
RA 4,18,13312
M 180,44,88
RA 8,18,14336
M 204,44,92
RA 12,18,15360
M 200,40,92
RA 16,18,16384
M 196,36,92
RA 20,18,17408
M 192,32,92
M 188,28,92
M 184,24,92
M 208,24,96
M 212,28,96
M 216,32,96
M 220,36,96
M 224,40,96
M 228,44,96
s_waitcnt lgkmcnt(0)
M 160,0,72
RV 88,35,48
M 164,4,72
RV 92,39,48
M 168,8,72
RV 96,43,48
M 172,12,72
RA 24,19,18432
M 176,16,72
RA 28,19,19456
M 180,20,72
RA 32,19,20480
M 204,20,76
RA 36,19,21504
M 200,16,76
RA 40,19,22528
M 196,12,76
RA 44,19,23552
M 192,8,76
M 188,4,76
v_cmp_nge_f32 vcc, 1.0, v12
M 184,0,76
buffer_load_dwordx4 v[6:9], v15, s[36:39], 0 offen sc1
v_and_b32 v13, 0x7ff87ff8, v13
M 208,0,80
s_lshl2_add_u32 s11, s10, 162304
M 212,4,80
buffer_load_dwordx4 v[100:103], v47, s[36:39], 0 offen offset:1024 sc1
v_addc_co_u32 v13, vcc, 0, v13, vcc
M 216,8,80
v_add_u32 v12, s11, v15
M 220,12,80
s_waitcnt vmcnt(2) lgkmcnt(0)
M 224,16,80
s_barrier
M 228,20,80
M 160,24,88
RV 72,32,64
M 164,28,88
ds_write_b32 v12, v13
RV 76,36,64
M 168,32,88
RV 80,40,64
M 172,36,88
RV 84,44,64
M 176,40,88
RA 0,16,24576
M 180,44,88
RA 4,16,25600
M 204,44,92
RA 8,16,26624
M 200,40,92
RA 12,16,27648
M 196,36,92
RA 16,16,28672
M 192,32,92
RA 20,16,29696
M 188,28,92
M 184,24,92
M 208,24,96
M 212,28,96
M 216,32,96
M 220,36,96
M 224,40,96
M 228,44,96
s_waitcnt lgkmcnt(0)
M 160,0,72
RV 88,33,80
s_waitcnt vmcnt(0)
v_bitop3_b32 v6, v6, v8, s66 bitop3:0x96
M 164,4,72
RV 92,37,80
v_bitop3_b32 v100, v100, v102, s66 bitop3:0x96
v_bitop3_b32 v7, v7, v9, s67 bitop3:0x96
M 168,8,72
RV 96,41,80
v_bitop3_b32 v101, v101, v103, s67 bitop3:0x96
v_or_b32_e64 v6, v6, v100
M 172,12,72
RA 24,17,30720
v_or_b32 v5, v7, v101
v_cmp_ne_u32 vcc, 0, v6
M 176,16,72
RA 28,17,31744
v_cmp_lt_u32 s[30:31], 7, v5
s_or_b64 vcc, vcc, s[30:31]
M 180,20,72
RA 32,17,32768
s_cbranch_scc1 L_ag13_0_%=
L_ok13_0_%=:
M 204,20,76
s_add_u32 m0, s41, 49152
RA 36,17,33792
M 200,16,76
buffer_load_dwordx4 v15, s[36:39], s40 offen sc1 lds
RA 40,17,34816
M 196,12,76
RA 44,17,35840
M 192,8,76
M 188,4,76
buffer_load_dwordx4 v15, s[36:39], s40 offen offset:1024 sc1 lds
M 184,0,76
M 208,0,80
M 212,4,80
buffer_load_dwordx4 v15, s[36:39], s40 offen offset:2048 sc1 lds
M 216,8,80
M 220,12,80
M 224,16,80
buffer_load_dwordx4 v15, s[36:39], s40 offen offset:3072 sc1 lds
M 228,20,80
s_add_u32 m0, m0, 4096
M 252,20,84
M 248,16,84
buffer_load_dwordx4 v45, s[36:39], s40 offen sc1 lds
M 244,12,84
M 240,8,84
M 236,4,84
buffer_load_dwordx4 v45, s[36:39], s40 offen offset:1024 sc1 lds
M 232,0,84
s_add_u32 s40, s40, 24704
s_waitcnt lgkmcnt(0)
M 160,24,88
RV 72,34,96
M 164,28,88
RV 76,38,96
M 168,32,88
s_add_u32 m0, s41, 0
RV 80,42,96
M 172,36,88
buffer_load_dwordx4 v15, s[36:39], s40 offen sc1 lds
RA 0,18,36864
M 176,40,88
RA 4,18,37888
M 180,44,88
RA 8,18,38912
M 204,44,92
RA 12,18,39936
M 200,40,92
buffer_load_dwordx4 v15, s[36:39], s40 offen offset:1024 sc1 lds
RA 16,18,40960
M 196,36,92
RA 20,18,41984
M 192,32,92
M 188,28,92
M 184,24,92
M 208,24,96
buffer_load_dwordx4 v15, s[36:39], s40 offen offset:2048 sc1 lds
M 212,28,96
M 216,32,96
M 220,36,96
M 224,40,96
M 228,44,96
buffer_load_dwordx4 v15, s[36:39], s40 offen offset:3072 sc1 lds
s_waitcnt lgkmcnt(0)
M 160,0,72
s_addk_i32 m0, 4096
RV 88,35,112
M 164,4,72
RV 92,39,112
M 168,8,72
RV 96,43,112
M 172,12,72
RA 24,19,43008
M 176,16,72
buffer_load_dwordx4 v45, s[36:39], s40 offen sc1 lds
RA 28,19,44032
M 180,20,72
RA 32,19,45056
M 204,20,76
RA 36,19,46080
M 200,16,76
RA 40,19,47104
M 196,12,76
RA 44,19,48128
M 192,8,76
buffer_load_dwordx4 v45, s[36:39], s40 offen offset:1024 sc1 lds
M 188,4,76
s_add_u32 s40, s40, 24704
M 184,0,76
M 208,0,80
M 212,4,80
M 216,8,80
M 220,12,80
s_waitcnt vmcnt(6) lgkmcnt(0)
M 224,16,80
s_barrier
M 228,20,80
M 160,24,88
RV 72,32,128
M 164,28,88
RV 76,36,128
M 168,32,88
RV 80,40,128
M 172,36,88
RV 84,44,128
M 176,40,88
RA 0,20,0
M 180,44,88
RA 4,20,1024
M 204,44,92
RA 8,20,2048
M 200,40,92
RA 12,20,3072
M 196,36,92
RA 16,20,4096
M 192,32,92
RA 20,20,5120
M 188,28,92
s_branch L_in13_0_%=
L_ag13_0_%=:
s_call_b64 s[72:73], L_retry_%=
s_branch L_ok13_0_%=
.p2align 6
s_nop 0
T_b82_%=:
s_setpc_b64 s[58:59]
L_in8_2_%=:
PH 77,213,26
PH 78,214,26
PH 79,215,26
PL 76,216,27
M 188,28,92
PL 77,217,27
PL 78,218,27
PL 79,219,27
PH 76,220,27
M 184,24,92
PH 77,221,27
PH 78,222,27
PH 79,223,27
M 160,0,72
RA 24,20,6144
buffer_load_dwordx4 v15, s[36:39], s40 offen offset:1024 sc1 lds
s_waitcnt lgkmcnt(2)
PM 88,224,24
PM 89,225,24
M 164,4,72
RA 28,20,7168
RA 32,20,8192
PM 90,226,24
PM 91,227,24
M 168,8,72
RA 36,20,9216
RA 40,20,10240
PH 88,228,24
PH 89,229,24
M 172,12,72
RA 44,20,11264
RV 48,32,160
PH 90,230,24
PH 91,231,24
M 176,16,72
RV 52,33,160
PL 88,232,25
PL 89,233,25
PL 90,234,25
M 180,20,72
buffer_load_dwordx4 v15, s[36:39], s40 offen offset:2048 sc1 lds
RV 56,34,160
PL 91,235,25
PH 88,236,25
M 204,20,76
RV 60,35,160
RV 208,36,160
PH 89,237,25
PH 90,238,25
PH 91,239,25
M 200,16,76
RV 212,37,160
RV 216,38,160
s_waitcnt lgkmcnt(8)
PM 92,240,26
PM 93,241,26
M 196,12,76
RV 220,39,160
PM 94,242,26
PM 95,243,26
PH 92,244,26
M 192,8,76
PH 93,245,26
PH 94,246,26
PH 95,247,26
PL 92,248,27
M 188,4,76
PL 93,249,27
PL 94,250,27
PL 95,251,27
PH 92,252,27
M 184,0,76
PH 93,253,27
PH 94,254,27
PH 95,255,27
M 160,24,88
RA 0,20,12288
buffer_load_dwordx4 v15, s[36:39], s40 offen offset:3072 sc1 lds
s_waitcnt lgkmcnt(2)
PM 72,48,24
PM 73,49,24
M 164,28,88
RA 4,20,13312
RA 8,20,14336
PM 74,50,24
PM 75,51,24
M 168,32,88
RA 12,20,15360
RA 16,20,16384
PH 72,52,24
PH 73,53,24
M 172,36,88
RA 20,20,17408
PH 74,54,24
PH 75,55,24
PL 72,56,25
M 176,40,88
s_addk_i32 m0, 4096
RV 224,32,176
RV 228,33,176
PL 73,57,25
PL 74,58,25
M 180,44,88
RV 232,34,176
buffer_load_dwordx4 v45, s[36:39], s40 offen sc1 lds
PL 75,59,25
PH 72,60,25
M 204,44,92
RV 236,35,176
RV 240,36,176
PH 73,61,25
PH 74,62,25
M 200,40,92
RV 244,37,176
RV 248,38,176
PH 75,63,25
)ASM" R"ASM(
s_waitcnt lgkmcnt(8)
PM 76,208,26
M 196,36,92
RV 252,39,176
PM 77,209,26
PM 78,210,26
PM 79,211,26
M 192,32,92
PH 76,212,26
PH 77,213,26
PH 78,214,26
PH 79,215,26
M 188,28,92
PL 76,216,27
PL 77,217,27
PL 78,218,27
PL 79,219,27
M 184,24,92
PH 76,220,27
PH 77,221,27
PH 78,222,27
PH 79,223,27
M 160,0,72
RA 24,20,18432
buffer_load_dwordx4 v45, s[36:39], s40 offen offset:1024 sc1 lds
s_waitcnt lgkmcnt(2)
PM 88,224,24
PM 89,225,24
M 164,4,72
RA 28,20,19456
RA 32,20,20480
PM 90,226,24
PM 91,227,24
M 168,8,72
RA 36,20,21504
PH 88,228,24
PH 89,229,24
M 172,12,72
s_add_u32 s40, s40, 24704
RA 40,20,22528
RA 44,20,23552
PH 90,230,24
PH 91,231,24
M 176,16,72
PL 88,232,25
PL 89,233,25
PL 90,234,25
PL 91,235,25
M 180,20,72
PH 88,236,25
PH 89,237,25
PH 90,238,25
PH 91,239,25
M 204,20,76
s_waitcnt lgkmcnt(3)
PM 92,240,26
PM 93,241,26
PM 94,242,26
PM 95,243,26
M 200,16,76
PH 92,244,26
PH 93,245,26
s_lshr_b32 s42, s42, 4
s_add_u32 s43, s43, 8
M 196,12,76
s_and_b32 s55, s42, 15
s_cmp_eq_u32 s55, 3
PH 94,246,26
PH 95,247,26
M 192,8,76
s_waitcnt vmcnt(6) lgkmcnt(0)
PL 92,248,27
PL 93,249,27
PL 94,250,27
PL 95,251,27
M 188,4,76
s_barrier
PH 92,252,27
PH 93,253,27
PH 94,254,27
PH 95,255,27
M 184,0,76
s_cbranch_scc1 L_body8_2_%=
s_branch L_disp8_2_%=
.p2align 12
.p2align 6
s_nop 0
T_t121_%=:
s_setpc_b64 s[58:59]
L_sel12_1_%=:
s_cmp_eq_u32 s55, 0
s_cbranch_scc1 L_bodz12_0_%=
s_cmp_eq_u32 s55, 1
s_cbranch_scc1 L_bodz12_1_%=
s_cmp_eq_u32 s55, 4
s_cbranch_scc1 L_tail12_0_%=
s_cmp_eq_u32 s55, 5
s_cbranch_scc1 L_tail12_1_%=
s_cmp_lt_u32 s55, 4
s_cbranch_scc1 L_bodz12_2_%=
s_branch L_tail12_2_%=
L_disp12_1_%=:
M 160,24,88
s_cmp_lg_u32 s55, 5
s_cbranch_scc1 L_sel12_1_%=
L_tail12_1_%=:
RA 0,16,0
v_add_u32 v32, s44, v152
v_mov_b32_e64 v6, v156
M 164,28,88
RA 4,16,1024
v_pk_max_i16 v25, v6, s50
v_pk_min_i16 v25, v25, s52
v_mad_i32_i16 v33, v25, s53, v32
M 168,32,88
RA 8,16,2048
v_mad_i32_i16 v33, v25, s54, v33 op_sel:[1,0,0,0]
v_add_u32_e64 v36, s44, v153
M 172,36,88
RA 12,16,3072
v_mov_b32_e64 v7, v157
v_pk_max_i16 v27, v7, s50
v_pk_min_i16 v27, v27, s52
M 176,40,88
RA 16,16,4096
v_mad_i32_i16 v37, v27, s53, v36
v_mad_i32_i16 v37, v27, s54, v37 op_sel:[1,0,0,0]
M 180,44,88
RA 20,16,5120
v_add_u32 v40, s44, v154
v_mov_b32 v8, v158
v_pk_max_i16 v29, v8, s50
M 204,44,92
RV 104,32,0
v_pk_min_i16 v29, v29, s52
v_mad_i32_i16 v41, v29, s53, v40
M 200,40,92
RV 48,33,0
v_mad_i32_i16 v41, v29, s54, v41 op_sel:[1,0,0,0]
v_and_b32_e64 v6, s56, v6
v_pk_add_f16 v25, v6, v6 op_sel:[0,1] op_sel_hi:[1,0]
M 196,36,92
RV 108,32,16
v_pk_add_f16 v24, v25, s51 neg_lo:[1,0]
v_and_b32_e64 v7, s56, v7
v_pk_add_f16 v27, v7, v7 op_sel:[0,1] op_sel_hi:[1,0]
M 192,32,92
RV 52,33,16
v_pk_add_f16 v26, v27, s51 neg_lo:[1,0]
v_and_b32 v8, s56, v8
M 188,28,92
RV 112,32,32
v_pk_add_f16 v29, v8, v8 op_sel:[0,1] op_sel_hi:[1,0]
v_pk_add_f16 v28, v29, s51 neg_lo:[1,0]
s_mul_i32 s11, s23, 192
M 184,24,92
RV 72,33,32
s_mul_i32 s16, s21, 43008
s_waitcnt lgkmcnt(4)
PM 104,104,24
M 208,24,96
RA 68,16,11264
PM 105,105,24
PM 106,106,24
PM 107,107,24
M 212,28,96
RA 64,16,10240
v_pk_fmac_f16 v104, v48, v25
v_pk_fmac_f16 v105, v49, v25
M 216,32,96
RA 60,16,9216
PH 106,50,24
v_pk_fmac_f16 v107, v51, v25
s_waitcnt lgkmcnt(4)
PM 108,108,24
M 220,36,96
RA 56,16,8192
PM 109,109,24
PM 110,110,24
M 224,40,96
RA 52,16,7168
PM 111,111,24
v_pk_fmac_f16 v108, v52, v25
v_pk_fmac_f16 v109, v53, v25
M 228,44,96
RA 48,16,6144
v_pk_fmac_f16 v110, v54, v25
v_pk_fmac_f16 v111, v55, v25
s_add_u32 s16, s16, s11
M 160,0,104
RV 116,36,0
s_waitcnt lgkmcnt(4)
PM 112,112,24
M 164,4,104
RV 76,37,0
PM 113,113,24
PM 114,114,24
M 168,8,104
RA 72,16,12288
PM 115,115,24
M 172,12,104
RA 76,16,13312
v_pk_fmac_f16 v112, v72, v25
v_pk_fmac_f16 v113, v73, v25
M 176,16,104
RA 80,16,14336
PH 114,74,24
M 180,20,104
RA 84,16,15360
PH 115,75,24
s_add_u32 s17, s16, 43008
M 180,68,108
RA 88,16,16384
s_add_u32 s18, s16, 86016
s_waitcnt lgkmcnt(5)
PM 116,116,26
v_and_b32 v47, 0xfff, v46
v_add_u32 v12, 162304, v15
s_waitcnt vmcnt(0)
M 176,64,108
RA 92,16,17408
M 172,60,108
buffer_store_dword v31, v30, s[36:39], 0 offen sc1
M 168,56,108
RV 120,36,16
M 164,52,108
RV 48,37,16
PM 117,117,26
PM 118,118,26
M 160,48,108
RV 124,36,32
PM 119,119,26
s_waitcnt lgkmcnt(4)
M 160,72,112
RV 52,37,32
v_pk_fmac_f16 v116, v76, v27
PH 117,77,26
M 164,76,112
RV 128,40,0
PH 118,78,26
M 168,80,112
RV 72,41,0
v_pk_fmac_f16 v119, v79, v27
v_cvt_pk_bf16_f32 v88, v160, v161
M 172,84,112
RV 132,40,16
s_waitcnt lgkmcnt(5)
PM 120,120,26
PM 121,121,26
M 176,88,112
RV 76,41,16
PM 122,122,26
M 180,92,112
RV 84,12,0
PM 123,123,26
PH 120,48,26
M 184,0,116
v_pk_fmac_f16 v121, v49, v27
v_pk_fmac_f16 v122, v50, v27
M 188,4,116
v_pk_fmac_f16 v123, v51, v27
s_waitcnt lgkmcnt(3)
PM 124,124,26
PM 125,125,26
M 192,8,116
RV 136,40,32
PM 126,126,26
M 196,12,116
RV 48,41,32
PM 127,127,26
PH 124,52,26
M 200,16,116
v_pk_fmac_f16 v125, v53, v27
v_pk_fmac_f16 v126, v54, v27
PH 127,55,26
M 204,20,116
v_cvt_pk_bf16_f32 v89, v162, v163
v_cvt_pk_bf16_f32 v90, v164, v165
M 204,68,120
PM 128,128,28
PM 129,129,28
PM 130,130,28
M 200,64,120
PM 131,131,28
PH 128,72,28
M 196,60,120
v_pk_fmac_f16 v129, v73, v29
v_pk_fmac_f16 v130, v74, v29
PH 131,75,28
M 192,56,120
v_cvt_pk_bf16_f32 v91, v166, v167
v_cvt_pk_bf16_f32 v92, v168, v169
M 188,52,120
buffer_store_dwordx4 v[88:91], v47, s[68:71], s16 offen offset:0 sc0 sc1 nt
v_cvt_pk_bf16_f32 v93, v170, v171
M 184,48,120
v_cvt_pk_bf16_f32 v94, v172, v173
v_cvt_pk_bf16_f32 v95, v174, v175
M 184,72,124
s_waitcnt lgkmcnt(0)
PM 132,132,28
PM 133,133,28
PM 134,134,28
M 188,76,124
PM 135,135,28
v_pk_fmac_f16 v132, v76, v29
PH 133,77,28
M 192,80,124
buffer_store_dwordx4 v[92:95], v47, s[68:71], s16 offen offset:64 sc0 sc1 nt
M 196,84,124
v_pk_fmac_f16 v134, v78, v29
v_pk_fmac_f16 v135, v79, v29
v_cvt_pk_bf16_f32 v96, v176, v177
M 200,88,124
v_cvt_pk_bf16_f32 v97, v178, v179
v_cvt_pk_bf16_f32 v98, v180, v181
M 204,92,124
v_cvt_pk_bf16_f32 v99, v182, v183
v_cvt_pk_bf16_f32 v88, v184, v185
v_cvt_pk_bf16_f32 v89, v186, v187
M 208,0,128
PM 136,136,28
PM 137,137,28
M 212,4,128
buffer_store_dwordx4 v[96:99], v47, s[68:71], s16 offen offset:128 sc0 sc1 nt
PM 138,138,28
M 216,8,128
PM 139,139,28
PH 136,48,28
M 220,12,128
v_pk_fmac_f16 v137, v49, v29
v_pk_fmac_f16 v138, v50, v29
PH 139,51,28
M 224,16,128
v_cvt_pk_bf16_f32 v90, v188, v189
v_cvt_pk_bf16_f32 v91, v190, v191
v_cvt_pk_bf16_f32 v92, v192, v193
M 228,20,128
ds_write_b128 v4, v[88:91] offset:0
M 228,68,132
v_cvt_pk_bf16_f32 v93, v194, v195
v_cvt_pk_bf16_f32 v94, v196, v197
v_cvt_pk_bf16_f32 v95, v198, v199
M 224,64,132
v_cvt_pk_bf16_f32 v96, v200, v201
v_cvt_pk_bf16_f32 v97, v202, v203
M 220,60,132
ds_write_b128 v4, v[92:95] offset:64
v_cvt_pk_bf16_f32 v98, v204, v205
M 216,56,132
v_cvt_pk_bf16_f32 v99, v206, v207
v_or3_b32 v84, v84, v85, v86
M 212,52,132
v_or3_b32 v84, v84, v87, v5
v_and_b32 v85, 0x40004001, v84
v_and_b32 v86, 0x70007000, v84
M 208,48,132
ds_write_b128 v4, v[96:99] offset:128
RV 144,1,0
M 208,72,136
RV 148,2,0
RV 100,3,0
M 212,76,136
v_and_b32 v87, 2, v84
v_cmp_ne_u32 vcc, 0, v85
v_cmp_eq_u32 s[30:31], 0, v86
M 216,80,136
v_cmp_eq_u32 s[58:59], 0, v87
v_cvt_pk_bf16_f32 v88, v208, v209
M 220,84,136
v_cvt_pk_bf16_f32 v89, v210, v211
v_cvt_pk_bf16_f32 v90, v212, v213
v_cvt_pk_bf16_f32 v91, v214, v215
M 224,88,136
ds_write_b128 v4, v[88:91] offset:0
M 228,92,136
v_cvt_pk_bf16_f32 v92, v216, v217
v_cvt_pk_bf16_f32 v93, v218, v219
v_cvt_pk_bf16_f32 v94, v220, v221
v_cvt_pk_bf16_f32 v95, v222, v223
s_waitcnt lgkmcnt(1)
buffer_store_dwordx4 v[144:147], v15, s[68:71], s17 offen offset:0 sc0 sc1 nt
ds_write_b128 v4, v[92:95] offset:64
v_cvt_pk_bf16_f32 v96, v224, v225
v_cvt_pk_bf16_f32 v97, v226, v227
buffer_store_dwordx4 v[148:151], v15, s[68:71], s17 offen offset:1024 sc0 sc1 nt
v_cvt_pk_bf16_f32 v98, v228, v229
v_cvt_pk_bf16_f32 v99, v230, v231
s_cmp_eq_u64 s[30:31], -1
s_cselect_b64 vcc, -1, vcc
ds_write_b128 v4, v[96:99] offset:128
RV 144,1,0
RV 148,2,0
buffer_store_dwordx4 v[100:103], v15, s[68:71], s17 offen offset:2048 sc0 sc1 nt
RV 100,3,0
s_cmp_eq_u64 s[58:59], -1
s_cselect_b64 vcc, -1, vcc
s_waitcnt lgkmcnt(0)
buffer_store_dwordx4 v[144:147], v15, s[68:71], s18 offen offset:0 sc0 sc1 nt
buffer_store_dwordx4 v[148:151], v15, s[68:71], s18 offen offset:1024 sc0 sc1 nt
buffer_store_dwordx4 v[100:103], v15, s[68:71], s18 offen offset:2048 sc0 sc1 nt
s_cbranch_vccnz L_done_%=
s_endpgm
.p2align 6
s_nop 0
T_t120_%=:
s_setpc_b64 s[58:59]
L_sel12_0_%=:
s_cmp_eq_u32 s55, 0
s_cbranch_scc1 L_bodz12_0_%=
s_cmp_eq_u32 s55, 1
s_cbranch_scc1 L_bodz12_1_%=
s_cmp_eq_u32 s55, 4
s_cbranch_scc1 L_tail12_0_%=
s_cmp_eq_u32 s55, 5
s_cbranch_scc1 L_tail12_1_%=
s_cmp_lt_u32 s55, 4
s_cbranch_scc1 L_bodz12_2_%=
s_branch L_tail12_2_%=
L_disp12_0_%=:
M 160,24,88
s_cmp_lg_u32 s55, 4
s_cbranch_scc1 L_sel12_0_%=
L_tail12_0_%=:
RA 0,16,0
v_add_u32 v32, s44, v152
M 164,28,88
RA 4,16,1024
v_add_u32_e64 v36, s44, v153
M 168,32,88
RA 8,16,2048
v_add_u32 v40, s44, v154
M 172,36,88
RA 12,16,3072
s_mul_i32 s11, s23, 192
M 176,40,88
RA 16,16,4096
s_mul_i32 s16, s21, 43008
M 180,44,88
RA 20,16,5120
s_add_u32 s16, s16, s11
M 204,44,92
RV 104,32,0
s_add_u32 s17, s16, 43008
M 200,40,92
RA 68,16,11264
s_add_u32 s18, s16, 86016
M 196,36,92
RA 64,16,10240
v_and_b32 v47, 0xfff, v46
M 192,32,92
RA 60,16,9216
v_add_u32 v12, 162304, v15
M 188,28,92
RA 56,16,8192
s_waitcnt vmcnt(0)
M 184,24,92
buffer_store_dword v31, v30, s[36:39], 0 offen sc1
M 208,24,96
RA 52,16,7168
M 212,28,96
RA 48,16,6144
M 216,32,96
RV 108,32,16
M 220,36,96
RA 72,16,12288
M 224,40,96
RA 76,16,13312
M 228,44,96
RA 80,16,14336
s_waitcnt lgkmcnt(6)
M 160,0,104
RA 84,16,15360
M 164,4,104
RA 88,16,16384
M 168,8,104
RA 92,16,17408
M 172,12,104
RV 112,32,32
M 176,16,104
RV 116,36,0
M 180,20,104
RV 84,12,0
s_waitcnt lgkmcnt(6)
M 180,68,108
RV 120,36,16
M 176,64,108
RV 124,36,32
M 172,60,108
M 168,56,108
RV 128,40,0
M 164,52,108
RV 132,40,16
M 160,48,108
s_waitcnt lgkmcnt(4)
M 160,72,112
RV 136,40,32
v_or3_b32 v84, v84, v85, v86
M 164,76,112
v_or3_b32 v84, v84, v87, v5
v_and_b32 v85, 0x40004001, v84
M 168,80,112
v_and_b32 v86, 0x70007000, v84
v_and_b32_e64 v87, 2, v84
M 172,84,112
v_cvt_pk_bf16_f32 v88, v160, v161
v_cvt_pk_bf16_f32 v89, v162, v163
M 176,88,112
v_cvt_pk_bf16_f32 v90, v164, v165
v_cvt_pk_bf16_f32 v91, v166, v167
M 180,92,112
buffer_store_dwordx4 v[88:91], v47, s[68:71], s16 offen offset:0 sc0 sc1 nt
M 184,0,116
v_cvt_pk_bf16_f32 v92, v168, v169
v_cvt_pk_bf16_f32 v93, v170, v171
M 188,4,116
v_cvt_pk_bf16_f32 v94, v172, v173
v_cvt_pk_bf16_f32 v95, v174, v175
M 192,8,116
v_cvt_pk_bf16_f32 v96, v176, v177
v_cvt_pk_bf16_f32 v97, v178, v179
M 196,12,116
buffer_store_dwordx4 v[92:95], v47, s[68:71], s16 offen offset:64 sc0 sc1 nt
M 200,16,116
v_cvt_pk_bf16_f32 v98, v180, v181
v_cvt_pk_bf16_f32 v99, v182, v183
M 204,20,116
v_cmp_ne_u32 vcc, 0, v85
v_cmp_eq_u32 s[30:31], 0, v86
s_waitcnt lgkmcnt(0)
M 204,68,120
v_cmp_eq_u32 s[58:59], 0, v87
s_cmp_eq_u64 s[30:31], -1
s_cselect_b64 vcc, -1, vcc
M 200,64,120
buffer_store_dwordx4 v[96:99], v47, s[68:71], s16 offen offset:128 sc0 sc1 nt
M 196,60,120
s_cmp_eq_u64 s[58:59], -1
s_cselect_b64 vcc, -1, vcc
M 192,56,120
M 188,52,120
M 184,48,120
M 184,72,124
M 188,76,124
M 192,80,124
M 196,84,124
M 200,88,124
M 204,92,124
M 208,0,128
M 212,4,128
M 216,8,128
v_cvt_pk_bf16_f32 v88, v184, v185
v_cvt_pk_bf16_f32 v89, v186, v187
M 220,12,128
v_cvt_pk_bf16_f32 v90, v188, v189
v_cvt_pk_bf16_f32 v91, v190, v191
M 224,16,128
ds_write_b128 v4, v[88:91] offset:0
M 228,20,128
v_cvt_pk_bf16_f32 v92, v192, v193
v_cvt_pk_bf16_f32 v93, v194, v195
M 228,68,132
v_cvt_pk_bf16_f32 v94, v196, v197
v_cvt_pk_bf16_f32 v95, v198, v199
M 224,64,132
v_cvt_pk_bf16_f32 v96, v200, v201
v_cvt_pk_bf16_f32 v97, v202, v203
M 220,60,132
ds_write_b128 v4, v[92:95] offset:64
M 216,56,132
v_cvt_pk_bf16_f32 v98, v204, v205
v_cvt_pk_bf16_f32 v99, v206, v207
M 212,52,132
M 208,48,132
M 208,72,136
ds_write_b128 v4, v[96:99] offset:128
M 212,76,136
RV 144,1,0
RV 148,2,0
M 216,80,136
RV 100,3,0
M 220,84,136
M 224,88,136
v_cvt_pk_bf16_f32 v88, v208, v209
v_cvt_pk_bf16_f32 v89, v210, v211
M 228,92,136
v_cvt_pk_bf16_f32 v90, v212, v213
v_cvt_pk_bf16_f32 v91, v214, v215
v_cvt_pk_bf16_f32 v92, v216, v217
v_cvt_pk_bf16_f32 v93, v218, v219
ds_write_b128 v4, v[88:91] offset:0
v_cvt_pk_bf16_f32 v94, v220, v221
v_cvt_pk_bf16_f32 v95, v222, v223
v_cvt_pk_bf16_f32 v96, v224, v225
v_cvt_pk_bf16_f32 v97, v226, v227
v_cvt_pk_bf16_f32 v98, v228, v229
s_waitcnt lgkmcnt(1)
buffer_store_dwordx4 v[144:147], v15, s[68:71], s17 offen offset:0 sc0 sc1 nt
ds_write_b128 v4, v[92:95] offset:64
v_cvt_pk_bf16_f32 v99, v230, v231
ds_write_b128 v4, v[96:99] offset:128
RV 144,1,0
buffer_store_dwordx4 v[148:151], v15, s[68:71], s17 offen offset:1024 sc0 sc1 nt
RV 148,2,0
buffer_store_dwordx4 v[100:103], v15, s[68:71], s17 offen offset:2048 sc0 sc1 nt
RV 100,3,0
s_waitcnt lgkmcnt(0)
buffer_store_dwordx4 v[144:147], v15, s[68:71], s18 offen offset:0 sc0 sc1 nt
buffer_store_dwordx4 v[148:151], v15, s[68:71], s18 offen offset:1024 sc0 sc1 nt
buffer_store_dwordx4 v[100:103], v15, s[68:71], s18 offen offset:2048 sc0 sc1 nt
s_cbranch_vccnz L_done_%=
s_endpgm
.p2align 12
.p2align 6
s_nop 0
T_b80_%=:
s_setpc_b64 s[58:59]
L_in8_0_%=:
M 180,44,88
M 204,44,92
M 200,40,92
M 196,36,92
s_add_u32 m0, s41, 24576
M 192,32,92
buffer_load_dwordx4 v15, s[36:39], s40 offen sc1 lds
M 188,28,92
M 184,24,92
s_waitcnt lgkmcnt(0)
M 160,0,72
RV 88,32,144
RV 92,36,144
M 164,4,72
RA 24,20,6144
RA 28,20,7168
M 168,8,72
RA 32,20,8192
RA 36,20,9216
M 172,12,72
RA 40,20,10240
RA 44,20,11264
M 176,16,72
M 180,20,72
buffer_load_dwordx4 v15, s[36:39], s40 offen offset:1024 sc1 lds
M 204,20,76
M 200,16,76
M 196,12,76
M 192,8,76
buffer_load_dwordx4 v15, s[36:39], s40 offen offset:2048 sc1 lds
M 188,4,76
M 184,0,76
s_waitcnt lgkmcnt(0)
M 160,24,88
RV 72,32,160
RV 76,36,160
M 164,28,88
RA 0,20,12288
RA 4,20,13312
M 168,32,88
RA 8,20,14336
RA 12,20,15360
M 172,36,88
RA 16,20,16384
RA 20,20,17408
M 176,40,88
M 180,44,88
buffer_load_dwordx4 v15, s[36:39], s40 offen offset:3072 sc1 lds
M 204,44,92
M 200,40,92
M 196,36,92
s_addk_i32 m0, 4096
M 192,32,92
buffer_load_dwordx4 v45, s[36:39], s40 offen sc1 lds
M 188,28,92
M 184,24,92
s_waitcnt lgkmcnt(0)
M 160,0,72
RV 88,32,176
RV 92,36,176
M 164,4,72
RA 24,20,18432
RA 28,20,19456
M 168,8,72
RA 32,20,20480
RA 36,20,21504
M 172,12,72
RA 40,20,22528
RA 44,20,23552
M 176,16,72
buffer_load_dwordx4 v45, s[36:39], s40 offen offset:1024 sc1 lds
M 180,20,72
s_addk_i32 s40, 24704
M 204,20,76
M 200,16,76
s_lshr_b32 s42, s42, 4
s_add_u32 s43, s43, 8
M 196,12,76
s_and_b32 s55, s42, 15
s_cmp_eq_u32 s55, 0
M 192,8,76
s_waitcnt vmcnt(6) lgkmcnt(0)
M 188,4,76
s_barrier
M 184,0,76
s_cbranch_scc0 L_disp8_0_%=
L_body8_0_%=:
M 160,24,88
L_bodz8_0_%=:
s_set_gpr_idx_on s43, gpr_idx(SRC1)
v_add_u32 v32, s44, v104
v_add_u32 v36, s44, v105
s_set_gpr_idx_off
M 164,28,88
RA 0,16,0
RA 4,16,1024
M 168,32,88
RV 72,32,0
RV 76,36,0
M 172,36,88
RA 8,16,2048
RA 12,16,3072
M 176,40,88
RA 16,16,4096
RA 20,16,5120
M 180,44,88
M 204,44,92
M 200,40,92
M 196,36,92
s_add_u32 m0, s41, 49152
M 192,32,92
buffer_load_dwordx4 v15, s[36:39], s40 offen sc1 lds
M 188,28,92
M 184,24,92
s_waitcnt lgkmcnt(0)
M 160,0,72
RV 88,32,16
RV 92,36,16
M 164,4,72
RA 24,16,6144
RA 28,16,7168
M 168,8,72
RA 32,16,8192
RA 36,16,9216
M 172,12,72
RA 40,16,10240
RA 44,16,11264
M 176,16,72
M 180,20,72
buffer_load_dwordx4 v15, s[36:39], s40 offen offset:1024 sc1 lds
M 204,20,76
M 200,16,76
M 196,12,76
M 192,8,76
buffer_load_dwordx4 v15, s[36:39], s40 offen offset:2048 sc1 lds
M 188,4,76
M 184,0,76
s_waitcnt lgkmcnt(0)
M 160,24,88
RV 72,32,32
RV 76,36,32
M 164,28,88
RA 0,16,12288
RA 4,16,13312
M 168,32,88
RA 8,16,14336
RA 12,16,15360
M 172,36,88
RA 16,16,16384
RA 20,16,17408
M 176,40,88
M 180,44,88
buffer_load_dwordx4 v15, s[36:39], s40 offen offset:3072 sc1 lds
M 204,44,92
M 200,40,92
M 196,36,92
s_addk_i32 m0, 4096
M 192,32,92
buffer_load_dwordx4 v45, s[36:39], s40 offen sc1 lds
M 188,28,92
M 184,24,92
s_waitcnt lgkmcnt(0)
M 160,0,72
RV 88,32,48
RV 92,36,48
M 164,4,72
RA 24,16,18432
RA 28,16,19456
M 168,8,72
RA 32,16,20480
RA 36,16,21504
M 172,12,72
RA 40,16,22528
RA 44,16,23552
M 176,16,72
M 180,20,72
buffer_load_dwordx4 v45, s[36:39], s40 offen offset:1024 sc1 lds
M 204,20,76
M 200,16,76
M 196,12,76
s_add_u32 s40, s40, 24704
M 192,8,76
s_waitcnt vmcnt(6) lgkmcnt(0)
M 188,4,76
s_barrier
M 184,0,76
M 160,24,88
RV 72,32,64
RV 76,36,64
M 164,28,88
RA 0,16,24576
RA 4,16,25600
M 168,32,88
RA 8,16,26624
RA 12,16,27648
M 172,36,88
RA 16,16,28672
RA 20,16,29696
M 176,40,88
M 180,44,88
M 204,44,92
M 200,40,92
M 196,36,92
s_mov_b32 m0, s41
M 192,32,92
buffer_load_dwordx4 v15, s[36:39], s40 offen sc1 lds
M 188,28,92
M 184,24,92
s_waitcnt lgkmcnt(0)
M 160,0,72
RV 88,32,80
RV 92,36,80
M 164,4,72
RA 24,16,30720
RA 28,16,31744
M 168,8,72
RA 32,16,32768
RA 36,16,33792
M 172,12,72
RA 40,16,34816
RA 44,16,35840
M 176,16,72
M 180,20,72
buffer_load_dwordx4 v15, s[36:39], s40 offen offset:1024 sc1 lds
M 204,20,76
M 200,16,76
M 196,12,76
M 192,8,76
buffer_load_dwordx4 v15, s[36:39], s40 offen offset:2048 sc1 lds
M 188,4,76
M 184,0,76
s_waitcnt lgkmcnt(0)
M 160,24,88
RV 72,32,96
RV 76,36,96
M 164,28,88
RA 0,16,36864
RA 4,16,37888
M 168,32,88
RA 8,16,38912
RA 12,16,39936
M 172,36,88
RA 16,16,40960
RA 20,16,41984
M 176,40,88
M 180,44,88
buffer_load_dwordx4 v15, s[36:39], s40 offen offset:3072 sc1 lds
M 204,44,92
M 200,40,92
M 196,36,92
s_add_u32 m0, m0, 4096
M 192,32,92
buffer_load_dwordx4 v45, s[36:39], s40 offen sc1 lds
M 188,28,92
M 184,24,92
s_waitcnt lgkmcnt(0)
M 160,0,72
RV 88,32,112
RV 92,36,112
M 164,4,72
RA 24,16,43008
RA 28,16,44032
M 168,8,72
RA 32,16,45056
RA 36,16,46080
M 172,12,72
RA 40,16,47104
RA 44,16,48128
M 176,16,72
M 180,20,72
buffer_load_dwordx4 v45, s[36:39], s40 offen offset:1024 sc1 lds
M 204,20,76
M 200,16,76
M 196,12,76
s_add_u32 s40, s40, 24704
M 192,8,76
s_waitcnt vmcnt(6) lgkmcnt(0)
M 188,4,76
s_barrier
M 184,0,76
M 160,24,88
RV 72,32,128
RV 76,36,128
M 164,28,88
RA 0,20,0
RA 4,20,1024
M 168,32,88
RA 8,20,2048
RA 12,20,3072
M 172,36,88
RA 16,20,4096
RA 20,20,5120
M 176,40,88
s_branch L_in8_0_%=
.p2align 12
.p2align 6
s_nop 0
T_a82_%=:
s_setpc_b64 s[58:59]
L_body8_2_%=:
M 160,24,88
L_bodz8_2_%=:
RA 0,16,0
RA 4,16,1024
s_set_gpr_idx_on s43, gpr_idx(SRC1)
v_add_u32 v32, s44, v104
v_add_u32 v36, s44, v105
v_add_u32 v6, 0, v108
v_add_u32 v7, 0, v109
s_set_gpr_idx_off
M 164,28,88
RA 8,16,2048
v_pk_max_i16 v25, v6, s50
v_pk_min_i16 v25, v25, s52
v_mad_i32_i16 v33, v25, s54, v32 op_sel:[1,0,0,0]
v_mad_i32_i16 v34, v25, s53, v32
v_mad_i32_i16 v35, v25, s53, v33
M 168,32,88
RA 12,16,3072
v_pk_max_i16 v27, v7, s50
v_pk_min_i16 v27, v27, s52
v_mad_i32_i16 v37, v27, s54, v36 op_sel:[1,0,0,0]
v_mad_i32_i16 v38, v27, s53, v36
v_mad_i32_i16 v39, v27, s53, v37
M 172,36,88
RV 48,32,0
RV 52,33,0
v_and_b32_e64 v6, s56, v6
v_pk_add_f16 v25, v6, s51 op_sel:[1,0] neg_lo:[1,0]
M 176,40,88
RV 56,34,0
RV 60,35,0
v_pk_add_f16 v6, v6, s51 op_sel_hi:[0,1] neg_lo:[1,0]
PM 24,25,6
M 180,44,88
RV 208,36,0
RV 212,37,0
v_pk_mul_f16 v25, v25, v6 op_sel:[0,1]
v_and_b32 v7, s56, v7
RV 216,38,0
RV 220,39,0
v_pk_add_f16 v27, v7, s51 op_sel:[1,0] neg_lo:[1,0]
v_pk_add_f16 v7, v7, s51 op_sel_hi:[0,1] neg_lo:[1,0]
M 204,44,92
RA 16,16,4096
RA 20,16,5120
PM 26,27,7
v_pk_mul_f16 v27, v27, v7 op_sel:[0,1]
M 200,40,92
RV 224,32,16
RV 228,33,16
RV 232,34,16
RV 236,35,16
RV 240,36,16
s_waitcnt lgkmcnt(11)
PM 72,48,24
PM 73,49,24
PM 74,50,24
M 196,36,92
RV 244,37,16
PM 75,51,24
PH 72,52,24
PH 73,53,24
M 192,32,92
s_add_u32 m0, s41, 49152
RV 248,38,16
PH 74,54,24
PH 75,55,24
PL 72,56,25
RV 252,39,16
PL 73,57,25
PL 74,58,25
PL 75,59,25
M 188,28,92
buffer_load_dwordx4 v15, s[36:39], s40 offen sc1 lds
PH 72,60,25
PH 73,61,25
PH 74,62,25
PH 75,63,25
s_waitcnt lgkmcnt(10)
PM 76,208,26
PM 77,209,26
PM 78,210,26
PM 79,211,26
PH 76,212,26
M 184,24,92
PH 77,213,26
PH 78,214,26
PH 79,215,26
PL 76,216,27
PL 77,217,27
PL 78,218,27
PL 79,219,27
PH 76,220,27
PH 77,221,27
PH 78,222,27
PH 79,223,27
M 160,0,72
RA 24,16,6144
buffer_load_dwordx4 v15, s[36:39], s40 offen offset:1024 sc1 lds
s_waitcnt lgkmcnt(4)
PM 88,224,24
PM 89,225,24
M 164,4,72
RA 28,16,7168
RA 32,16,8192
PM 90,226,24
PM 91,227,24
M 168,8,72
RA 36,16,9216
RA 40,16,10240
PH 88,228,24
PH 89,229,24
M 172,12,72
RA 44,16,11264
RV 48,32,32
PH 90,230,24
PH 91,231,24
M 176,16,72
RV 52,33,32
PL 88,232,25
PL 89,233,25
PL 90,234,25
M 180,20,72
buffer_load_dwordx4 v15, s[36:39], s40 offen offset:2048 sc1 lds
RV 56,34,32
PL 91,235,25
PH 88,236,25
M 204,20,76
RV 60,35,32
RV 208,36,32
PH 89,237,25
PH 90,238,25
PH 91,239,25
M 200,16,76
RV 212,37,32
RV 216,38,32
s_waitcnt lgkmcnt(8)
PM 92,240,26
PM 93,241,26
M 196,12,76
RV 220,39,32
PM 94,242,26
PM 95,243,26
PH 92,244,26
M 192,8,76
PH 93,245,26
PH 94,246,26
PH 95,247,26
PL 92,248,27
M 188,4,76
PL 93,249,27
PL 94,250,27
PL 95,251,27
PH 92,252,27
M 184,0,76
PH 93,253,27
PH 94,254,27
PH 95,255,27
M 160,24,88
RA 0,16,12288
buffer_load_dwordx4 v15, s[36:39], s40 offen offset:3072 sc1 lds
s_waitcnt lgkmcnt(2)
PM 72,48,24
PM 73,49,24
M 164,28,88
RA 4,16,13312
RA 8,16,14336
PM 74,50,24
PM 75,51,24
M 168,32,88
RA 12,16,15360
RA 16,16,16384
PH 72,52,24
PH 73,53,24
M 172,36,88
RA 20,16,17408
PH 74,54,24
PH 75,55,24
PL 72,56,25
M 176,40,88
s_add_u32 m0, m0, 4096
RV 224,32,48
RV 228,33,48
PL 73,57,25
PL 74,58,25
M 180,44,88
RV 232,34,48
buffer_load_dwordx4 v45, s[36:39], s40 offen sc1 lds
PL 75,59,25
PH 72,60,25
M 204,44,92
RV 236,35,48
RV 240,36,48
PH 73,61,25
PH 74,62,25
M 200,40,92
RV 244,37,48
RV 248,38,48
PH 75,63,25
s_waitcnt lgkmcnt(8)
PM 76,208,26
M 196,36,92
RV 252,39,48
PM 77,209,26
PM 78,210,26
PM 79,211,26
M 192,32,92
PH 76,212,26
PH 77,213,26
PH 78,214,26
PH 79,215,26
M 188,28,92
PL 76,216,27
PL 77,217,27
PL 78,218,27
PL 79,219,27
M 184,24,92
PH 76,220,27
PH 77,221,27
PH 78,222,27
PH 79,223,27
M 160,0,72
RA 24,16,18432
buffer_load_dwordx4 v45, s[36:39], s40 offen offset:1024 sc1 lds
s_waitcnt lgkmcnt(2)
PM 88,224,24
PM 89,225,24
M 164,4,72
RA 28,16,19456
RA 32,16,20480
PM 90,226,24
PM 91,227,24
M 168,8,72
RA 36,16,21504
RA 40,16,22528
PH 88,228,24
PH 89,229,24
M 172,12,72
RA 44,16,23552
RV 48,32,64
PH 90,230,24
PH 91,231,24
PL 88,232,25
M 176,16,72
RV 52,33,64
PL 89,233,25
PL 90,234,25
PL 91,235,25
M 180,20,72
RV 56,34,64
PH 88,236,25
PH 89,237,25
M 204,20,76
RV 60,35,64
RV 208,36,64
PH 90,238,25
PH 91,239,25
M 200,16,76
RV 212,37,64
RV 216,38,64
s_waitcnt lgkmcnt(8)
PM 92,240,26
PM 93,241,26
M 196,12,76
s_addk_i32 s40, 24704
RV 220,39,64
PM 94,242,26
PM 95,243,26
PH 92,244,26
M 192,8,76
s_waitcnt vmcnt(6) lgkmcnt(6)
PH 93,245,26
PH 94,246,26
PH 95,247,26
M 188,4,76
s_barrier
PL 92,248,27
PL 93,249,27
PL 94,250,27
M 184,0,76
PL 95,251,27
PH 92,252,27
PH 93,253,27
PH 94,254,27
PH 95,255,27
M 160,24,88
s_branch L_xa82_%=
.p2align 12
.p2align 6
s_nop 0
T_t82_%=:
s_setpc_b64 s[58:59]
L_sel8_2_%=:
s_cmp_eq_u32 s55, 0
s_cbranch_scc1 L_bodz8_0_%=
s_cmp_eq_u32 s55, 1
s_cbranch_scc1 L_bodz8_1_%=
s_cmp_eq_u32 s55, 4
s_cbranch_scc1 L_tail8_0_%=
s_cmp_eq_u32 s55, 5
s_cbranch_scc1 L_tail8_1_%=
s_cmp_lt_u32 s55, 4
s_cbranch_scc1 L_bodz8_2_%=
s_branch L_tail8_2_%=
L_disp8_2_%=:
M 160,24,88
s_cmp_lg_u32 s55, 7
s_cbranch_scc1 L_sel8_2_%=
L_tail8_2_%=:
RA 0,16,0
RA 4,16,1024
v_add_u32 v32, s44, v152
v_mov_b32_e64 v6, v156
v_pk_max_i16 v25, v6, s50
M 164,28,88
RA 8,16,2048
RA 12,16,3072
v_pk_min_i16 v25, v25, s52
v_mad_i32_i16 v33, v25, s54, v32 op_sel:[1,0,0,0]
v_mad_i32_i16 v34, v25, s53, v32
v_mad_i32_i16 v35, v25, s53, v33
M 168,32,88
RA 16,16,4096
RA 20,16,5120
v_add_u32 v36, s44, v153
v_mov_b32 v7, v157
v_pk_max_i16 v27, v7, s50
v_pk_min_i16 v27, v27, s52
M 172,36,88
RV 104,32,0
RV 48,33,0
v_mad_i32_i16 v37, v27, s54, v36 op_sel:[1,0,0,0]
v_mad_i32_i16 v38, v27, s53, v36
v_mad_i32_i16 v39, v27, s53, v37
v_and_b32_e64 v6, s56, v6
RV 52,34,0
RV 56,35,0
M 176,40,88
RV 108,32,16
RV 60,33,16
v_pk_add_f16 v25, v6, s51 op_sel:[1,0] neg_lo:[1,0]
v_pk_add_f16 v6, v6, s51 op_sel_hi:[0,1] neg_lo:[1,0]
M 180,44,88
RV 64,34,16
RV 68,35,16
PM 24,25,6
v_pk_mul_f16 v25, v25, v6 op_sel:[0,1]
v_and_b32 v7, s56, v7
v_pk_add_f16 v27, v7, s51 op_sel:[1,0] neg_lo:[1,0]
M 204,44,92
RA 68,16,11264
RA 64,16,10240
v_pk_add_f16 v7, v7, s51 op_sel_hi:[0,1] neg_lo:[1,0]
PM 26,27,7
v_pk_mul_f16 v27, v27, v7 op_sel:[0,1]
s_mul_i32 s11, s23, 192
M 200,40,92
RA 60,16,9216
RA 56,16,8192
s_waitcnt lgkmcnt(8)
PM 104,104,24
PM 105,105,24
PM 106,106,24
PM 107,107,24
M 196,36,92
RA 52,16,7168
RA 48,16,6144
PH 104,48,24
PH 105,49,24
PH 106,50,24
PH 107,51,24
M 192,32,92
RV 112,32,32
RV 72,33,32
PL 104,52,25
PL 105,53,25
PL 106,54,25
PL 107,55,25
PH 104,56,25
PH 105,57,25
PH 106,58,25
PH 107,59,25
M 188,28,92
RV 76,34,32
RV 80,35,32
M 184,24,92
RA 72,16,12288
RA 76,16,13312
s_waitcnt lgkmcnt(6)
PM 108,108,24
PM 109,109,24
PM 110,110,24
PM 111,111,24
M 160,0,104
RA 80,16,14336
RA 84,16,15360
PH 108,60,24
PH 109,61,24
PH 110,62,24
M 164,4,104
RA 88,16,16384
RA 92,16,17408
PH 111,63,24
PL 108,64,25
M 168,8,104
RV 116,36,0
RV 48,37,0
PL 109,65,25
PL 110,66,25
PL 111,67,25
M 172,12,104
RV 52,38,0
RV 56,39,0
PH 108,68,25
PH 109,69,25
PH 110,70,25
PH 111,71,25
M 176,16,104
RV 120,36,16
RV 60,37,16
s_waitcnt lgkmcnt(8)
PM 112,112,24
M 180,20,104
RV 64,38,16
RV 68,39,16
PM 113,113,24
PM 114,114,24
M 180,68,108
PM 115,115,24
PH 112,72,24
PH 113,73,24
PH 114,74,24
PH 115,75,24
s_mul_i32 s16, s21, 43008
s_add_u32 s16, s16, s11
s_add_u32 s17, s16, 43008
s_add_u32 s18, s16, 86016
v_and_b32 v47, 0xfff, v46
v_add_u32 v12, 162304, v15
s_waitcnt vmcnt(0)
M 176,64,108
M 172,60,108
buffer_store_dword v31, v30, s[36:39], 0 offen sc1
M 168,56,108
RV 84,12,0
PL 112,76,25
PL 113,77,25
PL 114,78,25
PL 115,79,25
PH 112,80,25
PH 113,81,25
PH 114,82,25
PH 115,83,25
M 164,52,108
M 160,48,108
RV 124,36,32
RV 72,37,32
s_waitcnt lgkmcnt(3)
PM 116,116,26
PM 117,117,26
M 160,72,112
RV 76,38,32
RV 80,39,32
PM 118,118,26
PM 119,119,26
PH 116,48,26
M 164,76,112
PH 117,49,26
PH 118,50,26
PH 119,51,26
PL 116,52,27
M 168,80,112
PL 117,53,27
PL 118,54,27
PL 119,55,27
PH 116,56,27
PH 117,57,27
M 172,84,112
PH 118,58,27
PH 119,59,27
PM 120,120,26
PM 121,121,26
M 176,88,112
PM 122,122,26
PM 123,123,26
PH 120,60,26
PH 121,61,26
PH 122,62,26
M 180,92,112
PH 123,63,26
PL 120,64,27
PL 121,65,27
PL 122,66,27
M 184,0,116
PL 123,67,27
PH 120,68,27
PH 121,69,27
PH 122,70,27
PH 123,71,27
M 188,4,116
s_waitcnt lgkmcnt(0)
PM 124,124,26
PM 125,125,26
PM 126,126,26
PM 127,127,26
M 192,8,116
PH 124,72,26
PH 125,73,26
PH 126,74,26
PH 127,75,26
PL 124,76,27
M 196,12,116
PL 125,77,27
PL 126,78,27
PL 127,79,27
PH 124,80,27
M 200,16,116
PH 125,81,27
PH 126,82,27
PH 127,83,27
v_cvt_pk_bf16_f32 v88, v160, v161
v_cvt_pk_bf16_f32 v89, v162, v163
M 204,20,116
v_cvt_pk_bf16_f32 v90, v164, v165
v_cvt_pk_bf16_f32 v91, v166, v167
v_cvt_pk_bf16_f32 v92, v168, v169
v_cvt_pk_bf16_f32 v93, v170, v171
M 204,68,120
buffer_store_dwordx4 v[88:91], v47, s[68:71], s16 offen offset:0 sc0 sc1 nt
v_cvt_pk_bf16_f32 v94, v172, v173
v_cvt_pk_bf16_f32 v95, v174, v175
v_cvt_pk_bf16_f32 v96, v176, v177
M 200,64,120
v_cvt_pk_bf16_f32 v97, v178, v179
v_cvt_pk_bf16_f32 v98, v180, v181
v_cvt_pk_bf16_f32 v99, v182, v183
v_or3_b32 v84, v84, v85, v86
M 196,60,120
v_or3_b32 v84, v84, v87, v5
v_and_b32 v85, 0x40004001, v84
v_and_b32 v86, 0x70007000, v84
v_and_b32 v87, 2, v84
v_cmp_ne_u32 vcc, 0, v85
M 192,56,120
buffer_store_dwordx4 v[92:95], v47, s[68:71], s16 offen offset:64 sc0 sc1 nt
v_cmp_eq_u32 s[30:31], 0, v86
v_cmp_eq_u32 s[58:59], 0, v87
M 188,52,120
s_cmp_eq_u64 s[30:31], -1
s_cselect_b64 vcc, -1, vcc
s_cmp_eq_u64 s[58:59], -1
s_cselect_b64 vcc, -1, vcc
M 184,48,120
M 184,72,124
M 188,76,124
buffer_store_dwordx4 v[96:99], v47, s[68:71], s16 offen offset:128 sc0 sc1 nt
M 192,80,124
M 196,84,124
M 200,88,124
M 204,92,124
s_nop 0
s_nop 0
v_cvt_pk_bf16_f32 v88, v184, v185
v_cvt_pk_bf16_f32 v89, v186, v187
v_cvt_pk_bf16_f32 v90, v188, v189
v_cvt_pk_bf16_f32 v91, v190, v191
v_cvt_pk_bf16_f32 v92, v192, v193
v_cvt_pk_bf16_f32 v93, v194, v195
ds_write_b128 v4, v[88:91] offset:0
v_cvt_pk_bf16_f32 v94, v196, v197
v_cvt_pk_bf16_f32 v95, v198, v199
v_cvt_pk_bf16_f32 v96, v200, v201
v_cvt_pk_bf16_f32 v97, v202, v203
v_cvt_pk_bf16_f32 v98, v204, v205
v_cvt_pk_bf16_f32 v99, v206, v207
ds_write_b128 v4, v[92:95] offset:64
ds_write_b128 v4, v[96:99] offset:128
RV 144,1,0
RV 148,2,0
RV 100,3,0
s_waitcnt lgkmcnt(0)
buffer_store_dwordx4 v[144:147], v15, s[68:71], s17 offen offset:0 sc0 sc1 nt
buffer_store_dwordx4 v[148:151], v15, s[68:71], s17 offen offset:1024 sc0 sc1 nt
buffer_store_dwordx4 v[100:103], v15, s[68:71], s17 offen offset:2048 sc0 sc1 nt
s_cbranch_vccnz L_done_%=
s_endpgm
.p2align 12
.p2align 6
s_nop 0
T_e120_%=:
s_setpc_b64 s[58:59]
L_ent12_0_%=:
s_set_gpr_idx_on s43, gpr_idx(SRC1)
v_add_u32 v32, s44, v104
v_add_u32 v36, s44, v105
v_add_u32 v40, s44, v106
s_set_gpr_idx_off
s_waitcnt lgkmcnt(0)
s_barrier
RA 0,16,0
RA 4,16,1024
RV 72,32,0
RV 76,36,0
RV 80,40,0
RA 8,16,2048
RA 12,16,3072
RA 16,16,4096
RA 20,16,5120
s_waitcnt lgkmcnt(0)
s_nop 0
s_waitcnt lgkmcnt(0)
v_mfma_f32_16x16x32_f16 v[160:163], a[0:3], v[72:75], 0
s_add_u32 m0, s41, 24576
RV 88,32,16
v_mfma_f32_16x16x32_f16 v[164:167], a[4:7], v[72:75], 0
buffer_load_dwordx4 v15, s[36:39], s40 offen sc1 lds
RV 92,36,16
v_mfma_f32_16x16x32_f16 v[168:171], a[8:11], v[72:75], 0
RV 96,40,16
v_mfma_f32_16x16x32_f16 v[172:175], a[12:15], v[72:75], 0
buffer_load_dwordx4 v15, s[36:39], s40 offen offset:1024 sc1 lds
RA 24,16,6144
v_mfma_f32_16x16x32_f16 v[176:179], a[16:19], v[72:75], 0
RA 28,16,7168
v_mfma_f32_16x16x32_f16 v[180:183], a[20:23], v[72:75], 0
buffer_load_dwordx4 v15, s[36:39], s40 offen offset:2048 sc1 lds
RA 32,16,8192
v_mfma_f32_16x16x32_f16 v[204:207], a[20:23], v[76:79], 0
RA 36,16,9216
v_mfma_f32_16x16x32_f16 v[200:203], a[16:19], v[76:79], 0
RA 40,16,10240
v_mfma_f32_16x16x32_f16 v[196:199], a[12:15], v[76:79], 0
buffer_load_dwordx4 v15, s[36:39], s40 offen offset:3072 sc1 lds
RA 44,16,11264
v_mfma_f32_16x16x32_f16 v[192:195], a[8:11], v[76:79], 0
s_add_u32 m0, m0, 4096
v_mfma_f32_16x16x32_f16 v[188:191], a[4:7], v[76:79], 0
buffer_load_dwordx4 v45, s[36:39], s40 offen sc1 lds
v_mfma_f32_16x16x32_f16 v[184:187], a[0:3], v[76:79], 0
v_mfma_f32_16x16x32_f16 v[208:211], a[0:3], v[80:83], 0
buffer_load_dwordx4 v45, s[36:39], s40 offen offset:1024 sc1 lds
v_mfma_f32_16x16x32_f16 v[212:215], a[4:7], v[80:83], 0
s_addk_i32 s40, 24704
v_mfma_f32_16x16x32_f16 v[216:219], a[8:11], v[80:83], 0
v_mfma_f32_16x16x32_f16 v[220:223], a[12:15], v[80:83], 0
v_mfma_f32_16x16x32_f16 v[224:227], a[16:19], v[80:83], 0
v_mfma_f32_16x16x32_f16 v[228:231], a[20:23], v[80:83], 0
s_waitcnt lgkmcnt(0)
M 160,24,88
RV 72,32,32
M 164,28,88
RV 76,36,32
M 168,32,88
RV 80,40,32
M 172,36,88
RA 0,16,12288
M 176,40,88
RA 4,16,13312
M 180,44,88
RA 8,16,14336
M 204,44,92
RA 12,16,15360
M 200,40,92
RA 16,16,16384
M 196,36,92
RA 20,16,17408
M 192,32,92
M 188,28,92
M 184,24,92
M 208,24,96
M 212,28,96
M 216,32,96
M 220,36,96
M 224,40,96
M 228,44,96
s_waitcnt lgkmcnt(0)
M 160,0,72
RV 88,32,48
M 164,4,72
RV 92,36,48
M 168,8,72
RV 96,40,48
M 172,12,72
RA 24,16,18432
M 176,16,72
RA 28,16,19456
M 180,20,72
RA 32,16,20480
M 204,20,76
RA 36,16,21504
M 200,16,76
RA 40,16,22528
M 196,12,76
RA 44,16,23552
M 192,8,76
M 188,4,76
v_cmp_nge_f32 vcc, 1.0, v12
M 184,0,76
v_and_b32 v13, 0x7ff87ff8, v13
M 208,0,80
s_lshl2_add_u32 s11, s10, 162304
M 212,4,80
v_addc_co_u32 v13, vcc, 0, v13, vcc
M 216,8,80
buffer_load_dwordx4 v[6:9], v15, s[36:39], 0 offen sc1
v_add_u32 v12, s11, v15
M 220,12,80
s_waitcnt vmcnt(1) lgkmcnt(0)
M 224,16,80
buffer_load_dwordx4 v[100:103], v47, s[36:39], 0 offen offset:1024 sc1
s_barrier
M 228,20,80
M 160,24,88
RV 72,32,64
M 164,28,88
ds_write_b32 v12, v13
RV 76,36,64
M 168,32,88
RV 80,40,64
M 172,36,88
RA 0,16,24576
M 176,40,88
RA 4,16,25600
M 180,44,88
RA 8,16,26624
M 204,44,92
RA 12,16,27648
M 200,40,92
RA 16,16,28672
M 196,36,92
RA 20,16,29696
M 192,32,92
M 188,28,92
M 184,24,92
M 208,24,96
M 212,28,96
M 216,32,96
M 220,36,96
M 224,40,96
M 228,44,96
s_waitcnt lgkmcnt(0)
M 160,0,72
RV 88,32,80
s_waitcnt vmcnt(0)
v_bitop3_b32 v6, v6, v8, s66 bitop3:0x96
M 164,4,72
RV 92,36,80
v_bitop3_b32 v100, v100, v102, s66 bitop3:0x96
v_bitop3_b32 v7, v7, v9, s67 bitop3:0x96
M 168,8,72
RV 96,40,80
v_bitop3_b32 v101, v101, v103, s67 bitop3:0x96
v_or_b32_e64 v6, v6, v100
M 172,12,72
RA 24,16,30720
v_or_b32 v5, v7, v101
v_cmp_ne_u32 vcc, 0, v6
M 176,16,72
RA 28,16,31744
v_cmp_lt_u32 s[30:31], 7, v5
s_or_b64 vcc, vcc, s[30:31]
M 180,20,72
RA 32,16,32768
s_cbranch_scc1 L_ag12_0_%=
L_ok12_0_%=:
M 204,20,76
s_add_u32 m0, s41, 49152
RA 36,16,33792
M 200,16,76
buffer_load_dwordx4 v15, s[36:39], s40 offen sc1 lds
RA 40,16,34816
M 196,12,76
RA 44,16,35840
M 192,8,76
buffer_load_dwordx4 v15, s[36:39], s40 offen offset:1024 sc1 lds
M 188,4,76
M 184,0,76
buffer_load_dwordx4 v15, s[36:39], s40 offen offset:2048 sc1 lds
M 208,0,80
M 212,4,80
M 216,8,80
buffer_load_dwordx4 v15, s[36:39], s40 offen offset:3072 sc1 lds
M 220,12,80
s_addk_i32 m0, 4096
M 224,16,80
buffer_load_dwordx4 v45, s[36:39], s40 offen sc1 lds
M 228,20,80
s_waitcnt lgkmcnt(0)
M 160,24,88
buffer_load_dwordx4 v45, s[36:39], s40 offen offset:1024 sc1 lds
RV 72,32,96
M 164,28,88
s_addk_i32 s40, 24704
RV 76,36,96
M 168,32,88
RV 80,40,96
M 172,36,88
RA 0,16,36864
M 176,40,88
s_add_u32 m0, s41, 0
RA 4,16,37888
M 180,44,88
buffer_load_dwordx4 v15, s[36:39], s40 offen sc1 lds
RA 8,16,38912
M 204,44,92
RA 12,16,39936
M 200,40,92
RA 16,16,40960
M 196,36,92
RA 20,16,41984
M 192,32,92
buffer_load_dwordx4 v15, s[36:39], s40 offen offset:1024 sc1 lds
M 188,28,92
M 184,24,92
M 208,24,96
M 212,28,96
M 216,32,96
buffer_load_dwordx4 v15, s[36:39], s40 offen offset:2048 sc1 lds
M 220,36,96
M 224,40,96
M 228,44,96
s_waitcnt lgkmcnt(0)
M 160,0,72
buffer_load_dwordx4 v15, s[36:39], s40 offen offset:3072 sc1 lds
RV 88,32,112
M 164,4,72
s_addk_i32 m0, 4096
RV 92,36,112
M 168,8,72
RV 96,40,112
M 172,12,72
RA 24,16,43008
M 176,16,72
RA 28,16,44032
M 180,20,72
buffer_load_dwordx4 v45, s[36:39], s40 offen sc1 lds
RA 32,16,45056
M 204,20,76
RA 36,16,46080
M 200,16,76
RA 40,16,47104
M 196,12,76
RA 44,16,48128
M 192,8,76
buffer_load_dwordx4 v45, s[36:39], s40 offen offset:1024 sc1 lds
M 188,4,76
s_add_u32 s40, s40, 24704
M 184,0,76
M 208,0,80
M 212,4,80
M 216,8,80
M 220,12,80
s_waitcnt vmcnt(6) lgkmcnt(0)
M 224,16,80
s_barrier
M 228,20,80
M 160,24,88
RV 72,32,128
M 164,28,88
RV 76,36,128
M 168,32,88
RV 80,40,128
M 172,36,88
RA 0,20,0
M 176,40,88
RA 4,20,1024
M 180,44,88
RA 8,20,2048
M 204,44,92
RA 12,20,3072
M 200,40,92
RA 16,20,4096
M 196,36,92
RA 20,20,5120
M 192,32,92
s_branch L_in12_0_%=
L_ag12_0_%=:
s_call_b64 s[72:73], L_retry_%=
s_branch L_ok12_0_%=
.p2align 12
.p2align 6
s_nop 0
T_t130_%=:
s_setpc_b64 s[58:59]
L_sel13_0_%=:
s_cmp_eq_u32 s55, 0
s_cbranch_scc1 L_bodz13_0_%=
s_cmp_eq_u32 s55, 1
s_cbranch_scc1 L_bodz13_1_%=
s_cmp_eq_u32 s55, 4
s_cbranch_scc1 L_tail13_0_%=
s_cmp_eq_u32 s55, 5
s_cbranch_scc1 L_tail13_1_%=
s_cmp_lt_u32 s55, 4
s_cbranch_scc1 L_bodz13_2_%=
s_branch L_tail13_2_%=
L_disp13_0_%=:
M 160,24,88
s_cmp_lg_u32 s55, 4
s_cbranch_scc1 L_sel13_0_%=
L_tail13_0_%=:
s_mul_i32 s63, s25, 6144
v_add_u32 v16, s63, v14
M 164,28,88
RA 0,16,0
s_mul_i32 s64, s61, 384
M 168,32,88
v_add_u32_e64 v17, s64, v16
s_mul_i32 s65, s62, 384
M 172,36,88
RA 4,16,1024
v_add_u32_e64 v18, s65, v16
M 176,40,88
RA 8,16,2048
v_add_u32 v44, s60, v155
v_add_u32 v32, s60, v152
M 180,44,88
RA 12,16,3072
v_add_u32_e64 v36, s60, v153
M 204,44,92
RA 16,16,4096
v_add_u32_e64 v40, s60, v154
M 200,40,92
RA 20,16,5120
v_add_u32_e64 v33, s61, v32
M 196,36,92
RV 104,44,0
v_add_u32_e64 v37, s61, v36
M 192,32,92
RV 108,32,0
v_add_u32 v41, s61, v40
v_add_u32 v34, s62, v32
M 188,28,92
RA 68,17,11264
v_add_u32_e64 v38, s62, v36
M 184,24,92
v_add_u32 v42, s62, v40
s_mul_i32 s11, s23, 192
M 208,24,96
RA 64,17,10240
s_mul_i32 s16, s21, 43008
M 212,28,96
RA 60,17,9216
s_add_u32 s16, s16, s11
M 216,32,96
s_add_u32 s17, s16, 43008
s_add_u32 s18, s16, 86016
s_mul_i32 s19, s22, 43008
M 220,36,96
RA 56,17,8192
s_add_u32 s19, s19, s11
M 224,40,96
RA 52,17,7168
s_lshl_b32 s11, s10, 6
M 228,44,96
RA 48,17,6144
s_add_u32 s19, s19, s11
s_waitcnt lgkmcnt(5)
M 232,0,104
RV 112,33,16
s_lshl_b32 s11, s25, 11
M 236,4,104
RA 72,18,12288
v_add_u32 v21, s11, v14
v_add_u32_e64 v20, s41, v15
M 240,8,104
v_and_b32 v47, 0xfff, v46
v_add_u32 v12, 162304, v15
M 244,12,104
RA 76,18,13312
s_waitcnt vmcnt(0)
M 248,16,104
ds_write_b128 v20, v[232:235] offset:24576
M 252,20,104
RA 80,18,14336
M 160,0,108
RA 84,18,15360
M 164,4,108
ds_write_b128 v20, v[236:239] offset:25600
M 168,8,108
RA 88,18,16384
M 172,12,108
RA 92,18,17408
M 176,16,108
ds_write_b128 v20, v[240:243] offset:26624
M 180,20,108
RV 116,34,32
s_waitcnt lgkmcnt(5)
M 180,68,112
RV 120,36,0
M 176,64,112
ds_write_b128 v20, v[244:247] offset:27648
M 172,60,112
RV 84,12,0
M 168,56,112
RV 124,37,16
M 164,52,112
ds_write_b128 v20, v[248:251] offset:28672
M 160,48,112
s_waitcnt lgkmcnt(4)
M 160,72,116
RV 128,38,32
M 164,76,116
ds_write_b128 v20, v[252:255] offset:29696
s_waitcnt lgkmcnt(3)
v_or3_b32 v84, v84, v85, v86
M 168,80,116
v_or3_b32 v84, v84, v87, v5
v_and_b32 v85, 0x40004001, v84
M 172,84,116
RV 132,40,0
v_cvt_pk_bf16_f32 v88, v160, v161
M 176,88,116
RV 136,41,16
v_cvt_pk_bf16_f32 v89, v162, v163
M 180,92,116
v_cvt_pk_bf16_f32 v90, v164, v165
v_cvt_pk_bf16_f32 v91, v166, v167
v_cvt_pk_bf16_f32 v92, v168, v169
M 184,0,120
buffer_store_dwordx4 v[88:91], v47, s[68:71], s16 offen offset:0 sc0 sc1 nt
M 188,4,120
RV 140,42,32
v_cvt_pk_bf16_f32 v93, v170, v171
M 192,8,120
v_cvt_pk_bf16_f32 v94, v172, v173
v_cvt_pk_bf16_f32 v95, v174, v175
M 196,12,120
v_cvt_pk_bf16_f32 v96, v176, v177
v_cvt_pk_bf16_f32 v97, v178, v179
M 200,16,120
v_cvt_pk_bf16_f32 v98, v180, v181
v_cvt_pk_bf16_f32 v99, v182, v183
s_waitcnt lgkmcnt(3)
M 204,20,120
buffer_store_dwordx4 v[92:95], v47, s[68:71], s16 offen offset:64 sc0 sc1 nt
M 204,68,124
s_barrier
RV 232,21,24576
M 200,64,124
RV 236,21,25600
v_and_b32 v86, 0x70007000, v84
M 196,60,124
RV 240,21,30720
v_and_b32 v87, 2, v84
M 192,56,124
RV 244,21,31744
v_cmp_ne_u32 vcc, 0, v85
v_cmp_eq_u32 s[30:31], 0, v86
M 188,52,124
buffer_store_dwordx4 v[96:99], v47, s[68:71], s16 offen offset:128 sc0 sc1 nt
M 184,48,124
RV 248,21,36864
v_cmp_eq_u32 s[58:59], 0, v87
M 184,72,128
RV 252,21,37888
s_cmp_eq_u64 s[30:31], -1
s_cselect_b64 vcc, -1, vcc
M 188,76,128
RV 104,21,43008
M 192,80,128
RV 108,21,44032
s_cmp_eq_u64 s[58:59], -1
s_cselect_b64 vcc, -1, vcc
M 196,84,128
v_cvt_pk_bf16_f32 v88, v184, v185
v_cvt_pk_bf16_f32 v89, v186, v187
M 200,88,128
v_cvt_pk_bf16_f32 v90, v188, v189
v_cvt_pk_bf16_f32 v91, v190, v191
M 204,92,128
ds_write_b128 v4, v[88:91] offset:0
s_waitcnt lgkmcnt(3)
M 208,0,132
v_cvt_pk_bf16_f32 v92, v192, v193
v_cvt_pk_bf16_f32 v93, v194, v195
M 212,4,132
v_cvt_pk_bf16_f32 v94, v196, v197
v_cvt_pk_bf16_f32 v95, v198, v199
v_cvt_pk_bf16_f32 v96, v200, v201
M 216,8,132
)ASM" R"ASM(
ds_write_b128 v4, v[92:95] offset:64
M 220,12,132
v_cvt_pk_bf16_f32 v97, v202, v203
v_cvt_pk_bf16_f32 v98, v204, v205
M 224,16,132
v_cvt_pk_bf16_f32 v99, v206, v207
s_waitcnt lgkmcnt(1)
v_add_f32_e64 v232, v232, v240
M 228,20,132
ds_write_b128 v4, v[96:99] offset:128
M 228,68,136
RV 144,1,0
RV 148,2,0
v_add_f32_e64 v233, v233, v241
M 224,64,136
RV 100,3,0
v_add_f32_e64 v234, v234, v242
M 220,60,136
v_add_f32 v235, v235, v243
v_add_f32 v236, v236, v244
M 216,56,136
v_add_f32 v237, v237, v245
v_add_f32 v238, v238, v246
M 212,52,136
v_add_f32 v239, v239, v247
v_add_f32 v248, v248, v104
M 208,48,136
v_add_f32 v249, v249, v105
v_add_f32 v250, v250, v106
v_add_f32_e64 v251, v251, v107
M 208,72,140
v_add_f32 v252, v252, v108
v_add_f32 v253, v253, v109
M 212,76,140
v_add_f32_e64 v254, v254, v110
v_add_f32 v255, v255, v111
M 216,80,140
s_waitcnt lgkmcnt(0)
buffer_store_dwordx4 v[144:147], v15, s[68:71], s17 offen offset:0 sc0 sc1 nt
M 220,84,140
v_cvt_pk_bf16_f32 v88, v208, v209
v_cvt_pk_bf16_f32 v89, v210, v211
M 224,88,140
v_cvt_pk_bf16_f32 v90, v212, v213
v_cvt_pk_bf16_f32 v91, v214, v215
v_cvt_pk_bf16_f32 v92, v216, v217
M 228,92,140
buffer_store_dwordx4 v[148:151], v15, s[68:71], s17 offen offset:1024 sc0 sc1 nt
ds_write_b128 v4, v[88:91] offset:0
v_cvt_pk_bf16_f32 v93, v218, v219
v_cvt_pk_bf16_f32 v94, v220, v221
v_cvt_pk_bf16_f32 v95, v222, v223
v_cvt_pk_bf16_f32 v96, v224, v225
v_cvt_pk_bf16_f32 v97, v226, v227
v_add_f32_e64 v232, v232, v248
buffer_store_dwordx4 v[100:103], v15, s[68:71], s17 offen offset:2048 sc0 sc1 nt
ds_write_b128 v4, v[92:95] offset:64
v_cvt_pk_bf16_f32 v98, v228, v229
v_cvt_pk_bf16_f32 v99, v230, v231
v_add_f32 v233, v233, v249
v_add_f32 v234, v234, v250
v_add_f32 v235, v235, v251
v_add_f32 v236, v236, v252
v_add_f32 v237, v237, v253
v_add_f32 v238, v238, v254
v_add_f32_e64 v239, v239, v255
ds_write_b128 v4, v[96:99] offset:128
RV 144,1,0
RV 148,2,0
RV 100,3,0
v_cvt_pk_bf16_f32 v240, v232, v233
v_cvt_pk_bf16_f32 v241, v234, v235
v_cvt_pk_bf16_f32 v242, v236, v237
v_cvt_pk_bf16_f32 v243, v238, v239
s_waitcnt lgkmcnt(0)
buffer_store_dwordx4 v[144:147], v15, s[68:71], s18 offen offset:0 sc0 sc1 nt
buffer_store_dwordx4 v[148:151], v15, s[68:71], s18 offen offset:1024 sc0 sc1 nt
buffer_store_dwordx4 v[100:103], v15, s[68:71], s18 offen offset:2048 sc0 sc1 nt
buffer_store_dwordx4 v[240:243], v46, s[68:71], s19 offen offset:0 sc0 sc1 nt
s_cbranch_vccnz L_done_%=
s_endpgm
.p2align 12
.p2align 6
s_nop 0
T_a81_%=:
s_setpc_b64 s[58:59]
L_body8_1_%=:
M 160,24,88
L_bodz8_1_%=:
RA 0,16,0
RA 4,16,1024
s_set_gpr_idx_on s43, gpr_idx(SRC1)
v_add_u32 v32, s44, v104
v_add_u32 v36, s44, v105
v_add_u32 v6, 0, v108
v_add_u32 v7, 0, v109
s_set_gpr_idx_off
M 164,28,88
RA 8,16,2048
v_pk_max_i16 v25, v6, s50
v_pk_min_i16 v25, v25, s52
v_mad_i32_i16 v33, v25, s53, v32
v_mad_i32_i16 v33, v25, s54, v33 op_sel:[1,0,0,0]
M 168,32,88
RA 12,16,3072
v_pk_max_i16 v27, v7, s50
v_pk_min_i16 v27, v27, s52
v_mad_i32_i16 v37, v27, s53, v36
v_mad_i32_i16 v37, v27, s54, v37 op_sel:[1,0,0,0]
M 172,36,88
RV 48,32,0
RV 52,33,0
v_and_b32_e64 v6, s56, v6
v_pk_add_f16 v25, v6, v6 op_sel:[0,1] op_sel_hi:[1,0]
M 176,40,88
RV 56,36,0
RV 60,37,0
v_pk_add_f16 v24, v25, s51 neg_lo:[1,0]
v_and_b32_e64 v7, s56, v7
M 180,44,88
RA 16,16,4096
RA 20,16,5120
v_pk_add_f16 v27, v7, v7 op_sel:[0,1] op_sel_hi:[1,0]
v_pk_add_f16 v26, v27, s51 neg_lo:[1,0]
M 204,44,92
RV 232,32,16
RV 236,33,16
M 200,40,92
RV 240,36,16
s_waitcnt lgkmcnt(7)
PM 72,48,24
PM 73,49,24
PM 74,50,24
M 196,36,92
s_add_u32 m0, s41, 49152
RV 244,37,16
PM 75,51,24
v_pk_fmac_f16 v72, v52, v25
PH 73,53,24
M 192,32,92
buffer_load_dwordx4 v15, s[36:39], s40 offen sc1 lds
PH 74,54,24
v_pk_fmac_f16 v75, v55, v25
M 188,28,92
s_waitcnt lgkmcnt(4)
PM 76,56,26
PM 77,57,26
PM 78,58,26
PM 79,59,26
M 184,24,92
v_pk_fmac_f16 v76, v60, v27
v_pk_fmac_f16 v77, v61, v27
PH 78,62,26
v_pk_fmac_f16 v79, v63, v27
M 160,0,72
RA 24,16,6144
buffer_load_dwordx4 v15, s[36:39], s40 offen offset:1024 sc1 lds
s_waitcnt lgkmcnt(2)
PM 88,232,24
M 164,4,72
RA 28,16,7168
PM 89,233,24
M 168,8,72
RA 32,16,8192
PM 90,234,24
M 172,12,72
RA 36,16,9216
RA 40,16,10240
PM 91,235,24
M 176,16,72
RA 44,16,11264
PH 88,236,24
M 180,20,72
buffer_load_dwordx4 v15, s[36:39], s40 offen offset:2048 sc1 lds
PH 89,237,24
M 204,20,76
RV 48,32,32
RV 52,33,32
PH 90,238,24
M 200,16,76
RV 56,36,32
v_pk_fmac_f16 v91, v239, v25
M 196,12,76
RV 60,37,32
s_waitcnt lgkmcnt(5)
PM 92,240,26
M 192,8,76
PM 93,241,26
PM 94,242,26
M 188,4,76
PM 95,243,26
v_pk_fmac_f16 v92, v244, v27
v_pk_fmac_f16 v93, v245, v27
M 184,0,76
PH 94,246,26
v_pk_fmac_f16 v95, v247, v27
M 160,24,88
RA 0,16,12288
buffer_load_dwordx4 v15, s[36:39], s40 offen offset:3072 sc1 lds
s_waitcnt lgkmcnt(2)
PM 72,48,24
M 164,28,88
RA 4,16,13312
PM 73,49,24
M 168,32,88
RA 8,16,14336
RA 12,16,15360
M 172,36,88
PM 74,50,24
PM 75,51,24
M 176,40,88
RA 16,16,16384
RA 20,16,17408
PH 72,52,24
M 180,44,88
s_add_u32 m0, m0, 4096
RV 232,32,48
PH 73,53,24
M 204,44,92
buffer_load_dwordx4 v45, s[36:39], s40 offen sc1 lds
M 200,40,92
RV 236,33,48
PH 74,54,24
M 196,36,92
RV 240,36,48
v_pk_fmac_f16 v75, v55, v25
M 192,32,92
RV 244,37,48
s_waitcnt lgkmcnt(4)
PM 76,56,26
PM 77,57,26
M 188,28,92
PM 78,58,26
PM 79,59,26
PH 76,60,26
M 184,24,92
v_pk_fmac_f16 v77, v61, v27
v_pk_fmac_f16 v78, v62, v27
v_pk_fmac_f16 v79, v63, v27
M 160,0,72
RA 24,16,18432
buffer_load_dwordx4 v45, s[36:39], s40 offen offset:1024 sc1 lds
s_waitcnt lgkmcnt(3)
PM 88,232,24
M 164,4,72
RA 28,16,19456
PM 89,233,24
M 168,8,72
RA 32,16,20480
PM 90,234,24
PM 91,235,24
M 172,12,72
RA 36,16,21504
RA 40,16,22528
PH 88,236,24
M 176,16,72
RA 44,16,23552
PH 89,237,24
M 180,20,72
PH 90,238,24
M 204,20,76
RV 48,32,64
RV 52,33,64
v_pk_fmac_f16 v91, v239, v25
M 200,16,76
RV 56,36,64
s_waitcnt lgkmcnt(6)
PM 92,240,26
M 196,12,76
s_add_u32 s40, s40, 24704
RV 60,37,64
PM 93,241,26
M 192,8,76
s_waitcnt vmcnt(6) lgkmcnt(4)
PM 94,242,26
M 188,4,76
s_barrier
PM 95,243,26
PH 92,244,26
M 184,0,76
v_pk_fmac_f16 v93, v245, v27
v_pk_fmac_f16 v94, v246, v27
v_pk_fmac_f16 v95, v247, v27
M 160,24,88
RA 0,16,24576
s_waitcnt lgkmcnt(2)
PM 72,48,24
M 164,28,88
RA 4,16,25600
PM 73,49,24
M 168,32,88
RA 8,16,26624
PM 74,50,24
M 172,36,88
RA 12,16,27648
RA 16,16,28672
PM 75,51,24
M 176,40,88
s_mov_b32 m0, s41
RA 20,16,29696
v_pk_fmac_f16 v72, v52, v25
M 180,44,88
buffer_load_dwordx4 v15, s[36:39], s40 offen sc1 lds
PH 73,53,24
M 204,44,92
RV 232,32,80
RV 236,33,80
PH 74,54,24
M 200,40,92
RV 240,36,80
v_pk_fmac_f16 v75, v55, v25
M 196,36,92
RV 244,37,80
s_waitcnt lgkmcnt(5)
PM 76,56,26
M 192,32,92
PM 77,57,26
PM 78,58,26
M 188,28,92
PM 79,59,26
v_pk_fmac_f16 v76, v60, v27
v_pk_fmac_f16 v77, v61, v27
M 184,24,92
PH 78,62,26
v_pk_fmac_f16 v79, v63, v27
M 160,0,72
RA 24,16,30720
buffer_load_dwordx4 v15, s[36:39], s40 offen offset:1024 sc1 lds
s_waitcnt lgkmcnt(2)
PM 88,232,24
M 164,4,72
RA 28,16,31744
PM 89,233,24
M 168,8,72
RA 32,16,32768
PM 90,234,24
M 172,12,72
RA 36,16,33792
RA 40,16,34816
PM 91,235,24
M 176,16,72
RA 44,16,35840
PH 88,236,24
M 180,20,72
buffer_load_dwordx4 v15, s[36:39], s40 offen offset:2048 sc1 lds
PH 89,237,24
M 204,20,76
RV 48,32,96
RV 52,33,96
PH 90,238,24
M 200,16,76
RV 56,36,96
v_pk_fmac_f16 v91, v239, v25
M 196,12,76
RV 60,37,96
s_waitcnt lgkmcnt(5)
PM 92,240,26
M 192,8,76
PM 93,241,26
PM 94,242,26
M 188,4,76
PM 95,243,26
v_pk_fmac_f16 v92, v244, v27
v_pk_fmac_f16 v93, v245, v27
M 184,0,76
PH 94,246,26
v_pk_fmac_f16 v95, v247, v27
M 160,24,88
RA 0,16,36864
buffer_load_dwordx4 v15, s[36:39], s40 offen offset:3072 sc1 lds
s_waitcnt lgkmcnt(2)
PM 72,48,24
M 164,28,88
RA 4,16,37888
PM 73,49,24
M 168,32,88
RA 8,16,38912
RA 12,16,39936
M 172,36,88
PM 74,50,24
PM 75,51,24
M 176,40,88
RA 16,16,40960
RA 20,16,41984
PH 72,52,24
M 180,44,88
s_add_u32 m0, m0, 4096
RV 232,32,112
PH 73,53,24
M 204,44,92
buffer_load_dwordx4 v45, s[36:39], s40 offen sc1 lds
M 200,40,92
RV 236,33,112
PH 74,54,24
M 196,36,92
RV 240,36,112
v_pk_fmac_f16 v75, v55, v25
M 192,32,92
RV 244,37,112
s_waitcnt lgkmcnt(4)
PM 76,56,26
PM 77,57,26
M 188,28,92
PM 78,58,26
PM 79,59,26
PH 76,60,26
M 184,24,92
v_pk_fmac_f16 v77, v61, v27
v_pk_fmac_f16 v78, v62, v27
v_pk_fmac_f16 v79, v63, v27
M 160,0,72
RA 24,16,43008
buffer_load_dwordx4 v45, s[36:39], s40 offen offset:1024 sc1 lds
s_waitcnt lgkmcnt(3)
PM 88,232,24
M 164,4,72
RA 28,16,44032
PM 89,233,24
M 168,8,72
RA 32,16,45056
PM 90,234,24
PM 91,235,24
M 172,12,72
RA 36,16,46080
RA 40,16,47104
PH 88,236,24
M 176,16,72
RA 44,16,48128
PH 89,237,24
M 180,20,72
PH 90,238,24
M 204,20,76
RV 48,32,128
RV 52,33,128
v_pk_fmac_f16 v91, v239, v25
M 200,16,76
RV 56,36,128
s_waitcnt lgkmcnt(6)
PM 92,240,26
M 196,12,76
s_add_u32 s40, s40, 24704
RV 60,37,128
PM 93,241,26
M 192,8,76
s_waitcnt vmcnt(6) lgkmcnt(4)
PM 94,242,26
M 188,4,76
s_barrier
PM 95,243,26
PH 92,244,26
M 184,0,76
v_pk_fmac_f16 v93, v245, v27
v_pk_fmac_f16 v94, v246, v27
v_pk_fmac_f16 v95, v247, v27
M 160,24,88
RA 0,20,0
s_waitcnt lgkmcnt(2)
PM 72,48,24
M 164,28,88
RA 4,20,1024
PM 73,49,24
M 168,32,88
RA 8,20,2048
PM 74,50,24
M 172,36,88
RA 12,20,3072
RA 16,20,4096
PM 75,51,24
M 176,40,88
s_add_u32 m0, s41, 24576
RA 20,20,5120
PH 72,52,24
M 180,44,88
buffer_load_dwordx4 v15, s[36:39], s40 offen sc1 lds
PH 73,53,24
M 204,44,92
RV 232,32,144
RV 236,33,144
PH 74,54,24
M 200,40,92
RV 240,36,144
v_pk_fmac_f16 v75, v55, v25
M 196,36,92
RV 244,37,144
s_waitcnt lgkmcnt(5)
PM 76,56,26
M 192,32,92
s_branch L_in8_1_%=
.p2align 6
s_nop 0
T_end_%=:
s_setpc_b64 s[58:59]
L_retry_%=:
s_sub_u32 s75, s75, 1
s_cbranch_scc1 L_giveup_%=
buffer_load_dwordx4 v[6:9], v15, s[36:39], 0 offen sc1
buffer_load_dwordx4 v[100:103], v47, s[36:39], 0 offen offset:1024 sc1
s_waitcnt vmcnt(0)
v_bitop3_b32 v6, v6, v8, s66 bitop3:0x96
v_bitop3_b32 v100, v100, v102, s66 bitop3:0x96
v_bitop3_b32 v7, v7, v9, s67 bitop3:0x96
v_bitop3_b32 v101, v101, v103, s67 bitop3:0x96
v_or_b32 v6, v6, v100
v_or_b32 v5, v7, v101
v_cmp_ne_u32 vcc, 0, v6
v_cmp_lt_u32 s[30:31], 7, v5
s_or_b64 vcc, vcc, s[30:31]
s_cbranch_scc1 L_retry_%=
s_setpc_b64 s[72:73]
L_giveup_%=:
v_mov_b32 v5, 1
s_mov_b32 s38, 2048
ds_write_b32 v12, v5
s_waitcnt lgkmcnt(0)
s_setpc_b64 s[72:73]
L_idle_%=:
v_cmp_nge_f32 vcc, 1.0, v12
v_and_b32 v13, 0x7ff87ff8, v13
v_add_u32 v12, 162316, v15
v_addc_co_u32 v13, vcc, 0, v13, vcc
s_mov_b32 s11, 0
s_mov_b32 s55, 17
ds_write_b32 v12, v13
s_add_u32 m0, s41, 24576
s_waitcnt lgkmcnt(0)
s_barrier
buffer_load_dwordx4 v15, s[36:39], s40 offen sc1 lds
buffer_load_dwordx4 v15, s[36:39], s40 offen offset:1024 sc1 lds
buffer_load_dwordx4 v15, s[36:39], s40 offen offset:2048 sc1 lds
buffer_load_dwordx4 v15, s[36:39], s40 offen offset:3072 sc1 lds
s_addk_i32 m0, 4096
s_nop 0
buffer_load_dwordx4 v45, s[36:39], s40 offen sc1 lds
buffer_load_dwordx4 v45, s[36:39], s40 offen offset:1024 sc1 lds
s_addk_i32 s40, 24704
s_waitcnt vmcnt(0)
s_barrier
s_call_b64 s[72:73], L_retry_%=
s_add_u32 m0, s41, 49152
v_add_u32_e64 v12, -12, v12
buffer_load_dwordx4 v15, s[36:39], s40 offen sc1 lds
buffer_load_dwordx4 v15, s[36:39], s40 offen offset:1024 sc1 lds
buffer_load_dwordx4 v15, s[36:39], s40 offen offset:2048 sc1 lds
buffer_load_dwordx4 v15, s[36:39], s40 offen offset:3072 sc1 lds
s_addk_i32 m0, 4096
s_nop 0
buffer_load_dwordx4 v45, s[36:39], s40 offen sc1 lds
buffer_load_dwordx4 v45, s[36:39], s40 offen offset:1024 sc1 lds
s_addk_i32 s40, 24704
L_idle1_%=:
s_add_u32 m0, s41, s11
s_add_u32 s11, s11, 24576
buffer_load_dwordx4 v15, s[36:39], s40 offen sc1 lds
buffer_load_dwordx4 v15, s[36:39], s40 offen offset:1024 sc1 lds
buffer_load_dwordx4 v15, s[36:39], s40 offen offset:2048 sc1 lds
buffer_load_dwordx4 v15, s[36:39], s40 offen offset:3072 sc1 lds
s_addk_i32 m0, 4096
s_nop 0
buffer_load_dwordx4 v45, s[36:39], s40 offen sc1 lds
buffer_load_dwordx4 v45, s[36:39], s40 offen offset:1024 sc1 lds
s_add_u32 s40, s40, 24704
s_cmp_eq_u32 s11, 73728
s_cselect_b32 s11, 0, s11
s_sub_u32 s55, s55, 1
s_cmp_lg_u32 s55, 0
s_waitcnt vmcnt(6) lgkmcnt(0)
s_barrier
s_cbranch_scc1 L_idle1_%=
ds_read_b128 v[84:87], v12
s_mov_b64 vcc, -1
s_waitcnt vmcnt(0) lgkmcnt(0)
v_or3_b32 v84, v84, v85, v86
v_or3_b32 v84, v84, v87, v5
L_done_%=:
s_mov_b32 s9, 0
s_cbranch_vccz L_fin_%=
v_and_b32 v72, 1, v84
v_cmp_ne_u32 vcc, 0, v72
s_cmp_lg_u64 vcc, 0
s_cselect_b32 s60, 1, 0
v_and_b32 v72, 1879076864, v84
v_cmp_ne_u32 vcc, 0, v72
s_cmp_lg_u64 vcc, 0
s_cselect_b32 s61, 1, 0
v_and_b32 v72, 2, v84
v_cmp_ne_u32 vcc, 0, v72
s_cmp_lg_u64 vcc, 0
s_cselect_b32 s63, 1, 0
v_and_b32 v72, 4, v84
v_cmp_ne_u32 vcc, 0, v72
s_cmp_lg_u64 vcc, 0
s_cselect_b32 s64, 1, 0
v_and_b32 v72, 1073758208, v84
v_cmp_ne_u32 vcc, 0, v72
s_cmp_lg_u64 vcc, 0
s_cselect_b32 s65, 1, 0
s_xor_b32 s62, s61, 1
s_andn2_b32 s64, s64, s63
s_or_b32 s9, s60, s62
s_or_b32 s9, s9, s64
s_cbranch_scc1 L_fin_%=
s_cmp_eq_u32 s65, 0
s_cbranch_scc1 L_fin_%=
v_mov_b64 v[72:73], 0
s_cmp_lt_u32 s24, 8
s_cbranch_scc1 L_d2_%=
v_pk_fma_f32 v[72:73], v[160:161], 0, v[72:73] op_sel_hi:[1,0,1]
v_pk_fma_f32 v[72:73], v[162:163], 0, v[72:73] op_sel_hi:[1,0,1]
v_pk_fma_f32 v[72:73], v[164:165], 0, v[72:73] op_sel_hi:[1,0,1]
v_pk_fma_f32 v[72:73], v[166:167], 0, v[72:73] op_sel_hi:[1,0,1]
v_pk_fma_f32 v[72:73], v[168:169], 0, v[72:73] op_sel_hi:[1,0,1]
v_pk_fma_f32 v[72:73], v[170:171], 0, v[72:73] op_sel_hi:[1,0,1]
v_pk_fma_f32 v[72:73], v[172:173], 0, v[72:73] op_sel_hi:[1,0,1]
v_pk_fma_f32 v[72:73], v[174:175], 0, v[72:73] op_sel_hi:[1,0,1]
v_pk_fma_f32 v[72:73], v[176:177], 0, v[72:73] op_sel_hi:[1,0,1]
v_pk_fma_f32 v[72:73], v[178:179], 0, v[72:73] op_sel_hi:[1,0,1]
v_pk_fma_f32 v[72:73], v[180:181], 0, v[72:73] op_sel_hi:[1,0,1]
v_pk_fma_f32 v[72:73], v[182:183], 0, v[72:73] op_sel_hi:[1,0,1]
v_pk_fma_f32 v[72:73], v[184:185], 0, v[72:73] op_sel_hi:[1,0,1]
v_pk_fma_f32 v[72:73], v[186:187], 0, v[72:73] op_sel_hi:[1,0,1]
v_pk_fma_f32 v[72:73], v[188:189], 0, v[72:73] op_sel_hi:[1,0,1]
v_pk_fma_f32 v[72:73], v[190:191], 0, v[72:73] op_sel_hi:[1,0,1]
v_pk_fma_f32 v[72:73], v[192:193], 0, v[72:73] op_sel_hi:[1,0,1]
v_pk_fma_f32 v[72:73], v[194:195], 0, v[72:73] op_sel_hi:[1,0,1]
v_pk_fma_f32 v[72:73], v[196:197], 0, v[72:73] op_sel_hi:[1,0,1]
v_pk_fma_f32 v[72:73], v[198:199], 0, v[72:73] op_sel_hi:[1,0,1]
v_pk_fma_f32 v[72:73], v[200:201], 0, v[72:73] op_sel_hi:[1,0,1]
v_pk_fma_f32 v[72:73], v[202:203], 0, v[72:73] op_sel_hi:[1,0,1]
v_pk_fma_f32 v[72:73], v[204:205], 0, v[72:73] op_sel_hi:[1,0,1]
v_pk_fma_f32 v[72:73], v[206:207], 0, v[72:73] op_sel_hi:[1,0,1]
s_cmp_lt_u32 s24, 12
s_cbranch_scc1 L_d2_%=
v_pk_fma_f32 v[72:73], v[208:209], 0, v[72:73] op_sel_hi:[1,0,1]
v_pk_fma_f32 v[72:73], v[210:211], 0, v[72:73] op_sel_hi:[1,0,1]
v_pk_fma_f32 v[72:73], v[212:213], 0, v[72:73] op_sel_hi:[1,0,1]
v_pk_fma_f32 v[72:73], v[214:215], 0, v[72:73] op_sel_hi:[1,0,1]
v_pk_fma_f32 v[72:73], v[216:217], 0, v[72:73] op_sel_hi:[1,0,1]
v_pk_fma_f32 v[72:73], v[218:219], 0, v[72:73] op_sel_hi:[1,0,1]
v_pk_fma_f32 v[72:73], v[220:221], 0, v[72:73] op_sel_hi:[1,0,1]
v_pk_fma_f32 v[72:73], v[222:223], 0, v[72:73] op_sel_hi:[1,0,1]
v_pk_fma_f32 v[72:73], v[224:225], 0, v[72:73] op_sel_hi:[1,0,1]
v_pk_fma_f32 v[72:73], v[226:227], 0, v[72:73] op_sel_hi:[1,0,1]
v_pk_fma_f32 v[72:73], v[228:229], 0, v[72:73] op_sel_hi:[1,0,1]
v_pk_fma_f32 v[72:73], v[230:231], 0, v[72:73] op_sel_hi:[1,0,1]
s_cmp_lt_u32 s24, 13
s_cbranch_scc1 L_d2_%=
v_pk_fma_f32 v[72:73], v[232:233], 0, v[72:73] op_sel_hi:[1,0,1]
v_pk_fma_f32 v[72:73], v[234:235], 0, v[72:73] op_sel_hi:[1,0,1]
v_pk_fma_f32 v[72:73], v[236:237], 0, v[72:73] op_sel_hi:[1,0,1]
v_pk_fma_f32 v[72:73], v[238:239], 0, v[72:73] op_sel_hi:[1,0,1]
L_d2_%=:
v_add_f32 v72, v72, v73
s_lshl_b32 s11, s10, 2
v_add_u32 v73, s11, v12
s_barrier
ds_write_b32 v73, v72
s_waitcnt lgkmcnt(0)
s_barrier
ds_read_b128 v[76:79], v12
s_waitcnt lgkmcnt(0)
v_pk_add_f32 v[76:77], v[76:77], v[78:79]
v_cmp_u_f32 vcc, v76, v77
s_cmp_lg_u64 vcc, 0
s_cselect_b32 s9, 1, 0
L_fin_%=:
s_cmp_lg_u32 s9, 0
s_cbranch_scc1 L_slow_%=
s_endpgm
.p2align 12
.p2align 6
s_nop 0
T_ef_%=:
s_setpc_b64 s[58:59]
L_s2r_%=:
v_cmp_ne_u32 vcc, 0, v232
v_add_u32_e64 v216, s41, v15
s_waitcnt vmcnt(10)
RA 128,231,0
RA 132,231,768
RA 136,231,64
RA 140,231,832
RA 144,231,128
RA 148,231,896
s_cbranch_vccnz L_modes_%=
L_modesr_%=:
v_and_b32_e64 v217, 48, v15
s_waitcnt lgkmcnt(4)
v_mfma_f32_16x16x32_bf16 v[160:163], a[128:131], v[226:229], 0
s_mov_b32 s66, 1779033703
v_mfma_f32_16x16x32_bf16 v[164:167], a[132:135], v[226:229], 0
s_mov_b32 s67, 1013904242
v_mfma_f32_16x16x32_bf16 v[176:179], a[128:131], v[224:227], 0
s_add_u32 s40, s41, -60800
v_mfma_f32_16x16x32_bf16 v[180:183], a[132:135], v[224:227], 0
s_and_b32 s55, s42, 15
s_waitcnt lgkmcnt(0)
v_mfma_f32_16x16x32_bf16 v[192:195], a[136:139], v[226:229], 0
buffer_load_dwordx4 a[96:99], v24, s[80:83], s19 offen
v_cvt_pk_f16_f32 v208, v160, v161
v_cvt_pk_f16_f32 v209, v162, v163
v_mfma_f32_16x16x32_bf16 v[196:199], a[140:143], v[226:229], 0
v_cvt_pk_f16_f32 v210, v164, v165
v_cvt_pk_f16_f32 v211, v166, v167
v_mfma_f32_16x16x32_bf16 v[160:163], a[136:139], v[224:227], 0
ds_write_b128 v216, v[208:211] offset:0
v_cvt_pk_f16_f32 v212, v176, v177
v_cvt_pk_f16_f32 v213, v178, v179
v_mfma_f32_16x16x32_bf16 v[164:167], a[140:143], v[224:227], 0
v_cvt_pk_f16_f32 v214, v180, v181
v_cvt_pk_f16_f32 v215, v182, v183
v_mfma_f32_16x16x32_bf16 v[176:179], a[144:147], v[226:229], 0
buffer_load_dwordx4 a[100:103], v25, s[80:83], s19 offen
ds_write_b128 v216, v[212:215] offset:1024
v_cvt_pk_f16_f32 v208, v192, v193
v_cvt_pk_f16_f32 v209, v194, v195
v_mfma_f32_16x16x32_bf16 v[180:183], a[148:151], v[226:229], 0
v_cvt_pk_f16_f32 v210, v196, v197
v_cvt_pk_f16_f32 v211, v198, v199
v_mfma_f32_16x16x32_bf16 v[192:195], a[144:147], v[224:227], 0
ds_write_b128 v216, v[208:211] offset:2048
v_cvt_pk_f16_f32 v212, v160, v161
v_cvt_pk_f16_f32 v213, v162, v163
v_mfma_f32_16x16x32_bf16 v[196:199], a[148:151], v[224:227], 0
v_cvt_pk_f16_f32 v214, v164, v165
v_cvt_pk_f16_f32 v215, v166, v167
v_mfma_f32_32x32x16_bf16 v[160:175], v[220:223], a[48:51], 0
buffer_load_dwordx4 a[104:107], v26, s[80:83], s19 offen
ds_write_b128 v216, v[212:215] offset:3072
v_cvt_pk_f16_f32 v208, v176, v177
v_cvt_pk_f16_f32 v209, v178, v179
v_cvt_pk_f16_f32 v210, v180, v181
v_cvt_pk_f16_f32 v211, v182, v183
v_mfma_f32_32x32x16_bf16 v[176:191], v[220:223], a[52:55], 0
ds_write_b128 v216, v[208:211] offset:4096
v_cvt_pk_f16_f32 v212, v192, v193
v_cvt_pk_f16_f32 v213, v194, v195
v_cvt_pk_f16_f32 v214, v196, v197
v_cvt_pk_f16_f32 v215, v198, v199
s_waitcnt vmcnt(11)
v_mfma_f32_32x32x16_bf16 v[192:207], v[220:223], a[56:59], 0
buffer_load_dwordx4 a[108:111], v27, s[80:83], s19 offen
ds_write_b128 v216, v[212:215] offset:5120
v_cvt_pk_f16_f32 v208, v160, v161
v_cvt_pk_f16_f32 v209, v162, v163
v_cvt_pk_f16_f32 v210, v164, v165
v_cvt_pk_f16_f32 v211, v166, v167
v_or3_b32 v13, v13, v208, v209
v_or3_b32 v13, v13, v210, v211
v_mfma_f32_32x32x16_bf16 v[160:175], v[220:223], a[60:63], 0
ds_write_b128 v96, v[208:211] offset:0
v_cvt_pk_f16_f32 v212, v176, v177
v_cvt_pk_f16_f32 v213, v178, v179
v_cvt_pk_f16_f32 v214, v180, v181
v_cvt_pk_f16_f32 v215, v182, v183
v_or3_b32 v13, v13, v212, v213
v_or3_b32 v13, v13, v214, v215
s_waitcnt vmcnt(10)
v_mfma_f32_32x32x16_bf16 v[176:191], v[220:223], a[64:67], 0
buffer_load_dwordx4 a[112:115], v28, s[80:83], s20 offen
ds_write_b128 v97, v[212:215] offset:0
v_cvt_pk_f16_f32 v208, v192, v193
v_cvt_pk_f16_f32 v209, v194, v195
v_cvt_pk_f16_f32 v210, v196, v197
v_cvt_pk_f16_f32 v211, v198, v199
v_or3_b32 v13, v13, v208, v209
v_or3_b32 v13, v13, v210, v211
v_mfma_f32_32x32x16_bf16 v[192:207], v[220:223], a[68:71], 0
ds_write_b128 v98, v[208:211] offset:0
v_cvt_pk_f16_f32 v212, v160, v161
v_cvt_pk_f16_f32 v213, v162, v163
v_cvt_pk_f16_f32 v214, v164, v165
v_cvt_pk_f16_f32 v215, v166, v167
v_or3_b32 v13, v13, v212, v213
v_or3_b32 v13, v13, v214, v215
s_waitcnt vmcnt(9)
v_mfma_f32_32x32x16_bf16 v[160:175], v[220:223], a[72:75], 0
buffer_load_dwordx4 a[116:119], v28, s[80:83], s51 offen
ds_write_b128 v99, v[212:215] offset:0
v_cvt_pk_f16_f32 v208, v176, v177
v_cvt_pk_f16_f32 v209, v178, v179
v_cvt_pk_f16_f32 v210, v180, v181
v_cvt_pk_f16_f32 v211, v182, v183
v_or3_b32 v13, v13, v208, v209
v_or3_b32 v13, v13, v210, v211
v_mfma_f32_32x32x16_bf16 v[176:191], v[220:223], a[76:79], 0
ds_write_b128 v96, v[208:211] offset:18432
v_cvt_pk_f16_f32 v212, v192, v193
v_cvt_pk_f16_f32 v213, v194, v195
v_cvt_pk_f16_f32 v214, v196, v197
v_cvt_pk_f16_f32 v215, v198, v199
v_or3_b32 v13, v13, v212, v213
v_or3_b32 v13, v13, v214, v215
s_waitcnt vmcnt(9)
v_mfma_f32_32x32x16_bf16 v[192:207], v[220:223], a[80:83], 0
buffer_load_dwordx4 a[120:123], v28, s[80:83], s52 offen
ds_write_b128 v97, v[212:215] offset:18432
v_cvt_pk_f16_f32 v208, v160, v161
v_cvt_pk_f16_f32 v209, v162, v163
v_cvt_pk_f16_f32 v210, v164, v165
v_cvt_pk_f16_f32 v211, v166, v167
v_or3_b32 v13, v13, v208, v209
v_or3_b32 v13, v13, v210, v211
s_waitcnt vmcnt(9)
v_mfma_f32_32x32x16_bf16 v[160:175], v[220:223], a[84:87], 0
ds_write_b128 v98, v[208:211] offset:18432
v_cvt_pk_f16_f32 v212, v176, v177
v_cvt_pk_f16_f32 v213, v178, v179
v_cvt_pk_f16_f32 v214, v180, v181
v_cvt_pk_f16_f32 v215, v182, v183
v_or3_b32 v13, v13, v212, v213
v_or3_b32 v13, v13, v214, v215
s_waitcnt vmcnt(8)
v_mfma_f32_32x32x16_bf16 v[176:191], v[220:223], a[88:91], 0
buffer_load_dwordx4 a[124:127], v29, s[80:83], 0 offen
ds_write_b128 v99, v[212:215] offset:18432
v_cvt_pk_f16_f32 v208, v192, v193
v_cvt_pk_f16_f32 v209, v194, v195
v_cvt_pk_f16_f32 v210, v196, v197
v_cvt_pk_f16_f32 v211, v198, v199
v_or3_b32 v13, v13, v208, v209
v_or3_b32 v13, v13, v210, v211
s_waitcnt vmcnt(8)
v_mfma_f32_32x32x16_bf16 v[192:207], v[220:223], a[92:95], 0
ds_write_b128 v96, v[208:211] offset:36864
v_cvt_pk_f16_f32 v212, v160, v161
v_cvt_pk_f16_f32 v213, v162, v163
v_cvt_pk_f16_f32 v214, v164, v165
v_cvt_pk_f16_f32 v215, v166, v167
v_or3_b32 v13, v13, v212, v213
v_or3_b32 v13, v13, v214, v215
s_waitcnt vmcnt(7)
v_mfma_f32_32x32x16_bf16 v[160:175], v[220:223], a[96:99], 0
ds_write_b128 v97, v[212:215] offset:36864
v_cvt_pk_f16_f32 v208, v176, v177
v_cvt_pk_f16_f32 v209, v178, v179
v_cvt_pk_f16_f32 v210, v180, v181
v_cvt_pk_f16_f32 v211, v182, v183
v_or3_b32 v13, v13, v208, v209
v_or3_b32 v13, v13, v210, v211
s_waitcnt vmcnt(6)
v_mfma_f32_32x32x16_bf16 v[176:191], v[220:223], a[100:103], 0
ds_write_b128 v98, v[208:211] offset:36864
v_cvt_pk_f16_f32 v212, v192, v193
v_cvt_pk_f16_f32 v213, v194, v195
v_cvt_pk_f16_f32 v214, v196, v197
v_cvt_pk_f16_f32 v215, v198, v199
v_or3_b32 v13, v13, v212, v213
v_or3_b32 v13, v13, v214, v215
s_waitcnt vmcnt(5)
v_mfma_f32_32x32x16_bf16 v[192:207], v[220:223], a[104:107], 0
buffer_load_dwordx4 v[6:9], v217, s[36:39], 0 offen offset:1152 sc1
ds_write_b128 v99, v[212:215] offset:36864
v_cvt_pk_f16_f32 v208, v160, v161
v_cvt_pk_f16_f32 v209, v162, v163
v_cvt_pk_f16_f32 v210, v164, v165
v_cvt_pk_f16_f32 v211, v166, v167
v_or3_b32 v13, v13, v208, v209
v_or3_b32 v13, v13, v210, v211
s_waitcnt vmcnt(5)
v_mfma_f32_32x32x16_bf16 v[160:175], v[220:223], a[108:111], 0
ds_write_b128 v96, v[208:211] offset:55296
v_cvt_pk_f16_f32 v212, v176, v177
v_cvt_pk_f16_f32 v213, v178, v179
v_cvt_pk_f16_f32 v214, v180, v181
v_cvt_pk_f16_f32 v215, v182, v183
v_or3_b32 v13, v13, v212, v213
v_or3_b32 v13, v13, v214, v215
s_waitcnt vmcnt(4)
v_mfma_f32_32x32x16_bf16 v[176:191], v[220:223], a[112:115], 0
ds_write_b128 v97, v[212:215] offset:55296
v_cvt_pk_f16_f32 v208, v192, v193
v_cvt_pk_f16_f32 v209, v194, v195
v_cvt_pk_f16_f32 v210, v196, v197
v_cvt_pk_f16_f32 v211, v198, v199
v_or3_b32 v13, v13, v208, v209
v_or3_b32 v13, v13, v210, v211
s_waitcnt vmcnt(3)
v_mfma_f32_32x32x16_bf16 v[192:207], v[220:223], a[116:119], 0
ds_write_b128 v98, v[208:211] offset:55296
v_cvt_pk_f16_f32 v212, v160, v161
v_cvt_pk_f16_f32 v213, v162, v163
v_cvt_pk_f16_f32 v214, v164, v165
v_cvt_pk_f16_f32 v215, v166, v167
v_or3_b32 v13, v13, v212, v213
v_or3_b32 v13, v13, v214, v215
s_waitcnt vmcnt(2)
v_mfma_f32_32x32x16_bf16 v[160:175], v[220:223], a[120:123], 0
ds_write_b128 v99, v[212:215] offset:55296
v_cvt_pk_f16_f32 v208, v176, v177
v_cvt_pk_f16_f32 v209, v178, v179
v_cvt_pk_f16_f32 v210, v180, v181
v_cvt_pk_f16_f32 v211, v182, v183
v_or3_b32 v13, v13, v208, v209
v_or3_b32 v13, v13, v210, v211
s_waitcnt vmcnt(1)
v_mfma_f32_32x32x16_bf16 v[176:191], v[220:223], a[124:127], 0
ds_write_b128 v100, v[208:211] offset:0
v_cvt_pk_f16_f32 v212, v192, v193
v_cvt_pk_f16_f32 v213, v194, v195
v_cvt_pk_f16_f32 v214, v196, v197
v_cvt_pk_f16_f32 v215, v198, v199
v_or3_b32 v13, v13, v212, v213
v_or3_b32 v13, v13, v214, v215
s_mov_b32 s51, 15360
s_mov_b32 s52, 65537
s_mov_b32 s53, 4608
ds_write_b128 v100, v[212:215] offset:4608
v_cvt_pk_f16_f32 v208, v160, v161
v_cvt_pk_f16_f32 v209, v162, v163
v_cvt_pk_f16_f32 v210, v164, v165
v_cvt_pk_f16_f32 v211, v166, v167
v_or3_b32 v13, v13, v208, v209
v_or3_b32 v13, v13, v210, v211
s_mov_b32 s54, 208
s_mov_b32 s50, -1
s_mov_b32 s56, 2147450879
ds_write_b128 v100, v[208:211] offset:9216
v_cvt_pk_f16_f32 v212, v176, v177
v_cvt_pk_f16_f32 v213, v178, v179
v_cvt_pk_f16_f32 v214, v180, v181
v_cvt_pk_f16_f32 v215, v182, v183
v_or3_b32 v13, v13, v212, v213
v_or3_b32 v13, v13, v214, v215
s_nop 1
ds_write_b128 v101, v[212:215] offset:4432
s_waitcnt vmcnt(0)
v_bitop3_b32 v6, v6, v8, s66 bitop3:0x96
v_bitop3_b32 v7, v7, v9, s67 bitop3:0x96
v_and_or_b32 v6, v7, -8, v6
v_cmp_ne_u32 vcc, 0, v6
s_cmp_lg_u32 s55, 0
s_cbranch_vccnz L_r1_%=
L_r1r_%=:
s_cbranch_scc1 L_entd_%=
s_cmp_eq_u32 s24, 13
s_cbranch_scc1 L_ent13_0_%=
s_cmp_eq_u32 s24, 12
s_cbranch_scc1 L_ent12_0_%=
s_cmp_eq_u32 s24, 8
s_cbranch_scc1 L_ent8_0_%=
s_branch L_idle_%=
L_entd_%=:
s_cmp_eq_u32 s24, 12
s_cbranch_scc0 L_entd12_%=
s_bitcmp1_b32 s55, 1
s_cbranch_scc1 L_ent12_2_%=
s_branch L_ent12_1_%=
L_entd12_%=:
s_cmp_eq_u32 s24, 8
s_cbranch_scc0 L_entd8_%=
s_bitcmp1_b32 s55, 1
s_cbranch_scc1 L_ent8_2_%=
s_branch L_ent8_1_%=
L_entd8_%=:
s_cmp_eq_u32 s24, 0
s_cbranch_scc1 L_idle_%=
s_bitcmp1_b32 s55, 1
s_cbranch_scc1 L_ent13_2_%=
s_branch L_ent13_1_%=
L_r1_%=:
s_sub_u32 s75, s75, 1
s_cbranch_scc1 L_g1_%=
buffer_load_dwordx4 v[6:9], v217, s[36:39], 0 offen offset:1152 sc1
s_waitcnt vmcnt(0)
v_bitop3_b32 v6, v6, v8, s66 bitop3:0x96
v_bitop3_b32 v7, v7, v9, s67 bitop3:0x96
v_and_or_b32 v6, v7, -8, v6
v_cmp_ne_u32 vcc, 0, v6
s_cbranch_vccnz L_r1_%=
s_cmp_lg_u32 s55, 0
s_branch L_r1r_%=
L_g1_%=:
s_mov_b32 s75, 0
s_mov_b32 s38, 2048
v_mov_b32 v12, 2.0
s_cmp_lg_u32 s55, 0
s_branch L_r1r_%=
L_hot_%=:
.p2align 3
L_ent12_2_%=:
s_set_gpr_idx_on s43, gpr_idx(SRC1)
v_add_u32 v32, s44, v104
v_add_u32 v36, s44, v105
v_add_u32 v40, s44, v106
v_add_u32 v6, 0, v108
v_add_u32 v7, 0, v109
v_add_u32 v8, 0, v110
s_set_gpr_idx_off
v_pk_max_i16 v25, v6, s50
v_pk_min_i16 v25, v25, s52
v_mad_i32_i16 v33, v25, s54, v32 op_sel:[1,0,0,0]
v_mad_i32_i16 v34, v25, s53, v32
v_mad_i32_i16 v35, v25, s53, v33
v_pk_max_i16 v27, v7, s50
v_pk_min_i16 v27, v27, s52
v_mad_i32_i16 v37, v27, s54, v36 op_sel:[1,0,0,0]
v_mad_i32_i16 v38, v27, s53, v36
v_mad_i32_i16 v39, v27, s53, v37
v_pk_max_i16 v29, v8, s50
v_pk_min_i16 v29, v29, s52
v_mad_i32_i16 v41, v29, s54, v40 op_sel:[1,0,0,0]
v_mad_i32_i16 v42, v29, s53, v40
v_mad_i32_i16 v43, v29, s53, v41
v_and_b32_e64 v6, s56, v6
v_pk_add_f16 v25, v6, s51 op_sel:[1,0] neg_lo:[1,0]
v_pk_add_f16 v6, v6, s51 op_sel_hi:[0,1] neg_lo:[1,0]
PM 24,25,6
v_pk_mul_f16 v25, v25, v6 op_sel:[0,1]
v_and_b32_e64 v7, s56, v7
v_pk_add_f16 v27, v7, s51 op_sel:[1,0] neg_lo:[1,0]
v_pk_add_f16 v7, v7, s51 op_sel_hi:[0,1] neg_lo:[1,0]
PM 26,27,7
v_pk_mul_f16 v27, v27, v7 op_sel:[0,1]
v_and_b32_e64 v8, s56, v8
v_pk_add_f16 v29, v8, s51 op_sel:[1,0] neg_lo:[1,0]
v_pk_add_f16 v8, v8, s51 op_sel_hi:[0,1] neg_lo:[1,0]
PM 28,29,8
v_pk_mul_f16 v29, v29, v8 op_sel:[0,1]
s_waitcnt lgkmcnt(0)
s_barrier
RA 0,16,0
RA 4,16,1024
RA 8,16,2048
RV 72,32,0
RV 48,33,0
RV 52,34,0
RV 56,35,0
RV 76,36,0
RV 60,37,0
s_waitcnt lgkmcnt(2)
PM 72,72,24
PM 73,73,24
RV 64,38,0
RV 68,39,0
PM 74,74,24
PM 75,75,24
PH 72,48,24
RA 12,16,3072
RA 16,16,4096
PH 73,49,24
PH 74,50,24
PH 75,51,24
PL 72,52,25
PL 73,53,25
PL 74,54,25
PL 75,55,25
PH 72,56,25
PH 73,57,25
PH 74,58,25
PH 75,59,25
RV 80,40,0
RV 48,41,0
s_waitcnt lgkmcnt(4)
PM 76,76,26
PM 77,77,26
RV 52,42,0
RV 56,43,0
PM 78,78,26
PM 79,79,26
PH 76,60,26
RA 20,16,5120
PH 77,61,26
PH 78,62,26
PH 79,63,26
PL 76,64,27
PL 77,65,27
PL 78,66,27
PL 79,67,27
PH 76,68,27
PH 77,69,27
PH 78,70,27
PH 79,71,27
RV 88,32,16
RV 60,33,16
s_waitcnt lgkmcnt(3)
PM 80,80,28
PM 81,81,28
RV 64,34,16
RV 68,35,16
PM 82,82,28
PM 83,83,28
PH 80,48,28
PH 81,49,28
PH 82,50,28
PH 83,51,28
PL 80,52,29
PL 81,53,29
PL 82,54,29
PL 83,55,29
PH 80,56,29
PH 81,57,29
PH 82,58,29
PH 83,59,29
s_waitcnt lgkmcnt(0)
s_nop 0
v_mfma_f32_16x16x32_f16 v[160:163], a[0:3], v[72:75], 0
s_add_u32 m0, s41, 24576
RV 92,36,16
RV 48,37,16
v_mfma_f32_16x16x32_f16 v[164:167], a[4:7], v[72:75], 0
buffer_load_dwordx4 v15, s[36:39], s40 offen sc1 lds
s_waitcnt lgkmcnt(2)
PM 88,88,24
PM 89,89,24
v_mfma_f32_16x16x32_f16 v[168:171], a[8:11], v[72:75], 0
RV 52,38,16
RV 56,39,16
PM 90,90,24
PM 91,91,24
PH 88,60,24
v_mfma_f32_16x16x32_f16 v[172:175], a[12:15], v[72:75], 0
buffer_load_dwordx4 v15, s[36:39], s40 offen offset:1024 sc1 lds
RA 24,16,6144
RA 28,16,7168
PH 89,61,24
PH 90,62,24
PH 91,63,24
v_mfma_f32_16x16x32_f16 v[176:179], a[16:19], v[72:75], 0
RA 32,16,8192
PL 88,64,25
PL 89,65,25
v_mfma_f32_16x16x32_f16 v[180:183], a[20:23], v[72:75], 0
buffer_load_dwordx4 v15, s[36:39], s40 offen offset:2048 sc1 lds
PL 90,66,25
PL 91,67,25
PH 88,68,25
v_mfma_f32_16x16x32_f16 v[204:207], a[20:23], v[76:79], 0
PH 89,69,25
PH 90,70,25
PH 91,71,25
v_mfma_f32_16x16x32_f16 v[200:203], a[16:19], v[76:79], 0
RV 96,40,16
RV 60,41,16
s_waitcnt lgkmcnt(5)
PM 92,92,26
PM 93,93,26
v_mfma_f32_16x16x32_f16 v[196:199], a[12:15], v[76:79], 0
buffer_load_dwordx4 v15, s[36:39], s40 offen offset:3072 sc1 lds
RV 64,42,16
RV 68,43,16
PM 94,94,26
PM 95,95,26
PH 92,48,26
v_mfma_f32_16x16x32_f16 v[192:195], a[8:11], v[76:79], 0
s_addk_i32 m0, 4096
RA 36,16,9216
RA 40,16,10240
PH 93,49,26
PH 94,50,26
PH 95,51,26
v_mfma_f32_16x16x32_f16 v[188:191], a[4:7], v[76:79], 0
buffer_load_dwordx4 v45, s[36:39], s40 offen sc1 lds
RA 44,16,11264
PL 92,52,27
PL 93,53,27
v_mfma_f32_16x16x32_f16 v[184:187], a[0:3], v[76:79], 0
PL 94,54,27
PL 95,55,27
PH 92,56,27
v_mfma_f32_16x16x32_f16 v[208:211], a[0:3], v[80:83], 0
buffer_load_dwordx4 v45, s[36:39], s40 offen offset:1024 sc1 lds
PH 93,57,27
PH 94,58,27
PH 95,59,27
v_mfma_f32_16x16x32_f16 v[212:215], a[4:7], v[80:83], 0
s_addk_i32 s40, 24704
RV 72,32,32
RV 48,33,32
s_waitcnt lgkmcnt(5)
PM 96,96,28
PM 97,97,28
v_mfma_f32_16x16x32_f16 v[216:219], a[8:11], v[80:83], 0
RV 52,34,32
RV 56,35,32
PM 98,98,28
PM 99,99,28
v_mfma_f32_16x16x32_f16 v[220:223], a[12:15], v[80:83], 0
PH 96,60,28
PH 97,61,28
PH 98,62,28
PH 99,63,28
v_mfma_f32_16x16x32_f16 v[224:227], a[16:19], v[80:83], 0
PL 96,64,29
PL 97,65,29
PL 98,66,29
PL 99,67,29
v_mfma_f32_16x16x32_f16 v[228:231], a[20:23], v[80:83], 0
PH 96,68,29
PH 97,69,29
PH 98,70,29
PH 99,71,29
M 160,24,88
RV 76,36,32
RV 60,37,32
s_waitcnt lgkmcnt(2)
PM 72,72,24
PM 73,73,24
M 164,28,88
RV 64,38,32
RV 68,39,32
PM 74,74,24
PM 75,75,24
PH 72,48,24
M 168,32,88
RA 0,16,12288
RA 4,16,13312
PH 73,49,24
PH 74,50,24
PH 75,51,24
M 172,36,88
RA 8,16,14336
PL 72,52,25
PL 73,53,25
M 176,40,88
PL 74,54,25
PL 75,55,25
PH 72,56,25
M 180,44,88
PH 73,57,25
PH 74,58,25
PH 75,59,25
M 204,44,92
RV 80,40,32
RV 48,41,32
s_waitcnt lgkmcnt(5)
PM 76,76,26
PM 77,77,26
M 200,40,92
RV 52,42,32
RV 56,43,32
PM 78,78,26
PM 79,79,26
PH 76,60,26
M 196,36,92
RA 12,16,15360
RA 16,16,16384
PH 77,61,26
PH 78,62,26
PH 79,63,26
M 192,32,92
RA 20,16,17408
PL 76,64,27
PL 77,65,27
M 188,28,92
PL 78,66,27
PL 79,67,27
PH 76,68,27
M 184,24,92
PH 77,69,27
PH 78,70,27
PH 79,71,27
M 208,24,96
RV 88,32,48
RV 60,33,48
s_waitcnt lgkmcnt(5)
PM 80,80,28
PM 81,81,28
M 212,28,96
RV 64,34,48
RV 68,35,48
PM 82,82,28
M 216,32,96
PM 83,83,28
PH 80,48,28
PH 81,49,28
M 220,36,96
PH 82,50,28
PH 83,51,28
PL 80,52,29
M 224,40,96
PL 81,53,29
PL 82,54,29
PL 83,55,29
PH 80,56,29
M 228,44,96
PH 81,57,29
PH 82,58,29
PH 83,59,29
M 160,0,72
RV 92,36,48
RV 48,37,48
s_waitcnt lgkmcnt(2)
PM 88,88,24
PM 89,89,24
M 164,4,72
RV 52,38,48
RV 56,39,48
PM 90,90,24
PM 91,91,24
PH 88,60,24
M 168,8,72
RA 24,16,18432
RA 28,16,19456
PH 89,61,24
PH 90,62,24
PH 91,63,24
M 172,12,72
RA 32,16,20480
PL 88,64,25
PL 89,65,25
M 176,16,72
PL 90,66,25
PL 91,67,25
PH 88,68,25
M 180,20,72
PH 89,69,25
PH 90,70,25
PH 91,71,25
M 204,20,76
RV 96,40,48
RV 60,41,48
s_waitcnt lgkmcnt(5)
PM 92,92,26
PM 93,93,26
M 200,16,76
RV 64,42,48
RV 68,43,48
PM 94,94,26
PM 95,95,26
PH 92,48,26
M 196,12,76
RA 36,16,21504
RA 40,16,22528
PH 93,49,26
PH 94,50,26
PH 95,51,26
M 192,8,76
RA 44,16,23552
PL 92,52,27
PL 93,53,27
M 188,4,76
PL 94,54,27
PL 95,55,27
PH 92,56,27
v_cmp_nge_f32 vcc, 1.0, v12
M 184,0,76
PH 93,57,27
PH 94,58,27
PH 95,59,27
v_and_b32 v13, 0x7ff87ff8, v13
M 208,0,80
RV 72,32,64
RV 48,33,64
s_waitcnt lgkmcnt(5)
PM 96,96,28
PM 97,97,28
s_lshl2_add_u32 s11, s10, 162304
M 212,4,80
RV 52,34,64
RV 56,35,64
PM 98,98,28
v_addc_co_u32 v13, vcc, 0, v13, vcc
M 216,8,80
buffer_load_dwordx4 v[6:9], v15, s[36:39], 0 offen sc1
PM 99,99,28
PH 96,60,28
PH 97,61,28
v_add_u32 v12, s11, v15
M 220,12,80
s_waitcnt vmcnt(1) lgkmcnt(4)
PH 98,62,28
PH 99,63,28
PL 96,64,29
M 224,16,80
buffer_load_dwordx4 v[100:103], v47, s[36:39], 0 offen offset:1024 sc1
s_barrier
PL 97,65,29
PL 98,66,29
PL 99,67,29
M 228,20,80
PH 96,68,29
PH 97,69,29
PH 98,70,29
PH 99,71,29
M 160,24,88
RV 76,36,64
RV 60,37,64
M 164,28,88
ds_write_b32 v12, v13
s_waitcnt lgkmcnt(2)
PM 72,72,24
PM 73,73,24
M 168,32,88
RV 64,38,64
RV 68,39,64
PM 74,74,24
PM 75,75,24
PH 72,48,24
M 172,36,88
RA 0,16,24576
RA 4,16,25600
PH 73,49,24
PH 74,50,24
PH 75,51,24
M 176,40,88
RA 8,16,26624
PL 72,52,25
PL 73,53,25
M 180,44,88
PL 74,54,25
PL 75,55,25
PH 72,56,25
M 204,44,92
PH 73,57,25
PH 74,58,25
PH 75,59,25
M 200,40,92
RV 80,40,64
RV 48,41,64
s_waitcnt lgkmcnt(5)
PM 76,76,26
PM 77,77,26
M 196,36,92
RV 52,42,64
RV 56,43,64
PM 78,78,26
PM 79,79,26
PH 76,60,26
M 192,32,92
RA 12,16,27648
RA 16,16,28672
PH 77,61,26
PH 78,62,26
PH 79,63,26
M 188,28,92
RA 20,16,29696
PL 76,64,27
PL 77,65,27
M 184,24,92
PL 78,66,27
PL 79,67,27
PH 76,68,27
M 208,24,96
PH 77,69,27
PH 78,70,27
PH 79,71,27
M 212,28,96
RV 88,32,80
RV 60,33,80
s_waitcnt lgkmcnt(5)
PM 80,80,28
PM 81,81,28
M 216,32,96
RV 64,34,80
RV 68,35,80
PM 82,82,28
PM 83,83,28
M 220,36,96
PH 80,48,28
PH 81,49,28
PH 82,50,28
PH 83,51,28
M 224,40,96
PL 80,52,29
PL 81,53,29
PL 82,54,29
PL 83,55,29
M 228,44,96
PH 80,56,29
PH 81,57,29
PH 82,58,29
PH 83,59,29
M 160,0,72
RV 92,36,80
RV 48,37,80
s_waitcnt vmcnt(0)
v_bitop3_b32 v6, v6, v8, s66 bitop3:0x96
M 164,4,72
s_waitcnt lgkmcnt(2)
PM 88,88,24
PM 89,89,24
v_bitop3_b32 v100, v100, v102, s66 bitop3:0x96
v_bitop3_b32 v7, v7, v9, s67 bitop3:0x96
M 168,8,72
RV 52,38,80
RV 56,39,80
PM 90,90,24
PM 91,91,24
PH 88,60,24
v_bitop3_b32 v101, v101, v103, s67 bitop3:0x96
v_or_b32_e64 v6, v6, v100
M 172,12,72
RA 24,16,30720
RA 28,16,31744
PH 89,61,24
PH 90,62,24
PH 91,63,24
v_or_b32_e64 v5, v7, v101
v_cmp_ne_u32 vcc, 0, v6
M 176,16,72
RA 32,16,32768
PL 88,64,25
PL 89,65,25
v_cmp_lt_u32 s[30:31], 7, v5
s_or_b64 vcc, vcc, s[30:31]
M 180,20,72
PL 90,66,25
PL 91,67,25
PH 88,68,25
s_cbranch_scc1 L_ag12_2_%=
L_ok12_2_%=:
M 204,20,76
s_add_u32 m0, s41, 49152
PH 89,69,25
PH 90,70,25
PH 91,71,25
M 200,16,76
buffer_load_dwordx4 v15, s[36:39], s40 offen sc1 lds
RV 96,40,80
RV 60,41,80
s_waitcnt lgkmcnt(5)
PM 92,92,26
PM 93,93,26
M 196,12,76
RV 64,42,80
RV 68,43,80
PM 94,94,26
PM 95,95,26
PH 92,48,26
M 192,8,76
buffer_load_dwordx4 v15, s[36:39], s40 offen offset:1024 sc1 lds
RA 36,16,33792
RA 40,16,34816
PH 93,49,26
PH 94,50,26
PH 95,51,26
M 188,4,76
RA 44,16,35840
PL 92,52,27
PL 93,53,27
M 184,0,76
buffer_load_dwordx4 v15, s[36:39], s40 offen offset:2048 sc1 lds
PL 94,54,27
PL 95,55,27
PH 92,56,27
M 208,0,80
PH 93,57,27
PH 94,58,27
PH 95,59,27
M 212,4,80
RV 72,32,96
RV 48,33,96
s_waitcnt lgkmcnt(5)
PM 96,96,28
PM 97,97,28
M 216,8,80
buffer_load_dwordx4 v15, s[36:39], s40 offen offset:3072 sc1 lds
RV 52,34,96
RV 56,35,96
PM 98,98,28
PM 99,99,28
M 220,12,80
s_addk_i32 m0, 4096
PH 96,60,28
PH 97,61,28
PH 98,62,28
PH 99,63,28
M 224,16,80
buffer_load_dwordx4 v45, s[36:39], s40 offen sc1 lds
PL 96,64,29
PL 97,65,29
PL 98,66,29
PL 99,67,29
M 228,20,80
PH 96,68,29
PH 97,69,29
PH 98,70,29
PH 99,71,29
M 160,24,88
buffer_load_dwordx4 v45, s[36:39], s40 offen offset:1024 sc1 lds
RV 76,36,96
RV 60,37,96
s_waitcnt lgkmcnt(2)
PM 72,72,24
PM 73,73,24
M 164,28,88
s_addk_i32 s40, 24704
RV 64,38,96
RV 68,39,96
PM 74,74,24
PM 75,75,24
PH 72,48,24
M 168,32,88
RA 0,16,36864
RA 4,16,37888
PH 73,49,24
PH 74,50,24
PH 75,51,24
M 172,36,88
RA 8,16,38912
PL 72,52,25
PL 73,53,25
M 176,40,88
s_add_u32 m0, s41, 0
PL 74,54,25
PL 75,55,25
PH 72,56,25
M 180,44,88
buffer_load_dwordx4 v15, s[36:39], s40 offen sc1 lds
PH 73,57,25
PH 74,58,25
PH 75,59,25
M 204,44,92
RV 80,40,96
RV 48,41,96
s_waitcnt lgkmcnt(5)
PM 76,76,26
PM 77,77,26
M 200,40,92
RV 52,42,96
RV 56,43,96
PM 78,78,26
PM 79,79,26
PH 76,60,26
M 196,36,92
RA 12,16,39936
RA 16,16,40960
PH 77,61,26
PH 78,62,26
PH 79,63,26
M 192,32,92
buffer_load_dwordx4 v15, s[36:39], s40 offen offset:1024 sc1 lds
RA 20,16,41984
PL 76,64,27
PL 77,65,27
M 188,28,92
PL 78,66,27
PL 79,67,27
PH 76,68,27
M 184,24,92
PH 77,69,27
PH 78,70,27
PH 79,71,27
M 208,24,96
RV 88,32,112
RV 60,33,112
s_waitcnt lgkmcnt(5)
PM 80,80,28
PM 81,81,28
M 212,28,96
RV 64,34,112
RV 68,35,112
PM 82,82,28
M 216,32,96
buffer_load_dwordx4 v15, s[36:39], s40 offen offset:2048 sc1 lds
PM 83,83,28
PH 80,48,28
PH 81,49,28
M 220,36,96
PH 82,50,28
PH 83,51,28
PL 80,52,29
M 224,40,96
PL 81,53,29
PL 82,54,29
PL 83,55,29
PH 80,56,29
M 228,44,96
PH 81,57,29
PH 82,58,29
PH 83,59,29
M 160,0,72
buffer_load_dwordx4 v15, s[36:39], s40 offen offset:3072 sc1 lds
RV 92,36,112
RV 48,37,112
s_waitcnt lgkmcnt(2)
PM 88,88,24
PM 89,89,24
M 164,4,72
s_add_u32 m0, m0, 4096
RV 52,38,112
RV 56,39,112
PM 90,90,24
PM 91,91,24
PH 88,60,24
M 168,8,72
RA 24,16,43008
RA 28,16,44032
PH 89,61,24
PH 90,62,24
PH 91,63,24
M 172,12,72
RA 32,16,45056
PL 88,64,25
PL 89,65,25
M 176,16,72
PL 90,66,25
PL 91,67,25
PH 88,68,25
M 180,20,72
buffer_load_dwordx4 v45, s[36:39], s40 offen sc1 lds
PH 89,69,25
PH 90,70,25
PH 91,71,25
M 204,20,76
RV 96,40,112
RV 60,41,112
s_waitcnt lgkmcnt(5)
PM 92,92,26
PM 93,93,26
M 200,16,76
RV 64,42,112
RV 68,43,112
PM 94,94,26
PM 95,95,26
PH 92,48,26
M 196,12,76
RA 36,16,46080
RA 40,16,47104
PH 93,49,26
PH 94,50,26
PH 95,51,26
M 192,8,76
buffer_load_dwordx4 v45, s[36:39], s40 offen offset:1024 sc1 lds
RA 44,16,48128
PL 92,52,27
PL 93,53,27
M 188,4,76
s_add_u32 s40, s40, 24704
PL 94,54,27
PL 95,55,27
PH 92,56,27
M 184,0,76
PH 93,57,27
PH 94,58,27
PH 95,59,27
M 208,0,80
RV 72,32,128
RV 48,33,128
s_waitcnt lgkmcnt(5)
PM 96,96,28
PM 97,97,28
M 212,4,80
RV 52,34,128
RV 56,35,128
PM 98,98,28
M 216,8,80
PM 99,99,28
PH 96,60,28
PH 97,61,28
M 220,12,80
s_waitcnt vmcnt(6) lgkmcnt(4)
PH 98,62,28
PH 99,63,28
PL 96,64,29
M 224,16,80
s_barrier
PL 97,65,29
PL 98,66,29
PL 99,67,29
M 228,20,80
PH 96,68,29
PH 97,69,29
PH 98,70,29
PH 99,71,29
M 160,24,88
RV 76,36,128
RV 60,37,128
M 164,28,88
s_branch L_in12_2_%=
L_ag12_2_%=:
s_call_b64 s[72:73], L_retry_%=
s_branch L_ok12_2_%=
.p2align 3
L_in12_2_%=:
s_waitcnt lgkmcnt(2)
PM 72,72,24
PM 73,73,24
M 168,32,88
RV 64,38,128
RV 68,39,128
PM 74,74,24
PM 75,75,24
PH 72,48,24
M 172,36,88
RA 0,20,0
RA 4,20,1024
PH 73,49,24
PH 74,50,24
PH 75,51,24
M 176,40,88
RA 8,20,2048
PL 72,52,25
PL 73,53,25
M 180,44,88
PL 74,54,25
PL 75,55,25
PH 72,56,25
M 204,44,92
PH 73,57,25
PH 74,58,25
PH 75,59,25
M 200,40,92
RV 80,40,128
RV 48,41,128
s_waitcnt lgkmcnt(5)
PM 76,76,26
PM 77,77,26
M 196,36,92
RV 52,42,128
RV 56,43,128
PM 78,78,26
PM 79,79,26
PH 76,60,26
M 192,32,92
RA 12,20,3072
RA 16,20,4096
PH 77,61,26
PH 78,62,26
PH 79,63,26
M 188,28,92
RA 20,20,5120
PL 76,64,27
PL 77,65,27
M 184,24,92
s_add_u32 m0, s41, 24576
PL 78,66,27
PL 79,67,27
PH 76,68,27
M 208,24,96
buffer_load_dwordx4 v15, s[36:39], s40 offen sc1 lds
PH 77,69,27
PH 78,70,27
PH 79,71,27
M 212,28,96
RV 88,32,144
RV 60,33,144
s_waitcnt lgkmcnt(5)
PM 80,80,28
PM 81,81,28
M 216,32,96
RV 64,34,144
RV 68,35,144
PM 82,82,28
PM 83,83,28
M 220,36,96
PH 80,48,28
PH 81,49,28
PH 82,50,28
PH 83,51,28
M 224,40,96
PL 80,52,29
PL 81,53,29
PL 82,54,29
PL 83,55,29
M 228,44,96
PH 80,56,29
PH 81,57,29
PH 82,58,29
PH 83,59,29
M 160,0,72
RV 92,36,144
RV 48,37,144
M 164,4,72
s_waitcnt lgkmcnt(2)
PM 88,88,24
PM 89,89,24
M 168,8,72
RV 52,38,144
RV 56,39,144
PM 90,90,24
PM 91,91,24
PH 88,60,24
M 172,12,72
RA 24,20,6144
RA 28,20,7168
PH 89,61,24
PH 90,62,24
PH 91,63,24
M 176,16,72
RA 32,20,8192
PL 88,64,25
PL 89,65,25
M 180,20,72
PL 90,66,25
PL 91,67,25
PH 88,68,25
M 204,20,76
buffer_load_dwordx4 v15, s[36:39], s40 offen offset:1024 sc1 lds
PH 89,69,25
PH 90,70,25
PH 91,71,25
M 200,16,76
RV 96,40,144
RV 60,41,144
s_waitcnt lgkmcnt(5)
PM 92,92,26
PM 93,93,26
M 196,12,76
RV 64,42,144
RV 68,43,144
PM 94,94,26
PM 95,95,26
PH 92,48,26
M 192,8,76
RA 36,20,9216
RA 40,20,10240
PH 93,49,26
PH 94,50,26
PH 95,51,26
M 188,4,76
RA 44,20,11264
PL 92,52,27
PL 93,53,27
M 184,0,76
PL 94,54,27
PL 95,55,27
PH 92,56,27
M 208,0,80
buffer_load_dwordx4 v15, s[36:39], s40 offen offset:2048 sc1 lds
PH 93,57,27
PH 94,58,27
PH 95,59,27
M 212,4,80
RV 72,32,160
RV 48,33,160
s_waitcnt lgkmcnt(5)
PM 96,96,28
PM 97,97,28
M 216,8,80
RV 52,34,160
RV 56,35,160
PM 98,98,28
PM 99,99,28
M 220,12,80
PH 96,60,28
PH 97,61,28
PH 98,62,28
PH 99,63,28
M 224,16,80
PL 96,64,29
PL 97,65,29
PL 98,66,29
PL 99,67,29
M 228,20,80
PH 96,68,29
PH 97,69,29
PH 98,70,29
PH 99,71,29
M 160,24,88
RV 76,36,160
RV 60,37,160
s_waitcnt lgkmcnt(2)
PM 72,72,24
PM 73,73,24
M 164,28,88
RV 64,38,160
RV 68,39,160
PM 74,74,24
PM 75,75,24
PH 72,48,24
M 168,32,88
RA 0,20,12288
RA 4,20,13312
PH 73,49,24
PH 74,50,24
PH 75,51,24
M 172,36,88
RA 8,20,14336
PL 72,52,25
PL 73,53,25
M 176,40,88
PL 74,54,25
PL 75,55,25
PH 72,56,25
M 180,44,88
buffer_load_dwordx4 v15, s[36:39], s40 offen offset:3072 sc1 lds
PH 73,57,25
PH 74,58,25
PH 75,59,25
M 204,44,92
RV 80,40,160
RV 48,41,160
s_waitcnt lgkmcnt(5)
PM 76,76,26
PM 77,77,26
M 200,40,92
RV 52,42,160
RV 56,43,160
PM 78,78,26
PM 79,79,26
PH 76,60,26
M 196,36,92
RA 12,20,15360
RA 16,20,16384
PH 77,61,26
PH 78,62,26
PH 79,63,26
M 192,32,92
RA 20,20,17408
PL 76,64,27
PL 77,65,27
M 188,28,92
PL 78,66,27
PL 79,67,27
PH 76,68,27
M 184,24,92
PH 77,69,27
PH 78,70,27
PH 79,71,27
)ASM" R"ASM(
M 208,24,96
RV 88,32,176
RV 60,33,176
s_waitcnt lgkmcnt(5)
PM 80,80,28
PM 81,81,28
M 212,28,96
RV 64,34,176
RV 68,35,176
PM 82,82,28
M 216,32,96
PM 83,83,28
PH 80,48,28
PH 81,49,28
M 220,36,96
PH 82,50,28
PH 83,51,28
PL 80,52,29
M 224,40,96
s_addk_i32 m0, 4096
PL 81,53,29
PL 82,54,29
PL 83,55,29
PH 80,56,29
M 228,44,96
buffer_load_dwordx4 v45, s[36:39], s40 offen sc1 lds
PH 81,57,29
PH 82,58,29
PH 83,59,29
M 160,0,72
RV 92,36,176
RV 48,37,176
s_waitcnt lgkmcnt(2)
PM 88,88,24
PM 89,89,24
M 164,4,72
RV 52,38,176
RV 56,39,176
PM 90,90,24
PM 91,91,24
PH 88,60,24
M 168,8,72
RA 24,20,18432
RA 28,20,19456
PH 89,61,24
PH 90,62,24
PH 91,63,24
M 172,12,72
RA 32,20,20480
PL 88,64,25
PL 89,65,25
M 176,16,72
PL 90,66,25
PL 91,67,25
PH 88,68,25
M 180,20,72
buffer_load_dwordx4 v45, s[36:39], s40 offen offset:1024 sc1 lds
PH 89,69,25
PH 90,70,25
PH 91,71,25
M 204,20,76
RV 96,40,176
RV 60,41,176
s_waitcnt lgkmcnt(5)
PM 92,92,26
PM 93,93,26
M 200,16,76
RV 64,42,176
RV 68,43,176
PM 94,94,26
PM 95,95,26
PH 92,48,26
M 196,12,76
RA 36,20,21504
RA 40,20,22528
PH 93,49,26
PH 94,50,26
PH 95,51,26
M 192,8,76
RA 44,20,23552
PL 92,52,27
PL 93,53,27
PL 94,54,27
M 188,4,76
s_add_u32 s40, s40, 24704
PL 95,55,27
PH 92,56,27
PH 93,57,27
M 184,0,76
PH 94,58,27
PH 95,59,27
s_waitcnt lgkmcnt(3)
PM 96,96,28
M 208,0,80
PM 97,97,28
PM 98,98,28
PM 99,99,28
PH 96,60,28
M 212,4,80
PH 97,61,28
PH 98,62,28
PH 99,63,28
PL 96,64,29
M 216,8,80
PL 97,65,29
PL 98,66,29
PL 99,67,29
PH 96,68,29
M 220,12,80
s_waitcnt vmcnt(6) lgkmcnt(0)
PH 97,69,29
s_lshr_b32 s42, s42, 4
s_add_u32 s43, s43, 8
M 224,16,80
s_barrier
s_and_b32 s55, s42, 15
s_cmp_eq_u32 s55, 3
PH 98,70,29
PH 99,71,29
M 228,20,80
s_cbranch_scc0 L_disp12_2_%=
L_body12_2_%=:
M 160,24,88
L_bodz12_2_%=:
RA 0,16,0
RA 4,16,1024
s_set_gpr_idx_on s43, gpr_idx(SRC1)
v_add_u32 v32, s44, v104
v_add_u32 v36, s44, v105
v_add_u32 v40, s44, v106
v_add_u32 v6, 0, v108
v_add_u32 v7, 0, v109
v_add_u32 v8, 0, v110
s_set_gpr_idx_off
M 164,28,88
RA 8,16,2048
v_pk_max_i16 v25, v6, s50
v_pk_min_i16 v25, v25, s52
v_mad_i32_i16 v33, v25, s54, v32 op_sel:[1,0,0,0]
v_mad_i32_i16 v34, v25, s53, v32
v_mad_i32_i16 v35, v25, s53, v33
M 168,32,88
RV 72,32,0
RV 48,33,0
RV 52,34,0
RV 56,35,0
v_pk_max_i16 v27, v7, s50
M 172,36,88
v_pk_min_i16 v27, v27, s52
v_mad_i32_i16 v37, v27, s54, v36 op_sel:[1,0,0,0]
v_mad_i32_i16 v38, v27, s53, v36
v_mad_i32_i16 v39, v27, s53, v37
v_pk_max_i16 v29, v8, s50
v_pk_min_i16 v29, v29, s52
M 176,40,88
v_mad_i32_i16 v41, v29, s54, v40 op_sel:[1,0,0,0]
v_mad_i32_i16 v42, v29, s53, v40
v_mad_i32_i16 v43, v29, s53, v41
v_and_b32_e64 v6, s56, v6
v_pk_add_f16 v25, v6, s51 op_sel:[1,0] neg_lo:[1,0]
v_pk_add_f16 v6, v6, s51 op_sel_hi:[0,1] neg_lo:[1,0]
M 180,44,88
PM 24,25,6
v_pk_mul_f16 v25, v25, v6 op_sel:[0,1]
v_and_b32_e64 v7, s56, v7
v_pk_add_f16 v27, v7, s51 op_sel:[1,0] neg_lo:[1,0]
v_pk_add_f16 v7, v7, s51 op_sel_hi:[0,1] neg_lo:[1,0]
PM 26,27,7
M 204,44,92
v_pk_mul_f16 v27, v27, v7 op_sel:[0,1]
v_and_b32 v8, s56, v8
v_pk_add_f16 v29, v8, s51 op_sel:[1,0] neg_lo:[1,0]
v_pk_add_f16 v8, v8, s51 op_sel_hi:[0,1] neg_lo:[1,0]
PM 28,29,8
v_pk_mul_f16 v29, v29, v8 op_sel:[0,1]
M 200,40,92
RV 76,36,0
RV 60,37,0
s_waitcnt lgkmcnt(2)
PM 72,72,24
PM 73,73,24
M 196,36,92
RV 64,38,0
RV 68,39,0
PM 74,74,24
PM 75,75,24
PH 72,48,24
M 192,32,92
RA 12,16,3072
RA 16,16,4096
PH 73,49,24
PH 74,50,24
PH 75,51,24
PL 72,52,25
PL 73,53,25
M 188,28,92
PL 74,54,25
PL 75,55,25
PH 72,56,25
PH 73,57,25
PH 74,58,25
PH 75,59,25
M 184,24,92
RV 80,40,0
RV 48,41,0
s_waitcnt lgkmcnt(4)
PM 76,76,26
PM 77,77,26
M 208,24,96
RV 52,42,0
RV 56,43,0
PM 78,78,26
PM 79,79,26
PH 76,60,26
M 212,28,96
s_add_u32 m0, s41, 49152
RA 20,16,5120
PH 77,61,26
PH 78,62,26
PH 79,63,26
PL 76,64,27
PL 77,65,27
PL 78,66,27
PL 79,67,27
PH 76,68,27
M 216,32,96
buffer_load_dwordx4 v15, s[36:39], s40 offen sc1 lds
PH 77,69,27
PH 78,70,27
PH 79,71,27
M 220,36,96
RV 88,32,16
RV 60,33,16
s_waitcnt lgkmcnt(3)
PM 80,80,28
PM 81,81,28
M 224,40,96
RV 64,34,16
RV 68,35,16
PM 82,82,28
PM 83,83,28
PH 80,48,28
PH 81,49,28
PH 82,50,28
PH 83,51,28
M 228,44,96
PL 80,52,29
PL 81,53,29
PL 82,54,29
PL 83,55,29
PH 80,56,29
PH 81,57,29
PH 82,58,29
PH 83,59,29
M 160,0,72
RV 92,36,16
RV 48,37,16
M 164,4,72
s_waitcnt lgkmcnt(2)
PM 88,88,24
PM 89,89,24
M 168,8,72
RV 52,38,16
RV 56,39,16
PM 90,90,24
PM 91,91,24
PH 88,60,24
M 172,12,72
RA 24,16,6144
RA 28,16,7168
PH 89,61,24
PH 90,62,24
PH 91,63,24
M 176,16,72
RA 32,16,8192
PL 88,64,25
PL 89,65,25
M 180,20,72
PL 90,66,25
PL 91,67,25
PH 88,68,25
M 204,20,76
buffer_load_dwordx4 v15, s[36:39], s40 offen offset:1024 sc1 lds
PH 89,69,25
PH 90,70,25
PH 91,71,25
M 200,16,76
RV 96,40,16
RV 60,41,16
s_waitcnt lgkmcnt(5)
PM 92,92,26
PM 93,93,26
M 196,12,76
RV 64,42,16
RV 68,43,16
PM 94,94,26
PM 95,95,26
PH 92,48,26
M 192,8,76
RA 36,16,9216
RA 40,16,10240
PH 93,49,26
PH 94,50,26
PH 95,51,26
M 188,4,76
RA 44,16,11264
PL 92,52,27
PL 93,53,27
M 184,0,76
PL 94,54,27
PL 95,55,27
PH 92,56,27
M 208,0,80
buffer_load_dwordx4 v15, s[36:39], s40 offen offset:2048 sc1 lds
PH 93,57,27
PH 94,58,27
PH 95,59,27
M 212,4,80
RV 72,32,32
RV 48,33,32
s_waitcnt lgkmcnt(5)
PM 96,96,28
PM 97,97,28
M 216,8,80
RV 52,34,32
RV 56,35,32
PM 98,98,28
PM 99,99,28
M 220,12,80
PH 96,60,28
PH 97,61,28
PH 98,62,28
PH 99,63,28
M 224,16,80
PL 96,64,29
PL 97,65,29
PL 98,66,29
PL 99,67,29
M 228,20,80
PH 96,68,29
PH 97,69,29
PH 98,70,29
PH 99,71,29
M 160,24,88
RV 76,36,32
RV 60,37,32
s_waitcnt lgkmcnt(2)
PM 72,72,24
PM 73,73,24
M 164,28,88
RV 64,38,32
RV 68,39,32
PM 74,74,24
PM 75,75,24
PH 72,48,24
M 168,32,88
RA 0,16,12288
RA 4,16,13312
PH 73,49,24
PH 74,50,24
PH 75,51,24
M 172,36,88
RA 8,16,14336
PL 72,52,25
PL 73,53,25
M 176,40,88
PL 74,54,25
PL 75,55,25
PH 72,56,25
M 180,44,88
buffer_load_dwordx4 v15, s[36:39], s40 offen offset:3072 sc1 lds
PH 73,57,25
PH 74,58,25
PH 75,59,25
M 204,44,92
RV 80,40,32
RV 48,41,32
s_waitcnt lgkmcnt(5)
PM 76,76,26
PM 77,77,26
M 200,40,92
RV 52,42,32
RV 56,43,32
PM 78,78,26
PM 79,79,26
PH 76,60,26
M 196,36,92
RA 12,16,15360
RA 16,16,16384
PH 77,61,26
PH 78,62,26
PH 79,63,26
M 192,32,92
RA 20,16,17408
PL 76,64,27
PL 77,65,27
M 188,28,92
PL 78,66,27
PL 79,67,27
PH 76,68,27
M 184,24,92
PH 77,69,27
PH 78,70,27
PH 79,71,27
M 208,24,96
RV 88,32,48
RV 60,33,48
s_waitcnt lgkmcnt(5)
PM 80,80,28
PM 81,81,28
M 212,28,96
RV 64,34,48
RV 68,35,48
PM 82,82,28
M 216,32,96
PM 83,83,28
PH 80,48,28
PH 81,49,28
M 220,36,96
PH 82,50,28
PH 83,51,28
PL 80,52,29
M 224,40,96
s_add_u32 m0, m0, 4096
PL 81,53,29
PL 82,54,29
PL 83,55,29
PH 80,56,29
M 228,44,96
buffer_load_dwordx4 v45, s[36:39], s40 offen sc1 lds
PH 81,57,29
PH 82,58,29
PH 83,59,29
M 160,0,72
RV 92,36,48
RV 48,37,48
s_waitcnt lgkmcnt(2)
PM 88,88,24
PM 89,89,24
M 164,4,72
RV 52,38,48
RV 56,39,48
PM 90,90,24
PM 91,91,24
PH 88,60,24
M 168,8,72
RA 24,16,18432
RA 28,16,19456
PH 89,61,24
PH 90,62,24
PH 91,63,24
M 172,12,72
RA 32,16,20480
PL 88,64,25
PL 89,65,25
M 176,16,72
PL 90,66,25
PL 91,67,25
PH 88,68,25
M 180,20,72
buffer_load_dwordx4 v45, s[36:39], s40 offen offset:1024 sc1 lds
PH 89,69,25
PH 90,70,25
PH 91,71,25
M 204,20,76
RV 96,40,48
RV 60,41,48
s_waitcnt lgkmcnt(5)
PM 92,92,26
PM 93,93,26
M 200,16,76
RV 64,42,48
RV 68,43,48
PM 94,94,26
PM 95,95,26
PH 92,48,26
M 196,12,76
RA 36,16,21504
RA 40,16,22528
PH 93,49,26
PH 94,50,26
PH 95,51,26
M 192,8,76
RA 44,16,23552
PL 92,52,27
PL 93,53,27
M 188,4,76
PL 94,54,27
PL 95,55,27
PH 92,56,27
M 184,0,76
PH 93,57,27
PH 94,58,27
PH 95,59,27
M 208,0,80
RV 72,32,64
RV 48,33,64
s_waitcnt lgkmcnt(5)
PM 96,96,28
PM 97,97,28
M 212,4,80
RV 52,34,64
RV 56,35,64
PM 98,98,28
M 216,8,80
s_addk_i32 s40, 24704
PM 99,99,28
PH 96,60,28
PH 97,61,28
M 220,12,80
s_waitcnt vmcnt(6) lgkmcnt(4)
PH 98,62,28
PH 99,63,28
PL 96,64,29
M 224,16,80
s_barrier
PL 97,65,29
PL 98,66,29
PL 99,67,29
M 228,20,80
PH 96,68,29
PH 97,69,29
PH 98,70,29
PH 99,71,29
M 160,24,88
RV 76,36,64
RV 60,37,64
M 164,28,88
s_waitcnt lgkmcnt(2)
PM 72,72,24
PM 73,73,24
M 168,32,88
RV 64,38,64
RV 68,39,64
PM 74,74,24
PM 75,75,24
PH 72,48,24
M 172,36,88
RA 0,16,24576
RA 4,16,25600
PH 73,49,24
PH 74,50,24
PH 75,51,24
M 176,40,88
RA 8,16,26624
PL 72,52,25
PL 73,53,25
M 180,44,88
PL 74,54,25
PL 75,55,25
PH 72,56,25
M 204,44,92
PH 73,57,25
PH 74,58,25
PH 75,59,25
M 200,40,92
RV 80,40,64
RV 48,41,64
s_waitcnt lgkmcnt(5)
PM 76,76,26
PM 77,77,26
M 196,36,92
RV 52,42,64
RV 56,43,64
PM 78,78,26
PM 79,79,26
PH 76,60,26
M 192,32,92
RA 12,16,27648
RA 16,16,28672
PH 77,61,26
PH 78,62,26
PH 79,63,26
M 188,28,92
RA 20,16,29696
PL 76,64,27
PL 77,65,27
M 184,24,92
s_mov_b32 m0, s41
PL 78,66,27
PL 79,67,27
PH 76,68,27
M 208,24,96
buffer_load_dwordx4 v15, s[36:39], s40 offen sc1 lds
PH 77,69,27
PH 78,70,27
PH 79,71,27
M 212,28,96
RV 88,32,80
RV 60,33,80
s_waitcnt lgkmcnt(5)
PM 80,80,28
PM 81,81,28
M 216,32,96
RV 64,34,80
RV 68,35,80
PM 82,82,28
PM 83,83,28
M 220,36,96
PH 80,48,28
PH 81,49,28
PH 82,50,28
PH 83,51,28
M 224,40,96
PL 80,52,29
PL 81,53,29
PL 82,54,29
PL 83,55,29
M 228,44,96
PH 80,56,29
PH 81,57,29
PH 82,58,29
PH 83,59,29
M 160,0,72
RV 92,36,80
RV 48,37,80
M 164,4,72
s_waitcnt lgkmcnt(2)
PM 88,88,24
PM 89,89,24
M 168,8,72
RV 52,38,80
RV 56,39,80
PM 90,90,24
PM 91,91,24
PH 88,60,24
M 172,12,72
RA 24,16,30720
RA 28,16,31744
PH 89,61,24
PH 90,62,24
PH 91,63,24
M 176,16,72
RA 32,16,32768
PL 88,64,25
PL 89,65,25
M 180,20,72
PL 90,66,25
PL 91,67,25
PH 88,68,25
M 204,20,76
buffer_load_dwordx4 v15, s[36:39], s40 offen offset:1024 sc1 lds
PH 89,69,25
PH 90,70,25
PH 91,71,25
M 200,16,76
RV 96,40,80
RV 60,41,80
s_waitcnt lgkmcnt(5)
PM 92,92,26
PM 93,93,26
M 196,12,76
RV 64,42,80
RV 68,43,80
PM 94,94,26
PM 95,95,26
PH 92,48,26
M 192,8,76
RA 36,16,33792
RA 40,16,34816
PH 93,49,26
PH 94,50,26
PH 95,51,26
M 188,4,76
RA 44,16,35840
PL 92,52,27
PL 93,53,27
M 184,0,76
PL 94,54,27
PL 95,55,27
PH 92,56,27
M 208,0,80
buffer_load_dwordx4 v15, s[36:39], s40 offen offset:2048 sc1 lds
PH 93,57,27
PH 94,58,27
PH 95,59,27
M 212,4,80
RV 72,32,96
RV 48,33,96
s_waitcnt lgkmcnt(5)
PM 96,96,28
PM 97,97,28
M 216,8,80
RV 52,34,96
RV 56,35,96
PM 98,98,28
PM 99,99,28
M 220,12,80
PH 96,60,28
PH 97,61,28
PH 98,62,28
PH 99,63,28
M 224,16,80
PL 96,64,29
PL 97,65,29
PL 98,66,29
PL 99,67,29
M 228,20,80
PH 96,68,29
PH 97,69,29
PH 98,70,29
PH 99,71,29
M 160,24,88
RV 76,36,96
RV 60,37,96
s_waitcnt lgkmcnt(2)
PM 72,72,24
PM 73,73,24
M 164,28,88
RV 64,38,96
RV 68,39,96
PM 74,74,24
PM 75,75,24
PH 72,48,24
M 168,32,88
RA 0,16,36864
RA 4,16,37888
PH 73,49,24
PH 74,50,24
PH 75,51,24
M 172,36,88
RA 8,16,38912
PL 72,52,25
PL 73,53,25
M 176,40,88
PL 74,54,25
PL 75,55,25
PH 72,56,25
M 180,44,88
buffer_load_dwordx4 v15, s[36:39], s40 offen offset:3072 sc1 lds
PH 73,57,25
PH 74,58,25
PH 75,59,25
M 204,44,92
RV 80,40,96
RV 48,41,96
s_waitcnt lgkmcnt(5)
PM 76,76,26
PM 77,77,26
M 200,40,92
RV 52,42,96
RV 56,43,96
PM 78,78,26
PM 79,79,26
PH 76,60,26
M 196,36,92
RA 12,16,39936
RA 16,16,40960
PH 77,61,26
PH 78,62,26
PH 79,63,26
M 192,32,92
RA 20,16,41984
PL 76,64,27
PL 77,65,27
M 188,28,92
PL 78,66,27
PL 79,67,27
PH 76,68,27
M 184,24,92
PH 77,69,27
PH 78,70,27
PH 79,71,27
M 208,24,96
RV 88,32,112
RV 60,33,112
s_waitcnt lgkmcnt(5)
PM 80,80,28
PM 81,81,28
M 212,28,96
RV 64,34,112
RV 68,35,112
PM 82,82,28
M 216,32,96
PM 83,83,28
PH 80,48,28
PH 81,49,28
M 220,36,96
PH 82,50,28
PH 83,51,28
PL 80,52,29
M 224,40,96
s_add_u32 m0, m0, 4096
PL 81,53,29
PL 82,54,29
PL 83,55,29
PH 80,56,29
M 228,44,96
buffer_load_dwordx4 v45, s[36:39], s40 offen sc1 lds
PH 81,57,29
PH 82,58,29
PH 83,59,29
M 160,0,72
RV 92,36,112
RV 48,37,112
s_waitcnt lgkmcnt(2)
PM 88,88,24
PM 89,89,24
M 164,4,72
RV 52,38,112
RV 56,39,112
PM 90,90,24
PM 91,91,24
PH 88,60,24
M 168,8,72
RA 24,16,43008
RA 28,16,44032
PH 89,61,24
PH 90,62,24
PH 91,63,24
M 172,12,72
RA 32,16,45056
PL 88,64,25
PL 89,65,25
M 176,16,72
PL 90,66,25
PL 91,67,25
PH 88,68,25
M 180,20,72
buffer_load_dwordx4 v45, s[36:39], s40 offen offset:1024 sc1 lds
PH 89,69,25
PH 90,70,25
PH 91,71,25
M 204,20,76
RV 96,40,112
RV 60,41,112
s_waitcnt lgkmcnt(5)
PM 92,92,26
PM 93,93,26
M 200,16,76
RV 64,42,112
RV 68,43,112
PM 94,94,26
PM 95,95,26
PH 92,48,26
M 196,12,76
RA 36,16,46080
RA 40,16,47104
PH 93,49,26
PH 94,50,26
PH 95,51,26
M 192,8,76
RA 44,16,48128
PL 92,52,27
PL 93,53,27
M 188,4,76
PL 94,54,27
PL 95,55,27
PH 92,56,27
M 184,0,76
PH 93,57,27
PH 94,58,27
PH 95,59,27
M 208,0,80
RV 72,32,128
RV 48,33,128
s_waitcnt lgkmcnt(5)
PM 96,96,28
PM 97,97,28
M 212,4,80
RV 52,34,128
RV 56,35,128
PM 98,98,28
M 216,8,80
s_addk_i32 s40, 24704
PM 99,99,28
PH 96,60,28
PH 97,61,28
M 220,12,80
s_waitcnt vmcnt(6) lgkmcnt(4)
PH 98,62,28
PH 99,63,28
PL 96,64,29
M 224,16,80
s_barrier
PL 97,65,29
PL 98,66,29
PL 99,67,29
M 228,20,80
PH 96,68,29
PH 97,69,29
PH 98,70,29
PH 99,71,29
M 160,24,88
RV 76,36,128
RV 60,37,128
M 164,28,88
s_branch L_in12_2_%=
.p2align 3
L_sel12_2_%=:
s_cmp_eq_u32 s55, 0
s_cbranch_scc1 L_bodz12_0_%=
s_cmp_eq_u32 s55, 1
s_cbranch_scc1 L_bodz12_1_%=
s_cmp_eq_u32 s55, 4
s_cbranch_scc1 L_tail12_0_%=
s_cmp_eq_u32 s55, 5
s_cbranch_scc1 L_tail12_1_%=
s_cmp_lt_u32 s55, 4
s_cbranch_scc1 L_bodz12_2_%=
s_branch L_tail12_2_%=
L_disp12_2_%=:
M 160,24,88
s_cmp_lg_u32 s55, 7
s_cbranch_scc1 L_sel12_2_%=
L_tail12_2_%=:
RA 0,16,0
RA 4,16,1024
v_add_u32 v32, s44, v152
v_mov_b32_e64 v6, v156
M 164,28,88
RA 8,16,2048
RA 12,16,3072
v_pk_max_i16 v25, v6, s50
v_pk_min_i16 v25, v25, s52
v_mad_i32_i16 v33, v25, s54, v32 op_sel:[1,0,0,0]
M 168,32,88
RA 16,16,4096
RA 20,16,5120
v_mad_i32_i16 v34, v25, s53, v32
v_mad_i32_i16 v35, v25, s53, v33
v_add_u32_e64 v36, s44, v153
M 172,36,88
RV 104,32,0
RV 48,33,0
v_mov_b32_e64 v7, v157
v_pk_max_i16 v27, v7, s50
v_pk_min_i16 v27, v27, s52
M 176,40,88
RV 52,34,0
RV 56,35,0
v_mad_i32_i16 v37, v27, s54, v36 op_sel:[1,0,0,0]
v_mad_i32_i16 v38, v27, s53, v36
v_mad_i32_i16 v39, v27, s53, v37
M 180,44,88
RV 108,32,16
RV 60,33,16
v_add_u32 v40, s44, v154
v_mov_b32 v8, v158
v_pk_max_i16 v29, v8, s50
M 204,44,92
RV 64,34,16
RV 68,35,16
v_pk_min_i16 v29, v29, s52
v_mad_i32_i16 v41, v29, s54, v40 op_sel:[1,0,0,0]
v_mad_i32_i16 v42, v29, s53, v40
M 200,40,92
RV 112,32,32
RV 72,33,32
v_mad_i32_i16 v43, v29, s53, v41
v_and_b32_e64 v6, s56, v6
v_pk_add_f16 v25, v6, s51 op_sel:[1,0] neg_lo:[1,0]
M 196,36,92
RV 76,34,32
RV 80,35,32
v_pk_add_f16 v6, v6, s51 op_sel_hi:[0,1] neg_lo:[1,0]
PM 24,25,6
v_pk_mul_f16 v25, v25, v6 op_sel:[0,1]
M 192,32,92
RA 68,16,11264
RA 64,16,10240
v_and_b32_e64 v7, s56, v7
v_pk_add_f16 v27, v7, s51 op_sel:[1,0] neg_lo:[1,0]
v_pk_add_f16 v7, v7, s51 op_sel_hi:[0,1] neg_lo:[1,0]
M 188,28,92
RA 60,16,9216
RA 56,16,8192
PM 26,27,7
v_pk_mul_f16 v27, v27, v7 op_sel:[0,1]
v_and_b32_e64 v8, s56, v8
M 184,24,92
RA 52,16,7168
RA 48,16,6144
v_pk_add_f16 v29, v8, s51 op_sel:[1,0] neg_lo:[1,0]
v_pk_add_f16 v8, v8, s51 op_sel_hi:[0,1] neg_lo:[1,0]
PM 28,29,8
M 208,24,96
v_pk_mul_f16 v29, v29, v8 op_sel:[0,1]
s_waitcnt lgkmcnt(6)
PM 104,104,24
PM 105,105,24
PM 106,106,24
PM 107,107,24
M 212,28,96
PH 104,48,24
PH 105,49,24
PH 106,50,24
PH 107,51,24
PL 104,52,25
M 216,32,96
PL 105,53,25
PL 106,54,25
PL 107,55,25
PH 104,56,25
PH 105,57,25
M 220,36,96
PH 106,58,25
PH 107,59,25
PM 108,108,24
PM 109,109,24
M 224,40,96
RV 116,36,0
RV 48,37,0
PM 110,110,24
PM 111,111,24
PH 108,60,24
M 228,44,96
RV 52,38,0
RV 56,39,0
PH 109,61,24
PH 110,62,24
PH 111,63,24
M 160,0,104
RA 72,16,12288
RA 76,16,13312
PL 108,64,25
PL 109,65,25
M 164,4,104
RA 80,16,14336
RA 84,16,15360
PL 110,66,25
PL 111,67,25
M 168,8,104
RA 88,16,16384
RA 92,16,17408
PH 108,68,25
PH 109,69,25
M 172,12,104
PH 110,70,25
PH 111,71,25
PM 112,112,24
PM 113,113,24
M 176,16,104
RV 120,36,16
RV 60,37,16
PM 114,114,24
PM 115,115,24
M 180,20,104
RV 64,38,16
RV 68,39,16
PH 112,72,24
PH 113,73,24
s_waitcnt lgkmcnt(6)
M 180,68,108
PH 114,74,24
PH 115,75,24
PL 112,76,25
PL 113,77,25
s_mul_i32 s11, s23, 192
s_mul_i32 s16, s21, 43008
s_add_u32 s16, s16, s11
s_add_u32 s17, s16, 43008
s_add_u32 s18, s16, 86016
v_and_b32 v47, 0xfff, v46
v_add_u32 v12, 162304, v15
s_waitcnt vmcnt(0)
M 176,64,108
M 172,60,108
M 168,56,108
buffer_store_dword v31, v30, s[36:39], 0 offen sc1
RV 84,12,0
PL 114,78,25
PL 115,79,25
PH 112,80,25
PH 113,81,25
PH 114,82,25
PH 115,83,25
M 164,52,108
M 160,48,108
RV 124,36,32
RV 72,37,32
PM 116,116,26
M 160,72,112
RV 76,38,32
RV 80,39,32
PM 117,117,26
PM 118,118,26
M 164,76,112
PM 119,119,26
PH 116,48,26
PH 117,49,26
M 168,80,112
PH 118,50,26
PH 119,51,26
PL 116,52,27
PL 117,53,27
M 172,84,112
PL 118,54,27
PL 119,55,27
PH 116,56,27
PH 117,57,27
PH 118,58,27
PH 119,59,27
s_waitcnt lgkmcnt(2)
M 176,88,112
RV 128,40,0
RV 48,41,0
M 180,92,112
RV 52,42,0
RV 56,43,0
PM 120,120,26
PM 121,121,26
M 184,0,116
PM 122,122,26
PM 123,123,26
PH 120,60,26
PH 121,61,26
M 188,4,116
PH 122,62,26
PH 123,63,26
PL 120,64,27
PL 121,65,27
M 192,8,116
PL 122,66,27
PL 123,67,27
PH 120,68,27
PH 121,69,27
M 196,12,116
PH 122,70,27
PH 123,71,27
s_waitcnt lgkmcnt(0)
PM 124,124,26
PM 125,125,26
M 200,16,116
RV 132,40,16
RV 60,41,16
PM 126,126,26
PM 127,127,26
M 204,20,116
RV 64,42,16
RV 68,43,16
PH 124,72,26
PH 125,73,26
M 204,68,120
PH 126,74,26
PH 127,75,26
PL 124,76,27
PL 125,77,27
M 200,64,120
PL 126,78,27
PL 127,79,27
PH 124,80,27
PH 125,81,27
M 196,60,120
PH 126,82,27
PH 127,83,27
PM 128,128,28
PM 129,129,28
M 192,56,120
RV 136,40,32
RV 72,41,32
PM 130,130,28
PM 131,131,28
M 188,52,120
RV 76,42,32
RV 80,43,32
PH 128,48,28
M 184,48,120
PH 129,49,28
PH 130,50,28
PH 131,51,28
PL 128,52,29
M 184,72,124
PL 129,53,29
PL 130,54,29
PL 131,55,29
PH 128,56,29
M 188,76,124
PH 129,57,29
PH 130,58,29
PH 131,59,29
s_waitcnt lgkmcnt(2)
PM 132,132,28
M 192,80,124
PM 133,133,28
PM 134,134,28
PM 135,135,28
PH 132,60,28
M 196,84,124
PH 133,61,28
PH 134,62,28
PH 135,63,28
PL 132,64,29
M 200,88,124
PL 133,65,29
PL 134,66,29
PL 135,67,29
PH 132,68,29
M 204,92,124
PH 133,69,29
PH 134,70,29
PH 135,71,29
v_cvt_pk_bf16_f32 v88, v160, v161
M 208,0,128
s_waitcnt lgkmcnt(0)
PM 136,136,28
PM 137,137,28
PM 138,138,28
PM 139,139,28
M 212,4,128
PH 136,72,28
PH 137,73,28
PH 138,74,28
PH 139,75,28
M 216,8,128
PL 136,76,29
PL 137,77,29
PL 138,78,29
PL 139,79,29
M 220,12,128
PH 136,80,29
PH 137,81,29
PH 138,82,29
PH 139,83,29
M 224,16,128
v_cvt_pk_bf16_f32 v89, v162, v163
v_cvt_pk_bf16_f32 v90, v164, v165
v_cvt_pk_bf16_f32 v91, v166, v167
v_cvt_pk_bf16_f32 v92, v168, v169
M 228,20,128
buffer_store_dwordx4 v[88:91], v47, s[68:71], s16 offen offset:0 sc0 sc1 nt
v_cvt_pk_bf16_f32 v93, v170, v171
v_cvt_pk_bf16_f32 v94, v172, v173
M 228,68,132
v_cvt_pk_bf16_f32 v95, v174, v175
v_cvt_pk_bf16_f32 v96, v176, v177
v_cvt_pk_bf16_f32 v97, v178, v179
v_cvt_pk_bf16_f32 v98, v180, v181
M 224,64,132
v_cvt_pk_bf16_f32 v99, v182, v183
v_cvt_pk_bf16_f32 v88, v184, v185
v_cvt_pk_bf16_f32 v89, v186, v187
v_cvt_pk_bf16_f32 v90, v188, v189
M 220,60,132
v_cvt_pk_bf16_f32 v91, v190, v191
v_or3_b32 v84, v84, v85, v86
v_or3_b32 v84, v84, v87, v5
M 216,56,132
buffer_store_dwordx4 v[92:95], v47, s[68:71], s16 offen offset:64 sc0 sc1 nt
v_and_b32 v85, 0x40004001, v84
v_and_b32 v86, 0x70007000, v84
M 212,52,132
ds_write_b128 v4, v[88:91] offset:0
v_cvt_pk_bf16_f32 v92, v192, v193
v_cvt_pk_bf16_f32 v93, v194, v195
M 208,48,132
v_cvt_pk_bf16_f32 v94, v196, v197
v_cvt_pk_bf16_f32 v95, v198, v199
v_and_b32 v87, 2, v84
v_cmp_ne_u32 vcc, 0, v85
M 208,72,136
buffer_store_dwordx4 v[96:99], v47, s[68:71], s16 offen offset:128 sc0 sc1 nt
v_cmp_eq_u32 s[30:31], 0, v86
v_cmp_eq_u32 s[58:59], 0, v87
M 212,76,136
ds_write_b128 v4, v[92:95] offset:64
v_cvt_pk_bf16_f32 v96, v200, v201
v_cvt_pk_bf16_f32 v97, v202, v203
M 216,80,136
v_cvt_pk_bf16_f32 v98, v204, v205
v_cvt_pk_bf16_f32 v99, v206, v207
v_cvt_pk_bf16_f32 v88, v208, v209
v_cvt_pk_bf16_f32 v89, v210, v211
M 220,84,136
ds_write_b128 v4, v[96:99] offset:128
RV 144,1,0
v_cvt_pk_bf16_f32 v90, v212, v213
M 224,88,136
RV 148,2,0
RV 100,3,0
v_cvt_pk_bf16_f32 v91, v214, v215
v_cvt_pk_bf16_f32 v92, v216, v217
M 228,92,136
ds_write_b128 v4, v[88:91] offset:0
v_cvt_pk_bf16_f32 v93, v218, v219
v_cvt_pk_bf16_f32 v94, v220, v221
v_cvt_pk_bf16_f32 v95, v222, v223
v_cvt_pk_bf16_f32 v96, v224, v225
v_cvt_pk_bf16_f32 v97, v226, v227
s_cmp_eq_u64 s[30:31], -1
s_cselect_b64 vcc, -1, vcc
s_cmp_eq_u64 s[58:59], -1
s_cselect_b64 vcc, -1, vcc
ds_write_b128 v4, v[92:95] offset:64
v_cvt_pk_bf16_f32 v98, v228, v229
v_cvt_pk_bf16_f32 v99, v230, v231
ds_write_b128 v4, v[96:99] offset:128
s_waitcnt lgkmcnt(2)
buffer_store_dwordx4 v[144:147], v15, s[68:71], s17 offen offset:0 sc0 sc1 nt
RV 144,1,0
buffer_store_dwordx4 v[148:151], v15, s[68:71], s17 offen offset:1024 sc0 sc1 nt
RV 148,2,0
buffer_store_dwordx4 v[100:103], v15, s[68:71], s17 offen offset:2048 sc0 sc1 nt
RV 100,3,0
s_waitcnt lgkmcnt(0)
buffer_store_dwordx4 v[144:147], v15, s[68:71], s18 offen offset:0 sc0 sc1 nt
buffer_store_dwordx4 v[148:151], v15, s[68:71], s18 offen offset:1024 sc0 sc1 nt
buffer_store_dwordx4 v[100:103], v15, s[68:71], s18 offen offset:2048 sc0 sc1 nt
s_cbranch_vccnz L_done_%=
s_endpgm
.p2align 3
L_ent13_1_%=:
RA 0,16,0
RA 4,16,1024
RA 8,16,2048
RA 12,16,3072
RA 16,16,4096
RA 20,16,5120
s_set_gpr_idx_on s43, gpr_idx(SRC1)
v_add_u32 v32, s44, v104
v_add_u32 v36, s44, v105
v_add_u32 v40, s44, v106
v_add_u32 v44, s44, v107
v_add_u32 v6, 0, v108
v_add_u32 v7, 0, v109
v_add_u32 v8, 0, v110
v_add_u32 v9, 0, v111
s_set_gpr_idx_off
v_pk_max_i16 v25, v6, s50
v_pk_min_i16 v25, v25, s52
v_mad_i32_i16 v56, v25, s53, v32
v_mad_i32_i16 v56, v25, s54, v56 op_sel:[1,0,0,0]
v_pk_max_i16 v27, v7, s50
v_pk_min_i16 v27, v27, s52
v_mad_i32_i16 v60, v27, s53, v36
v_mad_i32_i16 v60, v27, s54, v60 op_sel:[1,0,0,0]
v_pk_max_i16 v29, v8, s50
v_pk_min_i16 v29, v29, s52
v_mad_i32_i16 v64, v29, s53, v40
v_mad_i32_i16 v64, v29, s54, v64 op_sel:[1,0,0,0]
v_pk_max_i16 v31, v9, s50
v_pk_min_i16 v31, v31, s52
v_mad_i32_i16 v68, v31, s53, v44
v_mad_i32_i16 v68, v31, s54, v68 op_sel:[1,0,0,0]
v_and_b32_e64 v6, s56, v6
v_pk_add_f16 v25, v6, v6 op_sel:[0,1] op_sel_hi:[1,0]
v_pk_add_f16 v24, v25, s51 neg_lo:[1,0]
v_and_b32_e64 v7, s56, v7
v_pk_add_f16 v27, v7, v7 op_sel:[0,1] op_sel_hi:[1,0]
v_pk_add_f16 v26, v27, s51 neg_lo:[1,0]
v_and_b32_e64 v8, s56, v8
v_pk_add_f16 v29, v8, v8 op_sel:[0,1] op_sel_hi:[1,0]
v_pk_add_f16 v28, v29, s51 neg_lo:[1,0]
v_and_b32_e64 v9, s56, v9
v_pk_add_f16 v31, v9, v9 op_sel:[0,1] op_sel_hi:[1,0]
v_pk_add_f16 v30, v31, s51 neg_lo:[1,0]
v_add_u32 v33, s45, v32
v_add_u32 v57, s45, v56
v_add_u32 v37, s45, v36
v_add_u32 v61, s45, v60
v_add_u32 v41, s45, v40
v_add_u32 v65, s45, v64
v_add_u32 v34, s46, v32
v_add_u32 v58, s46, v56
v_add_u32 v38, s46, v36
v_add_u32 v62, s46, v60
v_add_u32 v42, s46, v40
v_add_u32 v66, s46, v64
v_add_u32 v35, s47, v32
v_add_u32 v59, s47, v56
v_add_u32 v39, s47, v36
v_add_u32 v63, s47, v60
v_add_u32 v43, s47, v40
v_add_u32 v67, s47, v64
s_waitcnt lgkmcnt(6)
s_barrier
RV 72,32,0
RV 48,56,0
RV 76,36,0
RV 52,60,0
s_waitcnt lgkmcnt(2)
PM 72,72,24
PM 73,73,24
PM 74,74,24
PM 75,75,24
v_pk_fmac_f16 v72, v48, v25
v_pk_fmac_f16 v73, v49, v25
v_pk_fmac_f16 v74, v50, v25
v_pk_fmac_f16 v75, v51, v25
RV 80,40,0
RV 48,64,0
s_waitcnt lgkmcnt(2)
PM 76,76,26
PM 77,77,26
PM 78,78,26
PM 79,79,26
v_pk_fmac_f16 v76, v52, v27
v_pk_fmac_f16 v77, v53, v27
PH 78,54,26
v_pk_fmac_f16 v79, v55, v27
RV 84,44,0
RV 52,68,0
s_waitcnt lgkmcnt(2)
PM 80,80,28
PM 81,81,28
PM 82,82,28
PM 83,83,28
v_pk_fmac_f16 v80, v48, v29
v_pk_fmac_f16 v81, v49, v29
PH 82,50,28
v_pk_fmac_f16 v83, v51, v29
RV 88,33,16
RV 48,57,16
s_waitcnt lgkmcnt(2)
PM 84,84,30
PM 85,85,30
PM 86,86,30
PM 87,87,30
v_pk_fmac_f16 v84, v52, v31
v_pk_fmac_f16 v85, v53, v31
PH 86,54,30
v_pk_fmac_f16 v87, v55, v31
s_waitcnt lgkmcnt(0)
s_nop 0
v_mfma_f32_16x16x32_f16 v[160:163], a[0:3], v[72:75], 0
s_add_u32 m0, s41, 24576
RV 92,37,16
RV 52,61,16
v_mfma_f32_16x16x32_f16 v[164:167], a[4:7], v[72:75], 0
buffer_load_dwordx4 v15, s[36:39], s40 offen sc1 lds
s_waitcnt lgkmcnt(2)
PM 88,88,24
v_mfma_f32_16x16x32_f16 v[168:171], a[8:11], v[72:75], 0
RA 24,17,6144
RA 28,17,7168
v_mfma_f32_16x16x32_f16 v[172:175], a[12:15], v[72:75], 0
buffer_load_dwordx4 v15, s[36:39], s40 offen offset:1024 sc1 lds
PM 89,89,24
PM 90,90,24
v_mfma_f32_16x16x32_f16 v[176:179], a[16:19], v[72:75], 0
RA 32,17,8192
v_mfma_f32_16x16x32_f16 v[180:183], a[20:23], v[72:75], 0
buffer_load_dwordx4 v15, s[36:39], s40 offen offset:2048 sc1 lds
PM 91,91,24
PH 88,48,24
v_mfma_f32_16x16x32_f16 v[204:207], a[20:23], v[76:79], 0
v_pk_fmac_f16 v89, v49, v25
v_pk_fmac_f16 v90, v50, v25
v_mfma_f32_16x16x32_f16 v[200:203], a[16:19], v[76:79], 0
PH 91,51,24
v_mfma_f32_16x16x32_f16 v[196:199], a[12:15], v[76:79], 0
buffer_load_dwordx4 v15, s[36:39], s40 offen offset:3072 sc1 lds
v_mfma_f32_16x16x32_f16 v[192:195], a[8:11], v[76:79], 0
s_add_u32 m0, m0, 4096
v_mfma_f32_16x16x32_f16 v[188:191], a[4:7], v[76:79], 0
buffer_load_dwordx4 v45, s[36:39], s40 offen sc1 lds
v_mfma_f32_16x16x32_f16 v[184:187], a[0:3], v[76:79], 0
v_mfma_f32_16x16x32_f16 v[208:211], a[0:3], v[80:83], 0
buffer_load_dwordx4 v45, s[36:39], s40 offen offset:1024 sc1 lds
RV 96,41,16
RV 48,65,16
v_mfma_f32_16x16x32_f16 v[212:215], a[4:7], v[80:83], 0
s_addk_i32 s40, 24704
s_waitcnt lgkmcnt(2)
PM 92,92,26
v_mfma_f32_16x16x32_f16 v[216:219], a[8:11], v[80:83], 0
RA 36,17,9216
RA 40,17,10240
v_mfma_f32_16x16x32_f16 v[220:223], a[12:15], v[80:83], 0
PM 93,93,26
PM 94,94,26
v_mfma_f32_16x16x32_f16 v[224:227], a[16:19], v[80:83], 0
RA 44,17,11264
PM 95,95,26
PH 92,52,26
v_mfma_f32_16x16x32_f16 v[228:231], a[20:23], v[80:83], 0
v_mfma_f32_16x16x32_f16 v[252:255], a[20:23], v[84:87], 0
v_pk_fmac_f16 v93, v53, v27
v_pk_fmac_f16 v94, v54, v27
v_pk_fmac_f16 v95, v55, v27
v_mfma_f32_16x16x32_f16 v[248:251], a[16:19], v[84:87], 0
RV 72,34,32
RV 52,58,32
v_mfma_f32_16x16x32_f16 v[244:247], a[12:15], v[84:87], 0
s_waitcnt lgkmcnt(3)
PM 96,96,28
v_mfma_f32_16x16x32_f16 v[240:243], a[8:11], v[84:87], 0
PM 97,97,28
PM 98,98,28
v_mfma_f32_16x16x32_f16 v[236:239], a[4:7], v[84:87], 0
PM 99,99,28
PH 96,48,28
v_mfma_f32_16x16x32_f16 v[232:235], a[0:3], v[84:87], 0
v_pk_fmac_f16 v97, v49, v29
v_pk_fmac_f16 v98, v50, v29
v_pk_fmac_f16 v99, v51, v29
M 160,24,88
RV 76,38,32
RV 48,62,32
M 164,28,88
s_waitcnt lgkmcnt(2)
PM 72,72,24
M 168,32,88
RA 0,18,12288
RA 4,18,13312
M 172,36,88
PM 73,73,24
PM 74,74,24
M 176,40,88
RA 8,18,14336
PM 75,75,24
PH 72,52,24
M 180,44,88
v_pk_fmac_f16 v73, v53, v25
v_pk_fmac_f16 v74, v54, v25
v_pk_fmac_f16 v75, v55, v25
M 204,44,92
M 200,40,92
M 196,36,92
M 192,32,92
RV 80,42,32
RV 52,66,32
s_waitcnt lgkmcnt(2)
PM 76,76,26
M 188,28,92
RA 12,18,15360
RA 16,18,16384
PM 77,77,26
PM 78,78,26
M 184,24,92
RA 20,18,17408
PM 79,79,26
PH 76,48,26
M 208,24,96
v_pk_fmac_f16 v77, v49, v27
v_pk_fmac_f16 v78, v50, v27
v_pk_fmac_f16 v79, v51, v27
M 212,28,96
RV 88,35,48
RV 48,59,48
M 216,32,96
s_waitcnt lgkmcnt(5)
PM 80,80,28
PM 81,81,28
M 220,36,96
PM 82,82,28
PM 83,83,28
PH 80,52,28
M 224,40,96
M 228,44,96
v_pk_fmac_f16 v81, v53, v29
v_pk_fmac_f16 v82, v54, v29
v_pk_fmac_f16 v83, v55, v29
M 160,0,72
RV 92,39,48
RV 52,63,48
M 164,4,72
s_waitcnt lgkmcnt(2)
PM 88,88,24
M 168,8,72
RA 24,19,18432
RA 28,19,19456
M 172,12,72
PM 89,89,24
PM 90,90,24
M 176,16,72
RA 32,19,20480
PM 91,91,24
PH 88,48,24
M 180,20,72
M 204,20,76
v_pk_fmac_f16 v89, v49, v25
v_pk_fmac_f16 v90, v50, v25
PH 91,51,24
M 200,16,76
RV 96,43,48
RV 48,67,48
M 196,12,76
s_waitcnt lgkmcnt(3)
PM 92,92,26
M 192,8,76
RA 36,19,21504
RA 40,19,22528
M 188,4,76
PM 93,93,26
PM 94,94,26
v_cmp_nge_f32 vcc, 1.0, v12
M 184,0,76
buffer_load_dwordx4 v[6:9], v15, s[36:39], 0 offen sc1
RA 44,19,23552
PM 95,95,26
v_and_b32 v13, 0x7ff87ff8, v13
M 208,0,80
PH 92,52,26
s_lshl2_add_u32 s11, s10, 162304
M 212,4,80
buffer_load_dwordx4 v[100:103], v47, s[36:39], 0 offen offset:1024 sc1
v_pk_fmac_f16 v93, v53, v27
v_pk_fmac_f16 v94, v54, v27
v_pk_fmac_f16 v95, v55, v27
v_addc_co_u32 v13, vcc, 0, v13, vcc
M 216,8,80
RV 72,32,64
RV 52,56,64
v_add_u32_e64 v12, s11, v15
M 220,12,80
s_waitcnt vmcnt(2) lgkmcnt(2)
PM 96,96,28
PM 97,97,28
M 224,16,80
s_barrier
PM 98,98,28
PM 99,99,28
M 228,20,80
v_pk_fmac_f16 v96, v48, v29
v_pk_fmac_f16 v97, v49, v29
PH 98,50,28
v_pk_fmac_f16 v99, v51, v29
M 160,24,88
RV 76,36,64
RV 48,60,64
M 164,28,88
ds_write_b32 v12, v13
s_waitcnt lgkmcnt(2)
PM 72,72,24
M 168,32,88
RA 0,16,24576
RA 4,16,25600
PM 73,73,24
M 172,36,88
PM 74,74,24
PM 75,75,24
PH 72,52,24
M 176,40,88
v_pk_fmac_f16 v73, v53, v25
v_pk_fmac_f16 v74, v54, v25
v_pk_fmac_f16 v75, v55, v25
M 180,44,88
RV 80,40,64
RV 52,64,64
s_waitcnt lgkmcnt(4)
PM 76,76,26
M 204,44,92
RA 8,16,26624
RA 12,16,27648
PM 77,77,26
M 200,40,92
PM 78,78,26
PM 79,79,26
PH 76,48,26
M 196,36,92
M 192,32,92
v_pk_fmac_f16 v77, v49, v27
v_pk_fmac_f16 v78, v50, v27
v_pk_fmac_f16 v79, v51, v27
M 188,28,92
RV 84,44,64
RV 48,68,64
s_waitcnt lgkmcnt(4)
PM 80,80,28
M 184,24,92
RA 16,16,28672
RA 20,16,29696
PM 81,81,28
M 208,24,96
PM 82,82,28
PM 83,83,28
PH 80,52,28
M 212,28,96
v_pk_fmac_f16 v81, v53, v29
v_pk_fmac_f16 v82, v54, v29
v_pk_fmac_f16 v83, v55, v29
M 216,32,96
RV 88,33,80
RV 52,57,80
M 220,36,96
s_waitcnt lgkmcnt(4)
PM 84,84,30
PM 85,85,30
M 224,40,96
PM 86,86,30
PM 87,87,30
PH 84,48,30
M 228,44,96
v_pk_fmac_f16 v85, v49, v31
v_pk_fmac_f16 v86, v50, v31
PH 87,51,30
M 160,0,72
RV 92,37,80
RV 48,61,80
s_waitcnt vmcnt(0)
v_bitop3_b32 v6, v6, v8, s66 bitop3:0x96
M 164,4,72
s_waitcnt lgkmcnt(2)
PM 88,88,24
v_bitop3_b32 v100, v100, v102, s66 bitop3:0x96
v_bitop3_b32 v7, v7, v9, s67 bitop3:0x96
M 168,8,72
RA 24,17,30720
RA 28,17,31744
v_bitop3_b32 v101, v101, v103, s67 bitop3:0x96
v_or_b32_e64 v6, v6, v100
M 172,12,72
PM 89,89,24
PM 90,90,24
v_or_b32_e64 v5, v7, v101
v_cmp_ne_u32 vcc, 0, v6
M 176,16,72
RA 32,17,32768
v_cmp_lt_u32 s[30:31], 7, v5
s_or_b64 vcc, vcc, s[30:31]
M 180,20,72
PM 91,91,24
v_pk_fmac_f16 v88, v52, v25
s_cbranch_scc1 L_ag13_1_%=
L_ok13_1_%=:
M 204,20,76
s_add_u32 m0, s41, 49152
M 200,16,76
buffer_load_dwordx4 v15, s[36:39], s40 offen sc1 lds
PH 89,53,24
M 196,12,76
PH 90,54,24
v_pk_fmac_f16 v91, v55, v25
M 192,8,76
RV 96,41,80
RV 52,65,80
M 188,4,76
buffer_load_dwordx4 v15, s[36:39], s40 offen offset:1024 sc1 lds
s_waitcnt lgkmcnt(2)
PM 92,92,26
M 184,0,76
RA 36,17,33792
RA 40,17,34816
M 208,0,80
PM 93,93,26
PM 94,94,26
M 212,4,80
buffer_load_dwordx4 v15, s[36:39], s40 offen offset:2048 sc1 lds
RA 44,17,35840
M 216,8,80
PM 95,95,26
PH 92,48,26
M 220,12,80
M 224,16,80
buffer_load_dwordx4 v15, s[36:39], s40 offen offset:3072 sc1 lds
PH 93,49,26
M 228,20,80
s_addk_i32 m0, 4096
v_pk_fmac_f16 v94, v50, v27
v_pk_fmac_f16 v95, v51, v27
M 252,20,84
RV 72,34,96
M 248,16,84
buffer_load_dwordx4 v45, s[36:39], s40 offen sc1 lds
RV 48,58,96
M 244,12,84
s_waitcnt lgkmcnt(2)
PM 96,96,28
PM 97,97,28
M 240,8,84
PM 98,98,28
PM 99,99,28
M 236,4,84
buffer_load_dwordx4 v45, s[36:39], s40 offen offset:1024 sc1 lds
v_pk_fmac_f16 v96, v52, v29
v_pk_fmac_f16 v97, v53, v29
M 232,0,84
s_addk_i32 s40, 24704
v_pk_fmac_f16 v98, v54, v29
PH 99,55,28
M 160,24,88
RV 76,38,96
RV 52,62,96
M 164,28,88
s_waitcnt lgkmcnt(2)
PM 72,72,24
M 168,32,88
s_add_u32 m0, s41, 0
RA 0,18,36864
RA 4,18,37888
M 172,36,88
buffer_load_dwordx4 v15, s[36:39], s40 offen sc1 lds
PM 73,73,24
PM 74,74,24
M 176,40,88
RA 8,18,38912
PM 75,75,24
PH 72,48,24
M 180,44,88
M 204,44,92
v_pk_fmac_f16 v73, v49, v25
v_pk_fmac_f16 v74, v50, v25
v_pk_fmac_f16 v75, v51, v25
M 200,40,92
buffer_load_dwordx4 v15, s[36:39], s40 offen offset:1024 sc1 lds
RV 80,42,96
RV 48,66,96
M 196,36,92
s_waitcnt lgkmcnt(3)
PM 76,76,26
M 192,32,92
RA 12,18,39936
RA 16,18,40960
M 188,28,92
PM 77,77,26
PM 78,78,26
M 184,24,92
RA 20,18,41984
PM 79,79,26
PH 76,52,26
M 208,24,96
buffer_load_dwordx4 v15, s[36:39], s40 offen offset:2048 sc1 lds
v_pk_fmac_f16 v77, v53, v27
v_pk_fmac_f16 v78, v54, v27
v_pk_fmac_f16 v79, v55, v27
M 212,28,96
RV 88,35,112
RV 52,59,112
M 216,32,96
s_waitcnt lgkmcnt(3)
PM 80,80,28
PM 81,81,28
M 220,36,96
PM 82,82,28
PM 83,83,28
PH 80,48,28
M 224,40,96
M 228,44,96
buffer_load_dwordx4 v15, s[36:39], s40 offen offset:3072 sc1 lds
v_pk_fmac_f16 v81, v49, v29
v_pk_fmac_f16 v82, v50, v29
PH 83,51,28
M 160,0,72
s_addk_i32 m0, 4096
RV 92,39,112
RV 48,63,112
M 164,4,72
s_waitcnt lgkmcnt(2)
PM 88,88,24
M 168,8,72
RA 24,19,43008
RA 28,19,44032
M 172,12,72
PM 89,89,24
PM 90,90,24
M 176,16,72
buffer_load_dwordx4 v45, s[36:39], s40 offen sc1 lds
RA 32,19,45056
PM 91,91,24
PH 88,52,24
M 180,20,72
M 204,20,76
v_pk_fmac_f16 v89, v53, v25
v_pk_fmac_f16 v90, v54, v25
v_pk_fmac_f16 v91, v55, v25
M 200,16,76
RV 96,43,112
RV 52,67,112
M 196,12,76
s_waitcnt lgkmcnt(3)
PM 92,92,26
M 192,8,76
buffer_load_dwordx4 v45, s[36:39], s40 offen offset:1024 sc1 lds
RA 36,19,46080
RA 40,19,47104
M 188,4,76
s_add_u32 s40, s40, 24704
PM 93,93,26
PM 94,94,26
M 184,0,76
RA 44,19,48128
PM 95,95,26
M 208,0,80
PH 92,48,26
M 212,4,80
v_pk_fmac_f16 v93, v49, v27
v_pk_fmac_f16 v94, v50, v27
PH 95,51,26
M 216,8,80
RV 72,32,128
RV 48,56,128
M 220,12,80
s_waitcnt vmcnt(6) lgkmcnt(2)
PM 96,96,28
PM 97,97,28
M 224,16,80
s_barrier
PM 98,98,28
PM 99,99,28
M 228,20,80
v_pk_fmac_f16 v96, v52, v29
v_pk_fmac_f16 v97, v53, v29
v_pk_fmac_f16 v98, v54, v29
v_pk_fmac_f16 v99, v55, v29
M 160,24,88
RV 76,36,128
RV 52,60,128
M 164,28,88
s_branch L_in13_1_%=
L_ag13_1_%=:
s_call_b64 s[72:73], L_retry_%=
s_branch L_ok13_1_%=
.p2align 3
L_in13_1_%=:
s_waitcnt lgkmcnt(2)
PM 72,72,24
M 168,32,88
RA 0,20,0
RA 4,20,1024
PM 73,73,24
M 172,36,88
PM 74,74,24
PM 75,75,24
v_pk_fmac_f16 v72, v48, v25
M 176,40,88
v_pk_fmac_f16 v73, v49, v25
v_pk_fmac_f16 v74, v50, v25
v_pk_fmac_f16 v75, v51, v25
M 180,44,88
RV 80,40,128
RV 48,64,128
s_waitcnt lgkmcnt(4)
PM 76,76,26
M 204,44,92
RA 8,20,2048
RA 12,20,3072
PM 77,77,26
M 200,40,92
s_add_u32 m0, s41, 24576
PM 78,78,26
PM 79,79,26
PH 76,52,26
M 196,36,92
buffer_load_dwordx4 v15, s[36:39], s40 offen sc1 lds
M 192,32,92
v_pk_fmac_f16 v77, v53, v27
v_pk_fmac_f16 v78, v54, v27
v_pk_fmac_f16 v79, v55, v27
M 188,28,92
RV 84,44,128
RV 52,68,128
s_waitcnt lgkmcnt(4)
PM 80,80,28
M 184,24,92
RA 16,20,4096
RA 20,20,5120
PM 81,81,28
M 208,24,96
PM 82,82,28
PM 83,83,28
PH 80,48,28
M 212,28,96
v_pk_fmac_f16 v81, v49, v29
v_pk_fmac_f16 v82, v50, v29
v_pk_fmac_f16 v83, v51, v29
M 216,32,96
RV 88,33,144
RV 48,57,144
M 220,36,96
s_waitcnt lgkmcnt(4)
PM 84,84,30
PM 85,85,30
M 224,40,96
PM 86,86,30
PM 87,87,30
PH 84,52,30
M 228,44,96
v_pk_fmac_f16 v85, v53, v31
v_pk_fmac_f16 v86, v54, v31
v_pk_fmac_f16 v87, v55, v31
M 160,0,72
RV 92,37,144
RV 52,61,144
M 164,4,72
s_waitcnt lgkmcnt(2)
PM 88,88,24
M 168,8,72
RA 24,21,6144
RA 28,21,7168
M 172,12,72
PM 89,89,24
PM 90,90,24
M 176,16,72
RA 32,21,8192
M 180,20,72
PM 91,91,24
PH 88,48,24
M 204,20,76
buffer_load_dwordx4 v15, s[36:39], s40 offen offset:1024 sc1 lds
M 200,16,76
PH 89,49,24
M 196,12,76
PH 90,50,24
v_pk_fmac_f16 v91, v51, v25
M 192,8,76
RV 96,41,144
RV 48,65,144
M 188,4,76
s_waitcnt lgkmcnt(2)
PM 92,92,26
M 184,0,76
RA 36,21,9216
RA 40,21,10240
M 208,0,80
PM 93,93,26
PM 94,94,26
M 212,4,80
RA 44,21,11264
M 216,8,80
)ASM" R"ASM(
PM 95,95,26
PH 92,52,26
M 220,12,80
buffer_load_dwordx4 v15, s[36:39], s40 offen offset:2048 sc1 lds
M 224,16,80
PH 93,53,26
M 228,20,80
PH 94,54,26
v_pk_fmac_f16 v95, v55, v27
M 252,20,84
RV 72,34,160
M 248,16,84
RV 52,58,160
M 244,12,84
s_waitcnt lgkmcnt(2)
PM 96,96,28
PM 97,97,28
M 240,8,84
PM 98,98,28
PM 99,99,28
M 236,4,84
v_pk_fmac_f16 v96, v48, v29
v_pk_fmac_f16 v97, v49, v29
M 232,0,84
PH 98,50,28
v_pk_fmac_f16 v99, v51, v29
M 160,24,88
RV 76,38,160
RV 48,62,160
M 164,28,88
s_waitcnt lgkmcnt(2)
PM 72,72,24
M 168,32,88
RA 0,22,12288
RA 4,22,13312
M 172,36,88
PM 73,73,24
PM 74,74,24
M 176,40,88
RA 8,22,14336
PM 75,75,24
PH 72,52,24
M 180,44,88
buffer_load_dwordx4 v15, s[36:39], s40 offen offset:3072 sc1 lds
M 204,44,92
v_pk_fmac_f16 v73, v53, v25
v_pk_fmac_f16 v74, v54, v25
v_pk_fmac_f16 v75, v55, v25
M 200,40,92
RV 80,42,160
RV 52,66,160
M 196,36,92
s_waitcnt lgkmcnt(3)
PM 76,76,26
M 192,32,92
RA 12,22,15360
RA 16,22,16384
M 188,28,92
PM 77,77,26
PM 78,78,26
M 184,24,92
RA 20,22,17408
PM 79,79,26
PH 76,48,26
M 208,24,96
v_pk_fmac_f16 v77, v49, v27
v_pk_fmac_f16 v78, v50, v27
PH 79,51,26
M 212,28,96
RV 88,35,176
RV 48,59,176
M 216,32,96
s_waitcnt lgkmcnt(3)
PM 80,80,28
PM 81,81,28
M 220,36,96
s_addk_i32 m0, 4096
PM 82,82,28
PM 83,83,28
PH 80,52,28
M 224,40,96
buffer_load_dwordx4 v45, s[36:39], s40 offen sc1 lds
M 228,44,96
v_pk_fmac_f16 v81, v53, v29
v_pk_fmac_f16 v82, v54, v29
v_pk_fmac_f16 v83, v55, v29
M 160,0,72
RV 92,39,176
RV 52,63,176
M 164,4,72
s_waitcnt lgkmcnt(2)
PM 88,88,24
M 168,8,72
RA 24,23,18432
RA 28,23,19456
M 172,12,72
PM 89,89,24
PM 90,90,24
M 176,16,72
RA 32,23,20480
PM 91,91,24
PH 88,48,24
M 180,20,72
buffer_load_dwordx4 v45, s[36:39], s40 offen offset:1024 sc1 lds
M 204,20,76
v_pk_fmac_f16 v89, v49, v25
v_pk_fmac_f16 v90, v50, v25
v_pk_fmac_f16 v91, v51, v25
M 200,16,76
RV 96,43,176
RV 48,67,176
M 196,12,76
s_waitcnt lgkmcnt(3)
PM 92,92,26
M 192,8,76
RA 36,23,21504
RA 40,23,22528
PM 93,93,26
PM 94,94,26
M 188,4,76
RA 44,23,23552
PM 95,95,26
PH 92,52,26
M 184,0,76
s_addk_i32 s40, 24704
v_pk_fmac_f16 v93, v53, v27
PH 94,54,26
M 208,0,80
v_pk_fmac_f16 v95, v55, v27
s_waitcnt lgkmcnt(3)
PM 96,96,28
M 212,4,80
PM 97,97,28
PM 98,98,28
PM 99,99,28
M 216,8,80
v_pk_fmac_f16 v96, v48, v29
v_pk_fmac_f16 v97, v49, v29
s_lshr_b32 s42, s42, 4
M 220,12,80
s_waitcnt vmcnt(6) lgkmcnt(0)
s_add_u32 s43, s43, 8
s_and_b32 s55, s42, 15
M 224,16,80
s_barrier
s_cmp_eq_u32 s55, 1
PH 98,50,28
v_pk_fmac_f16 v99, v51, v29
M 228,20,80
s_cbranch_scc0 L_disp13_1_%=
L_body13_1_%=:
M 160,24,88
L_bodz13_1_%=:
RA 0,16,0
RA 4,16,1024
M 164,28,88
s_set_gpr_idx_on s43, gpr_idx(SRC1)
v_add_u32 v32, s44, v104
v_add_u32 v36, s44, v105
v_add_u32 v40, s44, v106
v_add_u32 v44, s44, v107
v_add_u32 v6, 0, v108
v_add_u32 v7, 0, v109
v_add_u32 v8, 0, v110
v_add_u32 v9, 0, v111
s_set_gpr_idx_off
M 168,32,88
RA 8,16,2048
v_pk_max_i16 v25, v6, s50
v_pk_min_i16 v25, v25, s52
v_mad_i32_i16 v56, v25, s53, v32
v_mad_i32_i16 v56, v25, s54, v56 op_sel:[1,0,0,0]
M 172,36,88
RV 72,32,0
RV 48,56,0
v_pk_max_i16 v27, v7, s50
v_pk_min_i16 v27, v27, s52
v_mad_i32_i16 v60, v27, s53, v36
v_mad_i32_i16 v60, v27, s54, v60 op_sel:[1,0,0,0]
M 176,40,88
v_pk_max_i16 v29, v8, s50
v_pk_min_i16 v29, v29, s52
v_mad_i32_i16 v64, v29, s53, v40
v_mad_i32_i16 v64, v29, s54, v64 op_sel:[1,0,0,0]
v_pk_max_i16 v31, v9, s50
v_pk_min_i16 v31, v31, s52
M 180,44,88
v_mad_i32_i16 v68, v31, s53, v44
v_mad_i32_i16 v68, v31, s54, v68 op_sel:[1,0,0,0]
v_and_b32_e64 v6, s56, v6
v_pk_add_f16 v25, v6, v6 op_sel:[0,1] op_sel_hi:[1,0]
v_pk_add_f16 v24, v25, s51 neg_lo:[1,0]
v_and_b32_e64 v7, s56, v7
v_pk_add_f16 v27, v7, v7 op_sel:[0,1] op_sel_hi:[1,0]
M 204,44,92
v_pk_add_f16 v26, v27, s51 neg_lo:[1,0]
v_and_b32_e64 v8, s56, v8
v_pk_add_f16 v29, v8, v8 op_sel:[0,1] op_sel_hi:[1,0]
v_pk_add_f16 v28, v29, s51 neg_lo:[1,0]
v_and_b32 v9, s56, v9
v_pk_add_f16 v31, v9, v9 op_sel:[0,1] op_sel_hi:[1,0]
v_pk_add_f16 v30, v31, s51 neg_lo:[1,0]
M 200,40,92
RV 76,36,0
RV 52,60,0
s_waitcnt lgkmcnt(2)
PM 72,72,24
M 196,36,92
RA 12,16,3072
PM 73,73,24
PM 74,74,24
PM 75,75,24
PH 72,48,24
M 192,32,92
v_pk_fmac_f16 v73, v49, v25
v_pk_fmac_f16 v74, v50, v25
v_pk_fmac_f16 v75, v51, v25
v_add_u32 v33, s45, v32
v_add_u32_e64 v57, s45, v56
M 188,28,92
v_add_u32 v37, s45, v36
v_add_u32 v61, s45, v60
v_add_u32_e64 v41, s45, v40
v_add_u32 v65, s45, v64
M 184,24,92
RV 80,40,0
RV 48,64,0
s_waitcnt lgkmcnt(3)
PM 76,76,26
M 208,24,96
s_add_u32 m0, s41, 49152
RA 16,16,4096
PM 77,77,26
PM 78,78,26
PM 79,79,26
PH 76,52,26
M 212,28,96
buffer_load_dwordx4 v15, s[36:39], s40 offen sc1 lds
v_pk_fmac_f16 v77, v53, v27
v_pk_fmac_f16 v78, v54, v27
v_pk_fmac_f16 v79, v55, v27
M 216,32,96
RV 84,44,0
RV 52,68,0
s_waitcnt lgkmcnt(3)
PM 80,80,28
M 220,36,96
RA 20,16,5120
PM 81,81,28
PM 82,82,28
PM 83,83,28
v_pk_fmac_f16 v80, v48, v29
v_pk_fmac_f16 v81, v49, v29
PH 82,50,28
v_pk_fmac_f16 v83, v51, v29
M 224,40,96
RV 88,33,16
RV 48,57,16
s_waitcnt lgkmcnt(3)
PM 84,84,30
PM 85,85,30
M 228,44,96
PM 86,86,30
PM 87,87,30
v_pk_fmac_f16 v84, v52, v31
v_pk_fmac_f16 v85, v53, v31
PH 86,54,30
v_pk_fmac_f16 v87, v55, v31
M 160,0,72
RV 92,37,16
RV 52,61,16
M 164,4,72
s_waitcnt lgkmcnt(2)
PM 88,88,24
M 168,8,72
RA 24,17,6144
RA 28,17,7168
M 172,12,72
PM 89,89,24
PM 90,90,24
M 176,16,72
RA 32,17,8192
M 180,20,72
PM 91,91,24
PH 88,48,24
M 204,20,76
v_pk_fmac_f16 v89, v49, v25
v_pk_fmac_f16 v90, v50, v25
M 200,16,76
v_pk_fmac_f16 v91, v51, v25
v_add_u32 v34, s46, v32
M 196,12,76
v_add_u32 v58, s46, v56
v_add_u32 v38, s46, v36
M 192,8,76
buffer_load_dwordx4 v15, s[36:39], s40 offen offset:1024 sc1 lds
M 188,4,76
v_add_u32_e64 v62, s46, v60
M 184,0,76
v_add_u32_e64 v42, s46, v40
v_add_u32 v66, s46, v64
M 208,0,80
RV 96,41,16
RV 48,65,16
M 212,4,80
s_waitcnt lgkmcnt(2)
PM 92,92,26
M 216,8,80
RA 36,17,9216
RA 40,17,10240
M 220,12,80
PM 93,93,26
PM 94,94,26
M 224,16,80
RA 44,17,11264
PM 95,95,26
PH 92,52,26
M 228,20,80
buffer_load_dwordx4 v15, s[36:39], s40 offen offset:2048 sc1 lds
M 252,20,84
v_pk_fmac_f16 v93, v53, v27
v_pk_fmac_f16 v94, v54, v27
v_pk_fmac_f16 v95, v55, v27
M 248,16,84
RV 72,34,32
RV 52,58,32
M 244,12,84
s_waitcnt lgkmcnt(3)
PM 96,96,28
M 240,8,84
PM 97,97,28
PM 98,98,28
M 236,4,84
PM 99,99,28
PH 96,48,28
M 232,0,84
v_pk_fmac_f16 v97, v49, v29
v_pk_fmac_f16 v98, v50, v29
v_pk_fmac_f16 v99, v51, v29
M 160,24,88
RV 76,38,32
RV 48,62,32
M 164,28,88
s_waitcnt lgkmcnt(2)
PM 72,72,24
M 168,32,88
RA 0,18,12288
RA 4,18,13312
M 172,36,88
PM 73,73,24
PM 74,74,24
M 176,40,88
RA 8,18,14336
PM 75,75,24
PH 72,52,24
M 180,44,88
v_pk_fmac_f16 v73, v53, v25
v_pk_fmac_f16 v74, v54, v25
PH 75,55,24
M 204,44,92
v_add_u32 v35, s47, v32
v_add_u32 v59, s47, v56
v_add_u32_e64 v39, s47, v36
M 200,40,92
buffer_load_dwordx4 v15, s[36:39], s40 offen offset:3072 sc1 lds
M 196,36,92
v_add_u32 v63, s47, v60
v_add_u32 v43, s47, v40
v_add_u32 v67, s47, v64
M 192,32,92
RV 80,42,32
RV 52,66,32
s_waitcnt lgkmcnt(2)
PM 76,76,26
M 188,28,92
RA 12,18,15360
RA 16,18,16384
PM 77,77,26
PM 78,78,26
M 184,24,92
RA 20,18,17408
PM 79,79,26
PH 76,48,26
M 208,24,96
v_pk_fmac_f16 v77, v49, v27
v_pk_fmac_f16 v78, v50, v27
PH 79,51,26
M 212,28,96
RV 88,35,48
RV 48,59,48
M 216,32,96
s_waitcnt lgkmcnt(5)
PM 80,80,28
PM 81,81,28
M 220,36,96
s_addk_i32 m0, 4096
PM 82,82,28
PM 83,83,28
PH 80,52,28
M 224,40,96
buffer_load_dwordx4 v45, s[36:39], s40 offen sc1 lds
M 228,44,96
v_pk_fmac_f16 v81, v53, v29
v_pk_fmac_f16 v82, v54, v29
v_pk_fmac_f16 v83, v55, v29
M 160,0,72
RV 92,39,48
RV 52,63,48
M 164,4,72
s_waitcnt lgkmcnt(2)
PM 88,88,24
M 168,8,72
RA 24,19,18432
RA 28,19,19456
M 172,12,72
PM 89,89,24
PM 90,90,24
M 176,16,72
RA 32,19,20480
PM 91,91,24
PH 88,48,24
M 180,20,72
buffer_load_dwordx4 v45, s[36:39], s40 offen offset:1024 sc1 lds
M 204,20,76
v_pk_fmac_f16 v89, v49, v25
v_pk_fmac_f16 v90, v50, v25
v_pk_fmac_f16 v91, v51, v25
M 200,16,76
RV 96,43,48
RV 48,67,48
M 196,12,76
s_waitcnt lgkmcnt(3)
PM 92,92,26
M 192,8,76
RA 36,19,21504
RA 40,19,22528
M 188,4,76
PM 93,93,26
PM 94,94,26
M 184,0,76
RA 44,19,23552
PM 95,95,26
M 208,0,80
PH 92,52,26
M 212,4,80
v_pk_fmac_f16 v93, v53, v27
v_pk_fmac_f16 v94, v54, v27
PH 95,55,26
M 216,8,80
s_add_u32 s40, s40, 24704
RV 72,32,64
RV 52,56,64
M 220,12,80
s_waitcnt vmcnt(6) lgkmcnt(2)
PM 96,96,28
PM 97,97,28
M 224,16,80
s_barrier
PM 98,98,28
PM 99,99,28
M 228,20,80
v_pk_fmac_f16 v96, v48, v29
v_pk_fmac_f16 v97, v49, v29
PH 98,50,28
v_pk_fmac_f16 v99, v51, v29
M 160,24,88
RV 76,36,64
RV 48,60,64
M 164,28,88
s_waitcnt lgkmcnt(2)
PM 72,72,24
M 168,32,88
RA 0,16,24576
RA 4,16,25600
PM 73,73,24
M 172,36,88
PM 74,74,24
PM 75,75,24
PH 72,52,24
M 176,40,88
v_pk_fmac_f16 v73, v53, v25
v_pk_fmac_f16 v74, v54, v25
v_pk_fmac_f16 v75, v55, v25
M 180,44,88
RV 80,40,64
RV 52,64,64
s_waitcnt lgkmcnt(4)
PM 76,76,26
M 204,44,92
RA 8,16,26624
RA 12,16,27648
PM 77,77,26
M 200,40,92
s_mov_b32 m0, s41
PM 78,78,26
PM 79,79,26
v_pk_fmac_f16 v76, v48, v27
M 196,36,92
buffer_load_dwordx4 v15, s[36:39], s40 offen sc1 lds
M 192,32,92
v_pk_fmac_f16 v77, v49, v27
v_pk_fmac_f16 v78, v50, v27
v_pk_fmac_f16 v79, v51, v27
M 188,28,92
RV 84,44,64
RV 48,68,64
s_waitcnt lgkmcnt(4)
PM 80,80,28
M 184,24,92
RA 16,16,28672
RA 20,16,29696
PM 81,81,28
M 208,24,96
PM 82,82,28
PM 83,83,28
PH 80,52,28
M 212,28,96
v_pk_fmac_f16 v81, v53, v29
v_pk_fmac_f16 v82, v54, v29
v_pk_fmac_f16 v83, v55, v29
M 216,32,96
RV 88,33,80
RV 52,57,80
M 220,36,96
s_waitcnt lgkmcnt(4)
PM 84,84,30
PM 85,85,30
M 224,40,96
PM 86,86,30
PM 87,87,30
PH 84,48,30
M 228,44,96
v_pk_fmac_f16 v85, v49, v31
v_pk_fmac_f16 v86, v50, v31
v_pk_fmac_f16 v87, v51, v31
M 160,0,72
RV 92,37,80
RV 48,61,80
M 164,4,72
s_waitcnt lgkmcnt(2)
PM 88,88,24
M 168,8,72
RA 24,17,30720
RA 28,17,31744
M 172,12,72
PM 89,89,24
PM 90,90,24
M 176,16,72
RA 32,17,32768
M 180,20,72
PM 91,91,24
PH 88,52,24
M 204,20,76
buffer_load_dwordx4 v15, s[36:39], s40 offen offset:1024 sc1 lds
M 200,16,76
PH 89,53,24
M 196,12,76
PH 90,54,24
v_pk_fmac_f16 v91, v55, v25
M 192,8,76
RV 96,41,80
RV 52,65,80
M 188,4,76
s_waitcnt lgkmcnt(2)
PM 92,92,26
M 184,0,76
RA 36,17,33792
RA 40,17,34816
M 208,0,80
PM 93,93,26
PM 94,94,26
M 212,4,80
RA 44,17,35840
M 216,8,80
PM 95,95,26
PH 92,48,26
M 220,12,80
buffer_load_dwordx4 v15, s[36:39], s40 offen offset:2048 sc1 lds
M 224,16,80
PH 93,49,26
M 228,20,80
PH 94,50,26
v_pk_fmac_f16 v95, v51, v27
M 252,20,84
RV 72,34,96
M 248,16,84
RV 48,58,96
M 244,12,84
s_waitcnt lgkmcnt(2)
PM 96,96,28
PM 97,97,28
M 240,8,84
PM 98,98,28
PM 99,99,28
M 236,4,84
v_pk_fmac_f16 v96, v52, v29
v_pk_fmac_f16 v97, v53, v29
M 232,0,84
PH 98,54,28
v_pk_fmac_f16 v99, v55, v29
M 160,24,88
RV 76,38,96
RV 52,62,96
M 164,28,88
s_waitcnt lgkmcnt(2)
PM 72,72,24
M 168,32,88
RA 0,18,36864
RA 4,18,37888
M 172,36,88
PM 73,73,24
PM 74,74,24
M 176,40,88
RA 8,18,38912
PM 75,75,24
PH 72,48,24
M 180,44,88
buffer_load_dwordx4 v15, s[36:39], s40 offen offset:3072 sc1 lds
M 204,44,92
v_pk_fmac_f16 v73, v49, v25
v_pk_fmac_f16 v74, v50, v25
v_pk_fmac_f16 v75, v51, v25
M 200,40,92
RV 80,42,96
RV 48,66,96
M 196,36,92
s_waitcnt lgkmcnt(3)
PM 76,76,26
M 192,32,92
RA 12,18,39936
RA 16,18,40960
M 188,28,92
PM 77,77,26
PM 78,78,26
M 184,24,92
RA 20,18,41984
PM 79,79,26
PH 76,52,26
M 208,24,96
v_pk_fmac_f16 v77, v53, v27
v_pk_fmac_f16 v78, v54, v27
PH 79,55,26
M 212,28,96
RV 88,35,112
RV 52,59,112
M 216,32,96
s_waitcnt lgkmcnt(3)
PM 80,80,28
PM 81,81,28
M 220,36,96
s_addk_i32 m0, 4096
PM 82,82,28
PM 83,83,28
PH 80,48,28
M 224,40,96
buffer_load_dwordx4 v45, s[36:39], s40 offen sc1 lds
M 228,44,96
v_pk_fmac_f16 v81, v49, v29
v_pk_fmac_f16 v82, v50, v29
v_pk_fmac_f16 v83, v51, v29
M 160,0,72
RV 92,39,112
RV 48,63,112
M 164,4,72
s_waitcnt lgkmcnt(2)
PM 88,88,24
M 168,8,72
RA 24,19,43008
RA 28,19,44032
M 172,12,72
PM 89,89,24
PM 90,90,24
M 176,16,72
RA 32,19,45056
PM 91,91,24
PH 88,52,24
M 180,20,72
buffer_load_dwordx4 v45, s[36:39], s40 offen offset:1024 sc1 lds
M 204,20,76
v_pk_fmac_f16 v89, v53, v25
v_pk_fmac_f16 v90, v54, v25
v_pk_fmac_f16 v91, v55, v25
M 200,16,76
RV 96,43,112
RV 52,67,112
M 196,12,76
s_waitcnt lgkmcnt(3)
PM 92,92,26
M 192,8,76
RA 36,19,46080
RA 40,19,47104
M 188,4,76
PM 93,93,26
PM 94,94,26
M 184,0,76
RA 44,19,48128
PM 95,95,26
M 208,0,80
PH 92,48,26
M 212,4,80
v_pk_fmac_f16 v93, v49, v27
v_pk_fmac_f16 v94, v50, v27
PH 95,51,26
M 216,8,80
s_add_u32 s40, s40, 24704
RV 72,32,128
RV 48,56,128
M 220,12,80
s_waitcnt vmcnt(6) lgkmcnt(2)
PM 96,96,28
PM 97,97,28
M 224,16,80
s_barrier
PM 98,98,28
PM 99,99,28
M 228,20,80
v_pk_fmac_f16 v96, v52, v29
v_pk_fmac_f16 v97, v53, v29
v_pk_fmac_f16 v98, v54, v29
v_pk_fmac_f16 v99, v55, v29
M 160,24,88
RV 76,36,128
RV 52,60,128
M 164,28,88
s_branch L_in13_1_%=
.p2align 3
L_sel13_1_%=:
s_cmp_eq_u32 s55, 0
s_cbranch_scc1 L_bodz13_0_%=
s_cmp_eq_u32 s55, 1
s_cbranch_scc1 L_bodz13_1_%=
s_cmp_eq_u32 s55, 4
s_cbranch_scc1 L_tail13_0_%=
s_cmp_eq_u32 s55, 5
s_cbranch_scc1 L_tail13_1_%=
s_cmp_lt_u32 s55, 4
s_cbranch_scc1 L_bodz13_2_%=
s_branch L_tail13_2_%=
L_disp13_1_%=:
M 160,24,88
s_cmp_lg_u32 s55, 5
s_cbranch_scc1 L_sel13_1_%=
L_tail13_1_%=:
s_mul_i32 s63, s25, 6144
v_add_u32 v16, s63, v14
s_mul_i32 s64, s61, 384
v_add_u32_e64 v17, s64, v16
M 164,28,88
RA 0,16,0
s_mul_i32 s65, s62, 384
v_add_u32 v18, s65, v16
v_add_u32 v44, s60, v155
v_mov_b32_e64 v9, v159
M 168,32,88
RA 4,16,1024
v_pk_max_i16 v31, v9, s50
v_pk_min_i16 v31, v31, s52
v_mad_i32_i16 v68, v31, s53, v44
M 172,36,88
RA 8,16,2048
v_mad_i32_i16 v68, v31, s54, v68 op_sel:[1,0,0,0]
v_add_u32 v32, s60, v152
v_mov_b32 v6, v156
v_pk_max_i16 v25, v6, s50
M 176,40,88
RA 12,16,3072
v_pk_min_i16 v25, v25, s52
v_mad_i32_i16 v56, v25, s53, v32
v_mad_i32_i16 v56, v25, s54, v56 op_sel:[1,0,0,0]
M 180,44,88
RA 16,16,4096
v_add_u32 v36, s60, v153
v_mov_b32 v7, v157
v_pk_max_i16 v27, v7, s50
v_pk_min_i16 v27, v27, s52
M 204,44,92
RA 20,16,5120
v_mad_i32_i16 v60, v27, s53, v36
v_mad_i32_i16 v60, v27, s54, v60 op_sel:[1,0,0,0]
v_add_u32_e64 v40, s60, v154
M 200,40,92
RV 104,44,0
v_mov_b32_e64 v8, v158
v_pk_max_i16 v29, v8, s50
v_pk_min_i16 v29, v29, s52
v_mad_i32_i16 v64, v29, s53, v40
M 196,36,92
RV 48,68,0
v_mad_i32_i16 v64, v29, s54, v64 op_sel:[1,0,0,0]
v_add_u32 v33, s61, v32
v_add_u32 v57, s61, v56
M 192,32,92
RV 108,32,0
v_add_u32 v37, s61, v36
v_add_u32 v61, s61, v60
v_add_u32 v41, s61, v40
v_add_u32 v65, s61, v64
M 188,28,92
RV 52,56,0
v_add_u32 v34, s62, v32
v_add_u32 v58, s62, v56
v_add_u32 v38, s62, v36
v_add_u32 v62, s62, v60
M 184,24,92
v_add_u32 v42, s62, v40
v_add_u32 v66, s62, v64
v_and_b32_e64 v9, s56, v9
v_pk_add_f16 v31, v9, v9 op_sel:[0,1] op_sel_hi:[1,0]
M 208,24,96
RV 112,33,16
v_pk_add_f16 v30, v31, s51 neg_lo:[1,0]
v_and_b32_e64 v6, s56, v6
v_pk_add_f16 v25, v6, v6 op_sel:[0,1] op_sel_hi:[1,0]
v_pk_add_f16 v24, v25, s51 neg_lo:[1,0]
M 212,28,96
RV 72,57,16
v_and_b32_e64 v7, s56, v7
v_pk_add_f16 v27, v7, v7 op_sel:[0,1] op_sel_hi:[1,0]
v_pk_add_f16 v26, v27, s51 neg_lo:[1,0]
M 216,32,96
RA 68,17,11264
v_and_b32 v8, s56, v8
v_pk_add_f16 v29, v8, v8 op_sel:[0,1] op_sel_hi:[1,0]
v_pk_add_f16 v28, v29, s51 neg_lo:[1,0]
s_waitcnt lgkmcnt(3)
PM 104,104,30
M 220,36,96
RA 64,17,10240
PM 105,105,30
PM 106,106,30
PM 107,107,30
v_pk_fmac_f16 v104, v48, v31
v_pk_fmac_f16 v105, v49, v31
v_pk_fmac_f16 v106, v50, v31
v_pk_fmac_f16 v107, v51, v31
M 224,40,96
RA 60,17,9216
M 228,44,96
RA 56,17,8192
PM 108,108,24
PM 109,109,24
PM 110,110,24
M 232,0,104
RA 52,17,7168
PM 111,111,24
v_pk_fmac_f16 v108, v52, v25
v_pk_fmac_f16 v109, v53, v25
M 236,4,104
RA 48,17,6144
PH 110,54,24
v_pk_fmac_f16 v111, v55, v25
M 240,8,104
RV 116,34,32
s_waitcnt lgkmcnt(5)
PM 112,112,24
PM 113,113,24
PM 114,114,24
M 244,12,104
RV 76,58,32
PM 115,115,24
v_pk_fmac_f16 v112, v72, v25
v_pk_fmac_f16 v113, v73, v25
M 248,16,104
RA 72,18,12288
PH 114,74,24
v_pk_fmac_f16 v115, v75, v25
M 252,20,104
RA 76,18,13312
s_mul_i32 s11, s23, 192
s_mul_i32 s16, s21, 43008
s_add_u32 s16, s16, s11
M 160,0,108
RA 80,18,14336
s_add_u32 s17, s16, 43008
s_add_u32 s18, s16, 86016
s_mul_i32 s19, s22, 43008
s_add_u32 s19, s19, s11
s_lshl_b32 s11, s10, 6
s_add_u32 s19, s19, s11
s_lshl_b32 s11, s25, 11
v_add_u32 v21, s11, v14
v_add_u32 v20, s41, v15
v_and_b32 v47, 0xfff, v46
v_add_u32 v12, 162304, v15
s_waitcnt vmcnt(0)
M 164,4,108
M 168,8,108
M 172,12,108
ds_write_b128 v20, v[232:235] offset:24576
M 176,16,108
RA 84,18,15360
s_waitcnt lgkmcnt(2)
PM 116,116,24
M 180,20,108
RA 88,18,16384
PM 117,117,24
PM 118,118,24
PM 119,119,24
M 180,68,112
ds_write_b128 v20, v[236:239] offset:25600
PH 116,76,24
M 176,64,112
RA 92,18,17408
v_pk_fmac_f16 v117, v77, v25
v_pk_fmac_f16 v118, v78, v25
PH 119,79,24
M 172,60,112
ds_write_b128 v20, v[240:243] offset:26624
M 168,56,112
RV 120,36,0
RV 48,60,0
M 164,52,112
RV 124,37,16
M 160,48,112
ds_write_b128 v20, v[244:247] offset:27648
M 160,72,116
RV 52,61,16
M 164,76,116
RV 128,38,32
M 168,80,116
ds_write_b128 v20, v[248:251] offset:28672
s_waitcnt lgkmcnt(5)
PM 120,120,26
PM 121,121,26
M 172,84,116
RV 72,62,32
PM 122,122,26
PM 123,123,26
v_pk_fmac_f16 v120, v48, v27
v_pk_fmac_f16 v121, v49, v27
v_pk_fmac_f16 v122, v50, v27
v_pk_fmac_f16 v123, v51, v27
M 176,88,116
M 180,92,116
ds_write_b128 v20, v[252:255] offset:29696
s_waitcnt lgkmcnt(2)
PM 124,124,26
PM 125,125,26
M 184,0,120
RV 132,40,0
PM 126,126,26
PM 127,127,26
M 188,4,120
RV 76,64,0
v_pk_fmac_f16 v124, v52, v27
v_pk_fmac_f16 v125, v53, v27
PH 126,54,26
M 192,8,120
RV 136,41,16
v_pk_fmac_f16 v127, v55, v27
s_waitcnt lgkmcnt(4)
PM 128,128,26
M 196,12,120
RV 48,65,16
PM 129,129,26
PM 130,130,26
PM 131,131,26
M 200,16,120
RV 140,42,32
v_pk_fmac_f16 v128, v72, v27
v_pk_fmac_f16 v129, v73, v27
M 204,20,120
RV 52,66,32
PH 130,74,26
v_pk_fmac_f16 v131, v75, v27
v_cvt_pk_bf16_f32 v88, v160, v161
M 204,68,124
RV 84,12,0
s_waitcnt lgkmcnt(5)
PM 132,132,28
PM 133,133,28
M 200,64,124
PM 134,134,28
PM 135,135,28
v_pk_fmac_f16 v132, v76, v29
v_pk_fmac_f16 v133, v77, v29
M 196,60,124
v_pk_fmac_f16 v134, v78, v29
v_pk_fmac_f16 v135, v79, v29
v_cvt_pk_bf16_f32 v89, v162, v163
v_cvt_pk_bf16_f32 v90, v164, v165
M 192,56,124
v_cvt_pk_bf16_f32 v91, v166, v167
v_cvt_pk_bf16_f32 v92, v168, v169
v_cvt_pk_bf16_f32 v93, v170, v171
M 188,52,124
buffer_store_dwordx4 v[88:91], v47, s[68:71], s16 offen offset:0 sc0 sc1 nt
v_cvt_pk_bf16_f32 v94, v172, v173
v_cvt_pk_bf16_f32 v95, v174, v175
M 184,48,124
v_cvt_pk_bf16_f32 v96, v176, v177
v_cvt_pk_bf16_f32 v97, v178, v179
v_cvt_pk_bf16_f32 v98, v180, v181
M 184,72,128
s_waitcnt lgkmcnt(0)
PM 136,136,28
PM 137,137,28
PM 138,138,28
PM 139,139,28
M 188,76,128
v_pk_fmac_f16 v136, v48, v29
v_pk_fmac_f16 v137, v49, v29
v_pk_fmac_f16 v138, v50, v29
M 192,80,128
buffer_store_dwordx4 v[92:95], v47, s[68:71], s16 offen offset:64 sc0 sc1 nt
v_pk_fmac_f16 v139, v51, v29
v_cvt_pk_bf16_f32 v99, v182, v183
M 196,84,128
v_cvt_pk_bf16_f32 v88, v184, v185
v_cvt_pk_bf16_f32 v89, v186, v187
v_cvt_pk_bf16_f32 v90, v188, v189
M 200,88,128
v_cvt_pk_bf16_f32 v91, v190, v191
v_cvt_pk_bf16_f32 v92, v192, v193
v_cvt_pk_bf16_f32 v93, v194, v195
s_waitcnt lgkmcnt(0)
M 204,92,128
ds_write_b128 v4, v[88:91] offset:0
v_cvt_pk_bf16_f32 v94, v196, v197
M 208,0,132
buffer_store_dwordx4 v[96:99], v47, s[68:71], s16 offen offset:128 sc0 sc1 nt
PM 140,140,28
PM 141,141,28
M 212,4,132
PM 142,142,28
PM 143,143,28
v_pk_fmac_f16 v140, v52, v29
v_pk_fmac_f16 v141, v53, v29
M 216,8,132
PH 142,54,28
v_pk_fmac_f16 v143, v55, v29
v_cvt_pk_bf16_f32 v95, v198, v199
M 220,12,132
ds_write_b128 v4, v[92:95] offset:64
v_cvt_pk_bf16_f32 v96, v200, v201
v_cvt_pk_bf16_f32 v97, v202, v203
M 224,16,132
v_cvt_pk_bf16_f32 v98, v204, v205
v_cvt_pk_bf16_f32 v99, v206, v207
s_barrier
M 228,20,132
ds_write_b128 v4, v[96:99] offset:128
RV 144,1,0
v_or3_b32 v84, v84, v85, v86
M 228,68,136
RV 148,2,0
RV 100,3,0
v_or3_b32 v84, v84, v87, v5
M 224,64,136
RV 232,21,24576
v_and_b32 v85, 0x40004001, v84
v_and_b32 v86, 0x70007000, v84
v_and_b32_e64 v87, 2, v84
M 220,60,136
RV 236,21,25600
v_cmp_ne_u32 vcc, 0, v85
v_cmp_eq_u32 s[30:31], 0, v86
M 216,56,136
RV 240,21,30720
v_cmp_eq_u32 s[58:59], 0, v87
s_cmp_eq_u64 s[30:31], -1
s_cselect_b64 vcc, -1, vcc
M 212,52,136
RV 244,21,31744
s_cmp_eq_u64 s[58:59], -1
s_cselect_b64 vcc, -1, vcc
M 208,48,136
RV 248,21,36864
M 208,72,140
s_waitcnt lgkmcnt(4)
buffer_store_dwordx4 v[144:147], v15, s[68:71], s17 offen offset:0 sc0 sc1 nt
M 212,76,140
RV 252,21,37888
M 216,80,140
RV 104,21,43008
M 220,84,140
buffer_store_dwordx4 v[148:151], v15, s[68:71], s17 offen offset:1024 sc0 sc1 nt
v_cvt_pk_bf16_f32 v88, v208, v209
v_cvt_pk_bf16_f32 v89, v210, v211
M 224,88,140
RV 108,21,44032
v_cvt_pk_bf16_f32 v90, v212, v213
v_cvt_pk_bf16_f32 v91, v214, v215
v_cvt_pk_bf16_f32 v92, v216, v217
M 228,92,140
buffer_store_dwordx4 v[100:103], v15, s[68:71], s17 offen offset:2048 sc0 sc1 nt
v_cvt_pk_bf16_f32 v93, v218, v219
ds_write_b128 v4, v[88:91] offset:0
v_cvt_pk_bf16_f32 v94, v220, v221
v_cvt_pk_bf16_f32 v95, v222, v223
v_cvt_pk_bf16_f32 v96, v224, v225
v_cvt_pk_bf16_f32 v97, v226, v227
ds_write_b128 v4, v[92:95] offset:64
v_cvt_pk_bf16_f32 v98, v228, v229
v_cvt_pk_bf16_f32 v99, v230, v231
s_waitcnt lgkmcnt(2)
v_add_f32 v232, v232, v240
v_add_f32 v233, v233, v241
v_add_f32 v234, v234, v242
v_add_f32_e64 v235, v235, v243
ds_write_b128 v4, v[96:99] offset:128
RV 144,1,0
RV 148,2,0
RV 100,3,0
v_add_f32 v236, v236, v244
v_add_f32 v237, v237, v245
v_add_f32 v238, v238, v246
v_add_f32 v239, v239, v247
v_add_f32 v248, v248, v104
v_add_f32 v249, v249, v105
v_add_f32 v250, v250, v106
v_add_f32 v251, v251, v107
v_add_f32 v252, v252, v108
v_add_f32 v253, v253, v109
v_add_f32 v254, v254, v110
v_add_f32 v255, v255, v111
v_add_f32 v232, v232, v248
v_add_f32 v233, v233, v249
v_add_f32 v234, v234, v250
v_add_f32 v235, v235, v251
v_add_f32 v236, v236, v252
v_add_f32 v237, v237, v253
v_add_f32 v238, v238, v254
v_add_f32 v239, v239, v255
v_cvt_pk_bf16_f32 v240, v232, v233
v_cvt_pk_bf16_f32 v241, v234, v235
v_cvt_pk_bf16_f32 v242, v236, v237
v_cvt_pk_bf16_f32 v243, v238, v239
s_waitcnt lgkmcnt(0)
buffer_store_dwordx4 v[144:147], v15, s[68:71], s18 offen offset:0 sc0 sc1 nt
buffer_store_dwordx4 v[148:151], v15, s[68:71], s18 offen offset:1024 sc0 sc1 nt
buffer_store_dwordx4 v[100:103], v15, s[68:71], s18 offen offset:2048 sc0 sc1 nt
buffer_store_dwordx4 v[240:243], v46, s[68:71], s19 offen offset:0 sc0 sc1 nt
s_cbranch_vccnz L_done_%=
s_endpgm
.p2align 3
L_ent13_2_%=:
RA 0,16,0
RA 4,16,1024
RA 8,16,2048
RA 12,16,3072
RA 16,16,4096
RA 20,16,5120
s_set_gpr_idx_on s43, gpr_idx(SRC1)
v_add_u32 v32, s44, v104
v_add_u32 v36, s44, v105
v_add_u32 v40, s44, v106
v_add_u32 v44, s44, v107
v_add_u32 v6, 0, v108
v_add_u32 v7, 0, v109
v_add_u32 v8, 0, v110
v_add_u32 v9, 0, v111
s_set_gpr_idx_off
v_pk_ashrrev_i16 v25, 15, v6 op_sel_hi:[0,1]
v_mad_i32_i16 v32, v25, s53, v32
v_mad_i32_i16 v32, v25, s54, v32 op_sel:[1,0,0,0]
v_pk_ashrrev_i16 v27, 15, v7 op_sel_hi:[0,1]
v_mad_i32_i16 v36, v27, s53, v36
v_mad_i32_i16 v36, v27, s54, v36 op_sel:[1,0,0,0]
v_pk_ashrrev_i16 v29, 15, v8 op_sel_hi:[0,1]
v_mad_i32_i16 v40, v29, s53, v40
v_mad_i32_i16 v40, v29, s54, v40 op_sel:[1,0,0,0]
v_pk_ashrrev_i16 v31, 15, v9 op_sel_hi:[0,1]
v_mad_i32_i16 v44, v31, s53, v44
v_mad_i32_i16 v44, v31, s54, v44 op_sel:[1,0,0,0]
v_and_b32 v25, 0x3c003c00, v25
v_pk_add_f16 v6, v6, v25
v_pk_add_f16 v25, v6, s51 op_sel:[1,0] neg_lo:[1,0]
v_pk_add_f16 v6, v6, s51 op_sel_hi:[0,1] neg_lo:[1,0]
PM 24,25,6
v_pk_mul_f16 v25, v25, v6 op_sel:[0,1]
v_and_b32 v27, 0x3c003c00, v27
v_pk_add_f16 v7, v7, v27
v_pk_add_f16 v27, v7, s51 op_sel:[1,0] neg_lo:[1,0]
v_pk_add_f16 v7, v7, s51 op_sel_hi:[0,1] neg_lo:[1,0]
PM 26,27,7
v_pk_mul_f16 v27, v27, v7 op_sel:[0,1]
v_and_b32 v29, 0x3c003c00, v29
v_pk_add_f16 v8, v8, v29
v_pk_add_f16 v29, v8, s51 op_sel:[1,0] neg_lo:[1,0]
v_pk_add_f16 v8, v8, s51 op_sel_hi:[0,1] neg_lo:[1,0]
PM 28,29,8
v_pk_mul_f16 v29, v29, v8 op_sel:[0,1]
v_and_b32 v31, 0x3c003c00, v31
v_pk_add_f16 v9, v9, v31
v_pk_add_f16 v31, v9, s51 op_sel:[1,0] neg_lo:[1,0]
v_pk_add_f16 v9, v9, s51 op_sel_hi:[0,1] neg_lo:[1,0]
PM 30,31,9
v_pk_mul_f16 v31, v31, v9 op_sel:[0,1]
v_add_u32 v33, s45, v32
v_add_u32 v37, s45, v36
v_add_u32 v41, s45, v40
v_add_u32 v34, s46, v32
v_add_u32 v38, s46, v36
v_add_u32 v42, s46, v40
v_add_u32 v35, s47, v32
v_add_u32 v39, s47, v36
v_add_u32 v43, s47, v40
s_waitcnt lgkmcnt(6)
s_barrier
RV 72,32,0
RV 48,32,208
RV 52,32,4608
RV 56,32,4816
RV 76,36,0
RV 60,36,208
s_waitcnt lgkmcnt(2)
PM 72,72,24
PM 73,73,24
RV 64,36,4608
RV 68,36,4816
PM 74,74,24
PM 75,75,24
PH 72,48,24
PH 73,49,24
PH 74,50,24
PH 75,51,24
PL 72,52,25
PL 73,53,25
PL 74,54,25
PL 75,55,25
PH 72,56,25
PH 73,57,25
PH 74,58,25
PH 75,59,25
RV 80,40,0
RV 48,40,208
s_waitcnt lgkmcnt(2)
PM 76,76,26
PM 77,77,26
RV 52,40,4608
RV 56,40,4816
PM 78,78,26
PM 79,79,26
PH 76,60,26
PH 77,61,26
PH 78,62,26
PH 79,63,26
PL 76,64,27
PL 77,65,27
PL 78,66,27
PL 79,67,27
PH 76,68,27
PH 77,69,27
PH 78,70,27
PH 79,71,27
RV 84,44,0
RV 60,44,208
s_waitcnt lgkmcnt(2)
PM 80,80,28
PM 81,81,28
RV 64,44,4608
RV 68,44,4816
PM 82,82,28
PM 83,83,28
PH 80,48,28
PH 81,49,28
PH 82,50,28
PH 83,51,28
PL 80,52,29
PL 81,53,29
PL 82,54,29
PL 83,55,29
PH 80,56,29
PH 81,57,29
PH 82,58,29
PH 83,59,29
RV 88,33,16
RV 48,33,224
s_waitcnt lgkmcnt(2)
PM 84,84,30
PM 85,85,30
RV 52,33,4624
RV 56,33,4832
PM 86,86,30
PM 87,87,30
PH 84,60,30
PH 85,61,30
PH 86,62,30
PH 87,63,30
PL 84,64,31
PL 85,65,31
PL 86,66,31
PL 87,67,31
PH 84,68,31
PH 85,69,31
PH 86,70,31
PH 87,71,31
s_waitcnt lgkmcnt(0)
s_nop 0
v_mfma_f32_16x16x32_f16 v[160:163], a[0:3], v[72:75], 0
s_add_u32 m0, s41, 24576
RV 92,37,16
RV 60,37,224
v_mfma_f32_16x16x32_f16 v[164:167], a[4:7], v[72:75], 0
buffer_load_dwordx4 v15, s[36:39], s40 offen sc1 lds
s_waitcnt lgkmcnt(2)
PM 88,88,24
PM 89,89,24
v_mfma_f32_16x16x32_f16 v[168:171], a[8:11], v[72:75], 0
RV 64,37,4624
RV 68,37,4832
v_mfma_f32_16x16x32_f16 v[172:175], a[12:15], v[72:75], 0
buffer_load_dwordx4 v15, s[36:39], s40 offen offset:1024 sc1 lds
PM 90,90,24
PM 91,91,24
PH 88,48,24
v_mfma_f32_16x16x32_f16 v[176:179], a[16:19], v[72:75], 0
RA 24,17,6144
RA 28,17,7168
v_mfma_f32_16x16x32_f16 v[180:183], a[20:23], v[72:75], 0
buffer_load_dwordx4 v15, s[36:39], s40 offen offset:2048 sc1 lds
PH 89,49,24
PH 90,50,24
PH 91,51,24
v_mfma_f32_16x16x32_f16 v[204:207], a[20:23], v[76:79], 0
RA 32,17,8192
PL 88,52,25
PL 89,53,25
v_mfma_f32_16x16x32_f16 v[200:203], a[16:19], v[76:79], 0
PL 90,54,25
PL 91,55,25
PH 88,56,25
v_mfma_f32_16x16x32_f16 v[196:199], a[12:15], v[76:79], 0
buffer_load_dwordx4 v15, s[36:39], s40 offen offset:3072 sc1 lds
PH 89,57,25
PH 90,58,25
PH 91,59,25
v_mfma_f32_16x16x32_f16 v[192:195], a[8:11], v[76:79], 0
s_add_u32 m0, m0, 4096
v_mfma_f32_16x16x32_f16 v[188:191], a[4:7], v[76:79], 0
buffer_load_dwordx4 v45, s[36:39], s40 offen sc1 lds
v_mfma_f32_16x16x32_f16 v[184:187], a[0:3], v[76:79], 0
RV 96,41,16
RV 48,41,224
v_mfma_f32_16x16x32_f16 v[208:211], a[0:3], v[80:83], 0
buffer_load_dwordx4 v45, s[36:39], s40 offen offset:1024 sc1 lds
s_waitcnt lgkmcnt(2)
PM 92,92,26
PM 93,93,26
v_mfma_f32_16x16x32_f16 v[212:215], a[4:7], v[80:83], 0
s_addk_i32 s40, 24704
RV 52,41,4624
RV 56,41,4832
v_mfma_f32_16x16x32_f16 v[216:219], a[8:11], v[80:83], 0
PM 94,94,26
PM 95,95,26
PH 92,60,26
v_mfma_f32_16x16x32_f16 v[220:223], a[12:15], v[80:83], 0
RA 36,17,9216
RA 40,17,10240
PH 93,61,26
PH 94,62,26
PH 95,63,26
v_mfma_f32_16x16x32_f16 v[224:227], a[16:19], v[80:83], 0
RA 44,17,11264
PL 92,64,27
PL 93,65,27
v_mfma_f32_16x16x32_f16 v[228:231], a[20:23], v[80:83], 0
PL 94,66,27
PL 95,67,27
PH 92,68,27
v_mfma_f32_16x16x32_f16 v[252:255], a[20:23], v[84:87], 0
PH 93,69,27
PH 94,70,27
PH 95,71,27
v_mfma_f32_16x16x32_f16 v[248:251], a[16:19], v[84:87], 0
RV 72,34,32
RV 60,34,240
s_waitcnt lgkmcnt(5)
PM 96,96,28
PM 97,97,28
v_mfma_f32_16x16x32_f16 v[244:247], a[12:15], v[84:87], 0
RV 64,34,4640
RV 68,34,4848
PM 98,98,28
PM 99,99,28
v_mfma_f32_16x16x32_f16 v[240:243], a[8:11], v[84:87], 0
PH 96,48,28
PH 97,49,28
PH 98,50,28
PH 99,51,28
v_mfma_f32_16x16x32_f16 v[236:239], a[4:7], v[84:87], 0
PL 96,52,29
PL 97,53,29
PL 98,54,29
PL 99,55,29
v_mfma_f32_16x16x32_f16 v[232:235], a[0:3], v[84:87], 0
PH 96,56,29
PH 97,57,29
PH 98,58,29
PH 99,59,29
M 160,24,88
RV 76,38,32
RV 48,38,240
s_waitcnt lgkmcnt(2)
PM 72,72,24
PM 73,73,24
M 164,28,88
RV 52,38,4640
RV 56,38,4848
PM 74,74,24
PM 75,75,24
PH 72,60,24
M 168,32,88
RA 0,18,12288
RA 4,18,13312
PH 73,61,24
PH 74,62,24
PH 75,63,24
M 172,36,88
RA 8,18,14336
PL 72,64,25
PL 73,65,25
M 176,40,88
PL 74,66,25
PL 75,67,25
PH 72,68,25
M 180,44,88
PH 73,69,25
PH 74,70,25
PH 75,71,25
M 204,44,92
M 200,40,92
RV 80,42,32
RV 60,42,240
s_waitcnt lgkmcnt(3)
PM 76,76,26
PM 77,77,26
M 196,36,92
RV 64,42,4640
RV 68,42,4848
PM 78,78,26
PM 79,79,26
PH 76,48,26
M 192,32,92
RA 12,18,15360
RA 16,18,16384
PH 77,49,26
PH 78,50,26
PH 79,51,26
M 188,28,92
RA 20,18,17408
PL 76,52,27
PL 77,53,27
M 184,24,92
PL 78,54,27
PL 79,55,27
PH 76,56,27
M 208,24,96
PH 77,57,27
PH 78,58,27
PH 79,59,27
M 212,28,96
RV 88,35,48
RV 48,35,256
s_waitcnt lgkmcnt(5)
PM 80,80,28
PM 81,81,28
M 216,32,96
RV 52,35,4656
RV 56,35,4864
PM 82,82,28
PM 83,83,28
M 220,36,96
PH 80,60,28
PH 81,61,28
PH 82,62,28
PH 83,63,28
M 224,40,96
PL 80,64,29
PL 81,65,29
PL 82,66,29
PL 83,67,29
PH 80,68,29
M 228,44,96
PH 81,69,29
PH 82,70,29
PH 83,71,29
M 160,0,72
RV 92,39,48
RV 60,39,256
s_waitcnt lgkmcnt(2)
PM 88,88,24
PM 89,89,24
M 164,4,72
RV 64,39,4656
RV 68,39,4864
PM 90,90,24
PM 91,91,24
PH 88,48,24
M 168,8,72
RA 24,19,18432
RA 28,19,19456
PH 89,49,24
PH 90,50,24
PH 91,51,24
M 172,12,72
RA 32,19,20480
PL 88,52,25
PL 89,53,25
M 176,16,72
PL 90,54,25
PL 91,55,25
PH 88,56,25
M 180,20,72
PH 89,57,25
PH 90,58,25
PH 91,59,25
M 204,20,76
RV 96,43,48
RV 48,43,256
s_waitcnt lgkmcnt(5)
PM 92,92,26
PM 93,93,26
M 200,16,76
RV 52,43,4656
RV 56,43,4864
PM 94,94,26
PM 95,95,26
PH 92,60,26
M 196,12,76
RA 36,19,21504
RA 40,19,22528
PH 93,61,26
PH 94,62,26
PH 95,63,26
M 192,8,76
RA 44,19,23552
PL 92,64,27
PL 93,65,27
M 188,4,76
PL 94,66,27
PL 95,67,27
PH 92,68,27
v_cmp_nge_f32 vcc, 1.0, v12
M 184,0,76
buffer_load_dwordx4 v[6:9], v15, s[36:39], 0 offen sc1
PH 93,69,27
PH 94,70,27
PH 95,71,27
v_and_b32 v13, 0x7ff87ff8, v13
M 208,0,80
RV 72,32,64
RV 60,32,272
s_waitcnt lgkmcnt(5)
PM 96,96,28
PM 97,97,28
s_lshl2_add_u32 s11, s10, 162304
M 212,4,80
buffer_load_dwordx4 v[100:103], v47, s[36:39], 0 offen offset:1024 sc1
RV 64,32,4672
RV 68,32,4880
PM 98,98,28
v_addc_co_u32 v13, vcc, 0, v13, vcc
M 216,8,80
PM 99,99,28
PH 96,48,28
PH 97,49,28
v_add_u32 v12, s11, v15
M 220,12,80
s_waitcnt vmcnt(2) lgkmcnt(4)
PH 98,50,28
PH 99,51,28
PL 96,52,29
M 224,16,80
s_barrier
PL 97,53,29
PL 98,54,29
PL 99,55,29
M 228,20,80
PH 96,56,29
PH 97,57,29
PH 98,58,29
PH 99,59,29
M 160,24,88
RV 76,36,64
RV 48,36,272
s_waitcnt lgkmcnt(2)
PM 72,72,24
PM 73,73,24
M 164,28,88
ds_write_b32 v12, v13
RV 52,36,4672
RV 56,36,4880
PM 74,74,24
PM 75,75,24
PH 72,60,24
M 168,32,88
RA 0,16,24576
RA 4,16,25600
PH 73,61,24
PH 74,62,24
M 172,36,88
PH 75,63,24
PL 72,64,25
PL 73,65,25
PL 74,66,25
PL 75,67,25
M 176,40,88
PH 72,68,25
PH 73,69,25
PH 74,70,25
PH 75,71,25
M 180,44,88
RV 80,40,64
RV 60,40,272
s_waitcnt lgkmcnt(4)
PM 76,76,26
PM 77,77,26
M 204,44,92
RV 64,40,4672
RV 68,40,4880
PM 78,78,26
PM 79,79,26
PH 76,48,26
M 200,40,92
RA 8,16,26624
RA 12,16,27648
PH 77,49,26
PH 78,50,26
PH 79,51,26
M 196,36,92
PL 76,52,27
PL 77,53,27
PL 78,54,27
PL 79,55,27
PH 76,56,27
M 192,32,92
PH 77,57,27
PH 78,58,27
PH 79,59,27
M 188,28,92
RV 84,44,64
RV 48,44,272
s_waitcnt lgkmcnt(4)
PM 80,80,28
PM 81,81,28
M 184,24,92
RV 52,44,4672
RV 56,44,4880
PM 82,82,28
PM 83,83,28
PH 80,60,28
M 208,24,96
RA 16,16,28672
RA 20,16,29696
PH 81,61,28
PH 82,62,28
PH 83,63,28
PL 80,64,29
M 212,28,96
PL 81,65,29
PL 82,66,29
PL 83,67,29
PH 80,68,29
PH 81,69,29
PH 82,70,29
PH 83,71,29
M 216,32,96
RV 88,33,80
RV 60,33,288
s_waitcnt lgkmcnt(4)
PM 84,84,30
PM 85,85,30
M 220,36,96
RV 64,33,4688
RV 68,33,4896
PM 86,86,30
PM 87,87,30
PH 84,48,30
M 224,40,96
PH 85,49,30
PH 86,50,30
PH 87,51,30
PL 84,52,31
PL 85,53,31
M 228,44,96
PL 86,54,31
PL 87,55,31
PH 84,56,31
PH 85,57,31
PH 86,58,31
PH 87,59,31
M 160,0,72
RV 92,37,80
RV 48,37,288
s_waitcnt vmcnt(0)
v_bitop3_b32 v6, v6, v8, s66 bitop3:0x96
M 164,4,72
s_waitcnt lgkmcnt(2)
PM 88,88,24
PM 89,89,24
v_bitop3_b32 v100, v100, v102, s66 bitop3:0x96
v_bitop3_b32 v7, v7, v9, s67 bitop3:0x96
M 168,8,72
RV 52,37,4688
RV 56,37,4896
v_bitop3_b32 v101, v101, v103, s67 bitop3:0x96
v_or_b32_e64 v6, v6, v100
M 172,12,72
PM 90,90,24
PM 91,91,24
PH 88,60,24
v_or_b32_e64 v5, v7, v101
v_cmp_ne_u32 vcc, 0, v6
M 176,16,72
RA 24,17,30720
RA 28,17,31744
v_cmp_lt_u32 s[30:31], 7, v5
s_or_b64 vcc, vcc, s[30:31]
M 180,20,72
PH 89,61,24
PH 90,62,24
PH 91,63,24
s_cbranch_scc1 L_ag13_2_%=
L_ok13_2_%=:
M 204,20,76
s_add_u32 m0, s41, 49152
RA 32,17,32768
PL 88,64,25
PL 89,65,25
M 200,16,76
buffer_load_dwordx4 v15, s[36:39], s40 offen sc1 lds
PL 90,66,25
PL 91,67,25
PH 88,68,25
M 196,12,76
M 192,8,76
PH 89,69,25
PH 90,70,25
PH 91,71,25
M 188,4,76
buffer_load_dwordx4 v15, s[36:39], s40 offen offset:1024 sc1 lds
RV 96,41,80
RV 60,41,288
M 184,0,76
s_waitcnt lgkmcnt(2)
PM 92,92,26
PM 93,93,26
M 208,0,80
RV 64,41,4688
RV 68,41,4896
M 212,4,80
buffer_load_dwordx4 v15, s[36:39], s40 offen offset:2048 sc1 lds
PM 94,94,26
PM 95,95,26
PH 92,48,26
M 216,8,80
RA 36,17,33792
RA 40,17,34816
M 220,12,80
PH 93,49,26
PH 94,50,26
PH 95,51,26
M 224,16,80
buffer_load_dwordx4 v15, s[36:39], s40 offen offset:3072 sc1 lds
RA 44,17,35840
PL 92,52,27
PL 93,53,27
M 228,20,80
s_addk_i32 m0, 4096
PL 94,54,27
PL 95,55,27
PH 92,56,27
M 252,20,84
PH 93,57,27
PH 94,58,27
PH 95,59,27
M 248,16,84
buffer_load_dwordx4 v45, s[36:39], s40 offen sc1 lds
RV 72,34,96
RV 48,34,304
s_waitcnt lgkmcnt(3)
PM 96,96,28
PM 97,97,28
M 244,12,84
RV 52,34,4704
RV 56,34,4912
PM 98,98,28
PM 99,99,28
M 240,8,84
PH 96,60,28
PH 97,61,28
PH 98,62,28
PH 99,63,28
M 236,4,84
buffer_load_dwordx4 v45, s[36:39], s40 offen offset:1024 sc1 lds
PL 96,64,29
PL 97,65,29
PL 98,66,29
PL 99,67,29
M 232,0,84
s_add_u32 s40, s40, 24704
PH 96,68,29
PH 97,69,29
PH 98,70,29
PH 99,71,29
M 160,24,88
RV 76,38,96
RV 60,38,304
s_waitcnt lgkmcnt(2)
PM 72,72,24
PM 73,73,24
M 164,28,88
RV 64,38,4704
RV 68,38,4912
PM 74,74,24
PM 75,75,24
PH 72,48,24
M 168,32,88
s_add_u32 m0, s41, 0
RA 0,18,36864
RA 4,18,37888
PH 73,49,24
PH 74,50,24
PH 75,51,24
M 172,36,88
buffer_load_dwordx4 v15, s[36:39], s40 offen sc1 lds
RA 8,18,38912
PL 72,52,25
PL 73,53,25
M 176,40,88
PL 74,54,25
PL 75,55,25
PH 72,56,25
M 180,44,88
PH 73,57,25
PH 74,58,25
PH 75,59,25
M 204,44,92
RV 80,42,96
RV 48,42,304
s_waitcnt lgkmcnt(5)
PM 76,76,26
PM 77,77,26
M 200,40,92
buffer_load_dwordx4 v15, s[36:39], s40 offen offset:1024 sc1 lds
RV 52,42,4704
RV 56,42,4912
PM 78,78,26
PM 79,79,26
PH 76,60,26
M 196,36,92
RA 12,18,39936
RA 16,18,40960
PH 77,61,26
PH 78,62,26
PH 79,63,26
M 192,32,92
RA 20,18,41984
PL 76,64,27
PL 77,65,27
M 188,28,92
PL 78,66,27
PL 79,67,27
PH 76,68,27
M 184,24,92
PH 77,69,27
PH 78,70,27
PH 79,71,27
M 208,24,96
buffer_load_dwordx4 v15, s[36:39], s40 offen offset:2048 sc1 lds
RV 88,35,112
RV 60,35,320
s_waitcnt lgkmcnt(5)
PM 80,80,28
PM 81,81,28
M 212,28,96
RV 64,35,4720
RV 68,35,4928
PM 82,82,28
M 216,32,96
PM 83,83,28
PH 80,48,28
PH 81,49,28
M 220,36,96
PH 82,50,28
PH 83,51,28
PL 80,52,29
M 224,40,96
PL 81,53,29
PL 82,54,29
PL 83,55,29
PH 80,56,29
M 228,44,96
buffer_load_dwordx4 v15, s[36:39], s40 offen offset:3072 sc1 lds
PH 81,57,29
PH 82,58,29
PH 83,59,29
M 160,0,72
s_addk_i32 m0, 4096
RV 92,39,112
RV 48,39,320
s_waitcnt lgkmcnt(2)
PM 88,88,24
PM 89,89,24
M 164,4,72
RV 52,39,4720
RV 56,39,4928
PM 90,90,24
PM 91,91,24
PH 88,60,24
M 168,8,72
RA 24,19,43008
RA 28,19,44032
PH 89,61,24
PH 90,62,24
PH 91,63,24
M 172,12,72
RA 32,19,45056
PL 88,64,25
PL 89,65,25
M 176,16,72
buffer_load_dwordx4 v45, s[36:39], s40 offen sc1 lds
PL 90,66,25
PL 91,67,25
PH 88,68,25
M 180,20,72
PH 89,69,25
PH 90,70,25
PH 91,71,25
M 204,20,76
RV 96,43,112
RV 60,43,320
s_waitcnt lgkmcnt(5)
PM 92,92,26
PM 93,93,26
M 200,16,76
RV 64,43,4720
RV 68,43,4928
PM 94,94,26
PM 95,95,26
PH 92,48,26
M 196,12,76
RA 36,19,46080
RA 40,19,47104
PH 93,49,26
PH 94,50,26
PH 95,51,26
M 192,8,76
buffer_load_dwordx4 v45, s[36:39], s40 offen offset:1024 sc1 lds
RA 44,19,48128
PL 92,52,27
PL 93,53,27
M 188,4,76
s_addk_i32 s40, 24704
PL 94,54,27
PL 95,55,27
PH 92,56,27
M 184,0,76
PH 93,57,27
PH 94,58,27
PH 95,59,27
M 208,0,80
RV 72,32,128
RV 48,32,336
s_waitcnt lgkmcnt(5)
PM 96,96,28
PM 97,97,28
M 212,4,80
RV 52,32,4736
RV 56,32,4944
PM 98,98,28
M 216,8,80
PM 99,99,28
PH 96,60,28
PH 97,61,28
M 220,12,80
s_waitcnt vmcnt(6) lgkmcnt(4)
PH 98,62,28
PH 99,63,28
PL 96,64,29
M 224,16,80
s_barrier
PL 97,65,29
PL 98,66,29
PL 99,67,29
M 228,20,80
PH 96,68,29
PH 97,69,29
PH 98,70,29
PH 99,71,29
M 160,24,88
RV 76,36,128
RV 60,36,336
s_waitcnt lgkmcnt(2)
PM 72,72,24
PM 73,73,24
M 164,28,88
RV 64,36,4736
RV 68,36,4944
PM 74,74,24
PM 75,75,24
PH 72,48,24
M 168,32,88
RA 0,20,0
RA 4,20,1024
PH 73,49,24
PH 74,50,24
)ASM" R"ASM(
M 172,36,88
s_branch L_in13_2_%=
L_ag13_2_%=:
s_call_b64 s[72:73], L_retry_%=
s_branch L_ok13_2_%=
.p2align 3
L_in13_2_%=:
PH 75,51,24
PL 72,52,25
PL 73,53,25
PL 74,54,25
PL 75,55,25
M 176,40,88
PH 72,56,25
PH 73,57,25
PH 74,58,25
PH 75,59,25
M 180,44,88
RV 80,40,128
RV 48,40,336
s_waitcnt lgkmcnt(4)
PM 76,76,26
PM 77,77,26
M 204,44,92
RV 52,40,4736
RV 56,40,4944
PM 78,78,26
PM 79,79,26
PH 76,60,26
M 200,40,92
RA 8,20,2048
RA 12,20,3072
PH 77,61,26
PH 78,62,26
PH 79,63,26
M 196,36,92
s_add_u32 m0, s41, 24576
PL 76,64,27
PL 77,65,27
PL 78,66,27
PL 79,67,27
PH 76,68,27
M 192,32,92
buffer_load_dwordx4 v15, s[36:39], s40 offen sc1 lds
PH 77,69,27
PH 78,70,27
PH 79,71,27
M 188,28,92
RV 84,44,128
RV 60,44,336
s_waitcnt lgkmcnt(4)
PM 80,80,28
PM 81,81,28
M 184,24,92
RV 64,44,4736
RV 68,44,4944
PM 82,82,28
PM 83,83,28
PH 80,48,28
M 208,24,96
RA 16,20,4096
RA 20,20,5120
PH 81,49,28
PH 82,50,28
PH 83,51,28
PL 80,52,29
M 212,28,96
PL 81,53,29
PL 82,54,29
PL 83,55,29
PH 80,56,29
PH 81,57,29
PH 82,58,29
PH 83,59,29
M 216,32,96
RV 88,33,144
RV 48,33,352
s_waitcnt lgkmcnt(4)
PM 84,84,30
PM 85,85,30
M 220,36,96
RV 52,33,4752
RV 56,33,4960
PM 86,86,30
PM 87,87,30
PH 84,60,30
M 224,40,96
PH 85,61,30
PH 86,62,30
PH 87,63,30
PL 84,64,31
PL 85,65,31
M 228,44,96
PL 86,66,31
PL 87,67,31
PH 84,68,31
PH 85,69,31
PH 86,70,31
PH 87,71,31
M 160,0,72
RV 92,37,144
RV 60,37,352
M 164,4,72
s_waitcnt lgkmcnt(2)
PM 88,88,24
PM 89,89,24
M 168,8,72
RV 64,37,4752
RV 68,37,4960
M 172,12,72
PM 90,90,24
PM 91,91,24
PH 88,48,24
M 176,16,72
RA 24,21,6144
RA 28,21,7168
M 180,20,72
PH 89,49,24
PH 90,50,24
PH 91,51,24
M 204,20,76
RA 32,21,8192
PL 88,52,25
PL 89,53,25
M 200,16,76
PL 90,54,25
PL 91,55,25
PH 88,56,25
M 196,12,76
buffer_load_dwordx4 v15, s[36:39], s40 offen offset:1024 sc1 lds
M 192,8,76
PH 89,57,25
PH 90,58,25
PH 91,59,25
M 188,4,76
RV 96,41,144
RV 48,41,352
M 184,0,76
s_waitcnt lgkmcnt(2)
PM 92,92,26
PM 93,93,26
M 208,0,80
RV 52,41,4752
RV 56,41,4960
M 212,4,80
PM 94,94,26
PM 95,95,26
PH 92,60,26
M 216,8,80
RA 36,21,9216
RA 40,21,10240
M 220,12,80
PH 93,61,26
PH 94,62,26
PH 95,63,26
M 224,16,80
RA 44,21,11264
PL 92,64,27
PL 93,65,27
M 228,20,80
PL 94,66,27
PL 95,67,27
PH 92,68,27
M 252,20,84
buffer_load_dwordx4 v15, s[36:39], s40 offen offset:2048 sc1 lds
PH 93,69,27
PH 94,70,27
PH 95,71,27
M 248,16,84
RV 72,34,160
RV 60,34,368
s_waitcnt lgkmcnt(3)
PM 96,96,28
PM 97,97,28
M 244,12,84
RV 64,34,4768
RV 68,34,4976
PM 98,98,28
PM 99,99,28
M 240,8,84
PH 96,48,28
PH 97,49,28
PH 98,50,28
PH 99,51,28
M 236,4,84
PL 96,52,29
PL 97,53,29
PL 98,54,29
PL 99,55,29
M 232,0,84
PH 96,56,29
PH 97,57,29
PH 98,58,29
PH 99,59,29
M 160,24,88
RV 76,38,160
RV 48,38,368
s_waitcnt lgkmcnt(2)
PM 72,72,24
PM 73,73,24
M 164,28,88
RV 52,38,4768
RV 56,38,4976
PM 74,74,24
PM 75,75,24
PH 72,60,24
M 168,32,88
RA 0,22,12288
RA 4,22,13312
PH 73,61,24
PH 74,62,24
PH 75,63,24
M 172,36,88
RA 8,22,14336
PL 72,64,25
PL 73,65,25
M 176,40,88
PL 74,66,25
PL 75,67,25
PH 72,68,25
M 180,44,88
buffer_load_dwordx4 v15, s[36:39], s40 offen offset:3072 sc1 lds
PH 73,69,25
PH 74,70,25
PH 75,71,25
M 204,44,92
RV 80,42,160
RV 60,42,368
s_waitcnt lgkmcnt(5)
PM 76,76,26
PM 77,77,26
M 200,40,92
RV 64,42,4768
RV 68,42,4976
PM 78,78,26
PM 79,79,26
PH 76,48,26
M 196,36,92
RA 12,22,15360
RA 16,22,16384
PH 77,49,26
PH 78,50,26
PH 79,51,26
M 192,32,92
RA 20,22,17408
PL 76,52,27
PL 77,53,27
M 188,28,92
PL 78,54,27
PL 79,55,27
PH 76,56,27
M 184,24,92
PH 77,57,27
PH 78,58,27
PH 79,59,27
M 208,24,96
RV 88,35,176
RV 48,35,384
s_waitcnt lgkmcnt(5)
PM 80,80,28
PM 81,81,28
M 212,28,96
RV 52,35,4784
RV 56,35,4992
PM 82,82,28
M 216,32,96
PM 83,83,28
PH 80,60,28
PH 81,61,28
M 220,36,96
PH 82,62,28
PH 83,63,28
PL 80,64,29
M 224,40,96
s_addk_i32 m0, 4096
PL 81,65,29
PL 82,66,29
PL 83,67,29
PH 80,68,29
M 228,44,96
buffer_load_dwordx4 v45, s[36:39], s40 offen sc1 lds
PH 81,69,29
PH 82,70,29
PH 83,71,29
M 160,0,72
RV 92,39,176
RV 60,39,384
s_waitcnt lgkmcnt(2)
PM 88,88,24
PM 89,89,24
M 164,4,72
RV 64,39,4784
RV 68,39,4992
PM 90,90,24
PM 91,91,24
PH 88,48,24
M 168,8,72
RA 24,23,18432
RA 28,23,19456
PH 89,49,24
PH 90,50,24
PH 91,51,24
M 172,12,72
RA 32,23,20480
PL 88,52,25
PL 89,53,25
M 176,16,72
PL 90,54,25
PL 91,55,25
PH 88,56,25
M 180,20,72
buffer_load_dwordx4 v45, s[36:39], s40 offen offset:1024 sc1 lds
PH 89,57,25
PH 90,58,25
PH 91,59,25
M 204,20,76
RV 96,43,176
RV 48,43,384
s_waitcnt lgkmcnt(5)
PM 92,92,26
PM 93,93,26
M 200,16,76
RV 52,43,4784
RV 56,43,4992
PM 94,94,26
PM 95,95,26
PH 92,60,26
M 196,12,76
RA 36,23,21504
RA 40,23,22528
PH 93,61,26
PH 94,62,26
PH 95,63,26
M 192,8,76
RA 44,23,23552
PL 92,64,27
PL 93,65,27
PL 94,66,27
M 188,4,76
s_add_u32 s40, s40, 24704
PL 95,67,27
PH 92,68,27
PH 93,69,27
M 184,0,76
PH 94,70,27
PH 95,71,27
s_waitcnt lgkmcnt(3)
PM 96,96,28
M 208,0,80
PM 97,97,28
PM 98,98,28
PM 99,99,28
PH 96,48,28
M 212,4,80
PH 97,49,28
PH 98,50,28
PH 99,51,28
PL 96,52,29
M 216,8,80
PL 97,53,29
PL 98,54,29
PL 99,55,29
PH 96,56,29
M 220,12,80
s_waitcnt vmcnt(6) lgkmcnt(0)
PH 97,57,29
s_lshr_b32 s42, s42, 4
s_add_u32 s43, s43, 8
M 224,16,80
s_barrier
s_and_b32 s55, s42, 15
s_cmp_eq_u32 s55, 3
PH 98,58,29
PH 99,59,29
M 228,20,80
s_cbranch_scc0 L_disp13_2_%=
L_body13_2_%=:
M 160,24,88
L_bodz13_2_%=:
RA 0,16,0
RA 4,16,1024
s_set_gpr_idx_on s43, gpr_idx(SRC1)
v_add_u32 v32, s44, v104
v_add_u32 v36, s44, v105
v_add_u32 v40, s44, v106
v_add_u32 v44, s44, v107
v_add_u32 v6, 0, v108
v_add_u32 v7, 0, v109
v_add_u32 v8, 0, v110
v_add_u32 v9, 0, v111
s_set_gpr_idx_off
M 164,28,88
RA 8,16,2048
v_pk_ashrrev_i16 v25, 15, v6 op_sel_hi:[0,1]
v_mad_i32_i16 v32, v25, s53, v32
v_mad_i32_i16 v32, v25, s54, v32 op_sel:[1,0,0,0]
M 168,32,88
RV 72,32,0
RV 48,32,208
RV 52,32,4608
RV 56,32,4816
v_pk_ashrrev_i16 v27, 15, v7 op_sel_hi:[0,1]
v_mad_i32_i16 v36, v27, s53, v36
v_mad_i32_i16 v36, v27, s54, v36 op_sel:[1,0,0,0]
v_pk_ashrrev_i16 v29, 15, v8 op_sel_hi:[0,1]
v_mad_i32_i16 v40, v29, s53, v40
M 172,36,88
v_mad_i32_i16 v40, v29, s54, v40 op_sel:[1,0,0,0]
v_pk_ashrrev_i16 v31, 15, v9 op_sel_hi:[0,1]
v_mad_i32_i16 v44, v31, s53, v44
v_mad_i32_i16 v44, v31, s54, v44 op_sel:[1,0,0,0]
v_and_b32 v25, 0x3c003c00, v25
v_pk_add_f16 v6, v6, v25
v_pk_add_f16 v25, v6, s51 op_sel:[1,0] neg_lo:[1,0]
v_pk_add_f16 v6, v6, s51 op_sel_hi:[0,1] neg_lo:[1,0]
PM 24,25,6
M 176,40,88
v_pk_mul_f16 v25, v25, v6 op_sel:[0,1]
v_and_b32 v27, 0x3c003c00, v27
v_pk_add_f16 v7, v7, v27
v_pk_add_f16 v27, v7, s51 op_sel:[1,0] neg_lo:[1,0]
v_pk_add_f16 v7, v7, s51 op_sel_hi:[0,1] neg_lo:[1,0]
PM 26,27,7
v_pk_mul_f16 v27, v27, v7 op_sel:[0,1]
v_and_b32 v29, 0x3c003c00, v29
v_pk_add_f16 v8, v8, v29
M 180,44,88
v_pk_add_f16 v29, v8, s51 op_sel:[1,0] neg_lo:[1,0]
v_pk_add_f16 v8, v8, s51 op_sel_hi:[0,1] neg_lo:[1,0]
PM 28,29,8
v_pk_mul_f16 v29, v29, v8 op_sel:[0,1]
v_and_b32 v31, 0x3c003c00, v31
v_pk_add_f16 v9, v9, v31
v_pk_add_f16 v31, v9, s51 op_sel:[1,0] neg_lo:[1,0]
v_pk_add_f16 v9, v9, s51 op_sel_hi:[0,1] neg_lo:[1,0]
PM 30,31,9
v_pk_mul_f16 v31, v31, v9 op_sel:[0,1]
M 204,44,92
RV 76,36,0
RV 60,36,208
s_waitcnt lgkmcnt(2)
PM 72,72,24
PM 73,73,24
M 200,40,92
RV 64,36,4608
RV 68,36,4816
PM 74,74,24
PM 75,75,24
PH 72,48,24
M 196,36,92
RA 12,16,3072
PH 73,49,24
PH 74,50,24
PH 75,51,24
PL 72,52,25
PL 73,53,25
PL 74,54,25
PL 75,55,25
M 192,32,92
PH 72,56,25
PH 73,57,25
PH 74,58,25
PH 75,59,25
v_add_u32 v33, s45, v32
v_add_u32 v37, s45, v36
v_add_u32 v41, s45, v40
M 188,28,92
RV 80,40,0
RV 48,40,208
s_waitcnt lgkmcnt(3)
PM 76,76,26
PM 77,77,26
RV 52,40,4608
RV 56,40,4816
PM 78,78,26
PM 79,79,26
PH 76,60,26
M 184,24,92
s_add_u32 m0, s41, 49152
RA 16,16,4096
PH 77,61,26
PH 78,62,26
PH 79,63,26
PL 76,64,27
PL 77,65,27
PL 78,66,27
PL 79,67,27
PH 76,68,27
M 208,24,96
buffer_load_dwordx4 v15, s[36:39], s40 offen sc1 lds
PH 77,69,27
PH 78,70,27
PH 79,71,27
M 212,28,96
RV 84,44,0
RV 60,44,208
s_waitcnt lgkmcnt(3)
PM 80,80,28
PM 81,81,28
RV 64,44,4608
RV 68,44,4816
PM 82,82,28
PM 83,83,28
PH 80,48,28
M 216,32,96
RA 20,16,5120
PH 81,49,28
PH 82,50,28
PH 83,51,28
PL 80,52,29
PL 81,53,29
PL 82,54,29
PL 83,55,29
PH 80,56,29
PH 81,57,29
PH 82,58,29
PH 83,59,29
M 220,36,96
RV 88,33,16
RV 48,33,224
s_waitcnt lgkmcnt(3)
PM 84,84,30
PM 85,85,30
M 224,40,96
RV 52,33,4624
RV 56,33,4832
PM 86,86,30
PM 87,87,30
PH 84,60,30
PH 85,61,30
PH 86,62,30
PH 87,63,30
M 228,44,96
PL 84,64,31
PL 85,65,31
PL 86,66,31
PL 87,67,31
PH 84,68,31
PH 85,69,31
PH 86,70,31
PH 87,71,31
M 160,0,72
RV 92,37,16
RV 60,37,224
M 164,4,72
s_waitcnt lgkmcnt(2)
PM 88,88,24
PM 89,89,24
M 168,8,72
RV 64,37,4624
RV 68,37,4832
M 172,12,72
PM 90,90,24
PM 91,91,24
PH 88,48,24
M 176,16,72
RA 24,17,6144
RA 28,17,7168
M 180,20,72
PH 89,49,24
PH 90,50,24
PH 91,51,24
M 204,20,76
RA 32,17,8192
PL 88,52,25
PL 89,53,25
M 200,16,76
PL 90,54,25
PL 91,55,25
PH 88,56,25
M 196,12,76
PH 89,57,25
PH 90,58,25
PH 91,59,25
M 192,8,76
buffer_load_dwordx4 v15, s[36:39], s40 offen offset:1024 sc1 lds
M 188,4,76
v_add_u32 v34, s46, v32
v_add_u32 v38, s46, v36
v_add_u32 v42, s46, v40
M 184,0,76
RV 96,41,16
RV 48,41,224
M 208,0,80
s_waitcnt lgkmcnt(2)
PM 92,92,26
PM 93,93,26
M 212,4,80
RV 52,41,4624
RV 56,41,4832
M 216,8,80
PM 94,94,26
PM 95,95,26
PH 92,60,26
M 220,12,80
RA 36,17,9216
RA 40,17,10240
PH 93,61,26
PH 94,62,26
PH 95,63,26
M 224,16,80
RA 44,17,11264
PL 92,64,27
PL 93,65,27
M 228,20,80
PL 94,66,27
PL 95,67,27
PH 92,68,27
M 252,20,84
buffer_load_dwordx4 v15, s[36:39], s40 offen offset:2048 sc1 lds
PH 93,69,27
PH 94,70,27
PH 95,71,27
M 248,16,84
RV 72,34,32
RV 60,34,240
s_waitcnt lgkmcnt(5)
PM 96,96,28
PM 97,97,28
M 244,12,84
RV 64,34,4640
RV 68,34,4848
PM 98,98,28
PM 99,99,28
M 240,8,84
PH 96,48,28
PH 97,49,28
PH 98,50,28
PH 99,51,28
M 236,4,84
PL 96,52,29
PL 97,53,29
PL 98,54,29
PL 99,55,29
M 232,0,84
PH 96,56,29
PH 97,57,29
PH 98,58,29
PH 99,59,29
M 160,24,88
RV 76,38,32
RV 48,38,240
s_waitcnt lgkmcnt(2)
PM 72,72,24
PM 73,73,24
M 164,28,88
RV 52,38,4640
RV 56,38,4848
PM 74,74,24
PM 75,75,24
PH 72,60,24
M 168,32,88
RA 0,18,12288
RA 4,18,13312
PH 73,61,24
PH 74,62,24
PH 75,63,24
M 172,36,88
RA 8,18,14336
PL 72,64,25
PL 73,65,25
M 176,40,88
PL 74,66,25
PL 75,67,25
PH 72,68,25
M 180,44,88
PH 73,69,25
PH 74,70,25
PH 75,71,25
M 204,44,92
buffer_load_dwordx4 v15, s[36:39], s40 offen offset:3072 sc1 lds
v_add_u32 v35, s47, v32
v_add_u32 v39, s47, v36
v_add_u32 v43, s47, v40
M 200,40,92
RV 80,42,32
RV 60,42,240
s_waitcnt lgkmcnt(3)
PM 76,76,26
PM 77,77,26
M 196,36,92
RV 64,42,4640
RV 68,42,4848
PM 78,78,26
PM 79,79,26
PH 76,48,26
M 192,32,92
RA 12,18,15360
RA 16,18,16384
PH 77,49,26
PH 78,50,26
PH 79,51,26
M 188,28,92
RA 20,18,17408
PL 76,52,27
PL 77,53,27
M 184,24,92
PL 78,54,27
PL 79,55,27
PH 76,56,27
M 208,24,96
PH 77,57,27
PH 78,58,27
PH 79,59,27
M 212,28,96
RV 88,35,48
RV 48,35,256
s_waitcnt lgkmcnt(5)
PM 80,80,28
PM 81,81,28
M 216,32,96
RV 52,35,4656
RV 56,35,4864
PM 82,82,28
PM 83,83,28
M 220,36,96
PH 80,60,28
PH 81,61,28
PH 82,62,28
PH 83,63,28
M 224,40,96
s_addk_i32 m0, 4096
PL 80,64,29
PL 81,65,29
PL 82,66,29
PL 83,67,29
PH 80,68,29
M 228,44,96
buffer_load_dwordx4 v45, s[36:39], s40 offen sc1 lds
PH 81,69,29
PH 82,70,29
PH 83,71,29
M 160,0,72
RV 92,39,48
RV 60,39,256
s_waitcnt lgkmcnt(2)
PM 88,88,24
PM 89,89,24
M 164,4,72
RV 64,39,4656
RV 68,39,4864
PM 90,90,24
PM 91,91,24
PH 88,48,24
M 168,8,72
RA 24,19,18432
RA 28,19,19456
PH 89,49,24
PH 90,50,24
PH 91,51,24
M 172,12,72
RA 32,19,20480
PL 88,52,25
PL 89,53,25
M 176,16,72
PL 90,54,25
PL 91,55,25
PH 88,56,25
M 180,20,72
buffer_load_dwordx4 v45, s[36:39], s40 offen offset:1024 sc1 lds
PH 89,57,25
PH 90,58,25
PH 91,59,25
M 204,20,76
RV 96,43,48
RV 48,43,256
s_waitcnt lgkmcnt(5)
PM 92,92,26
PM 93,93,26
M 200,16,76
RV 52,43,4656
RV 56,43,4864
PM 94,94,26
PM 95,95,26
PH 92,60,26
M 196,12,76
RA 36,19,21504
RA 40,19,22528
PH 93,61,26
PH 94,62,26
PH 95,63,26
M 192,8,76
RA 44,19,23552
PL 92,64,27
PL 93,65,27
M 188,4,76
PL 94,66,27
PL 95,67,27
PH 92,68,27
M 184,0,76
PH 93,69,27
PH 94,70,27
PH 95,71,27
M 208,0,80
RV 72,32,64
RV 60,32,272
s_waitcnt lgkmcnt(5)
PM 96,96,28
PM 97,97,28
M 212,4,80
RV 64,32,4672
RV 68,32,4880
PM 98,98,28
M 216,8,80
s_add_u32 s40, s40, 24704
PM 99,99,28
PH 96,48,28
PH 97,49,28
M 220,12,80
s_waitcnt vmcnt(6) lgkmcnt(4)
PH 98,50,28
PH 99,51,28
PL 96,52,29
M 224,16,80
s_barrier
PL 97,53,29
PL 98,54,29
PL 99,55,29
M 228,20,80
PH 96,56,29
PH 97,57,29
PH 98,58,29
PH 99,59,29
M 160,24,88
RV 76,36,64
RV 48,36,272
s_waitcnt lgkmcnt(2)
PM 72,72,24
PM 73,73,24
M 164,28,88
RV 52,36,4672
RV 56,36,4880
PM 74,74,24
PM 75,75,24
PH 72,60,24
M 168,32,88
RA 0,16,24576
RA 4,16,25600
PH 73,61,24
PH 74,62,24
M 172,36,88
PH 75,63,24
PL 72,64,25
PL 73,65,25
PL 74,66,25
PL 75,67,25
M 176,40,88
PH 72,68,25
PH 73,69,25
PH 74,70,25
PH 75,71,25
M 180,44,88
RV 80,40,64
RV 60,40,272
s_waitcnt lgkmcnt(4)
PM 76,76,26
PM 77,77,26
M 204,44,92
RV 64,40,4672
RV 68,40,4880
PM 78,78,26
PM 79,79,26
PH 76,48,26
M 200,40,92
RA 8,16,26624
RA 12,16,27648
PH 77,49,26
PH 78,50,26
PH 79,51,26
M 196,36,92
s_mov_b32 m0, s41
PL 76,52,27
PL 77,53,27
PL 78,54,27
PL 79,55,27
PH 76,56,27
M 192,32,92
buffer_load_dwordx4 v15, s[36:39], s40 offen sc1 lds
PH 77,57,27
PH 78,58,27
PH 79,59,27
M 188,28,92
RV 84,44,64
RV 48,44,272
s_waitcnt lgkmcnt(4)
PM 80,80,28
PM 81,81,28
M 184,24,92
RV 52,44,4672
RV 56,44,4880
PM 82,82,28
PM 83,83,28
PH 80,60,28
M 208,24,96
RA 16,16,28672
RA 20,16,29696
PH 81,61,28
PH 82,62,28
PH 83,63,28
PL 80,64,29
M 212,28,96
PL 81,65,29
PL 82,66,29
PL 83,67,29
PH 80,68,29
PH 81,69,29
PH 82,70,29
PH 83,71,29
M 216,32,96
RV 88,33,80
RV 60,33,288
s_waitcnt lgkmcnt(4)
PM 84,84,30
PM 85,85,30
M 220,36,96
RV 64,33,4688
RV 68,33,4896
PM 86,86,30
PM 87,87,30
PH 84,48,30
M 224,40,96
PH 85,49,30
PH 86,50,30
PH 87,51,30
PL 84,52,31
PL 85,53,31
M 228,44,96
PL 86,54,31
PL 87,55,31
PH 84,56,31
PH 85,57,31
PH 86,58,31
PH 87,59,31
M 160,0,72
RV 92,37,80
RV 48,37,288
M 164,4,72
s_waitcnt lgkmcnt(2)
PM 88,88,24
PM 89,89,24
M 168,8,72
RV 52,37,4688
RV 56,37,4896
M 172,12,72
PM 90,90,24
PM 91,91,24
PH 88,60,24
M 176,16,72
RA 24,17,30720
RA 28,17,31744
M 180,20,72
PH 89,61,24
PH 90,62,24
PH 91,63,24
M 204,20,76
RA 32,17,32768
PL 88,64,25
PL 89,65,25
M 200,16,76
PL 90,66,25
PL 91,67,25
PH 88,68,25
M 196,12,76
buffer_load_dwordx4 v15, s[36:39], s40 offen offset:1024 sc1 lds
M 192,8,76
PH 89,69,25
PH 90,70,25
PH 91,71,25
M 188,4,76
RV 96,41,80
RV 60,41,288
M 184,0,76
s_waitcnt lgkmcnt(2)
PM 92,92,26
PM 93,93,26
M 208,0,80
RV 64,41,4688
RV 68,41,4896
M 212,4,80
PM 94,94,26
PM 95,95,26
PH 92,48,26
M 216,8,80
RA 36,17,33792
RA 40,17,34816
M 220,12,80
PH 93,49,26
PH 94,50,26
PH 95,51,26
M 224,16,80
RA 44,17,35840
PL 92,52,27
PL 93,53,27
M 228,20,80
PL 94,54,27
PL 95,55,27
PH 92,56,27
M 252,20,84
buffer_load_dwordx4 v15, s[36:39], s40 offen offset:2048 sc1 lds
PH 93,57,27
PH 94,58,27
PH 95,59,27
M 248,16,84
RV 72,34,96
RV 48,34,304
s_waitcnt lgkmcnt(3)
PM 96,96,28
PM 97,97,28
M 244,12,84
RV 52,34,4704
RV 56,34,4912
PM 98,98,28
PM 99,99,28
M 240,8,84
PH 96,60,28
PH 97,61,28
PH 98,62,28
PH 99,63,28
M 236,4,84
PL 96,64,29
PL 97,65,29
PL 98,66,29
PL 99,67,29
M 232,0,84
PH 96,68,29
PH 97,69,29
PH 98,70,29
PH 99,71,29
M 160,24,88
RV 76,38,96
RV 60,38,304
s_waitcnt lgkmcnt(2)
PM 72,72,24
PM 73,73,24
M 164,28,88
RV 64,38,4704
RV 68,38,4912
PM 74,74,24
PM 75,75,24
PH 72,48,24
M 168,32,88
RA 0,18,36864
RA 4,18,37888
PH 73,49,24
PH 74,50,24
PH 75,51,24
M 172,36,88
RA 8,18,38912
PL 72,52,25
PL 73,53,25
M 176,40,88
PL 74,54,25
PL 75,55,25
PH 72,56,25
M 180,44,88
buffer_load_dwordx4 v15, s[36:39], s40 offen offset:3072 sc1 lds
PH 73,57,25
PH 74,58,25
PH 75,59,25
M 204,44,92
RV 80,42,96
RV 48,42,304
s_waitcnt lgkmcnt(5)
PM 76,76,26
PM 77,77,26
M 200,40,92
RV 52,42,4704
RV 56,42,4912
PM 78,78,26
PM 79,79,26
PH 76,60,26
M 196,36,92
RA 12,18,39936
RA 16,18,40960
PH 77,61,26
PH 78,62,26
PH 79,63,26
M 192,32,92
RA 20,18,41984
PL 76,64,27
PL 77,65,27
M 188,28,92
PL 78,66,27
PL 79,67,27
PH 76,68,27
M 184,24,92
PH 77,69,27
PH 78,70,27
PH 79,71,27
M 208,24,96
RV 88,35,112
RV 60,35,320
s_waitcnt lgkmcnt(5)
PM 80,80,28
PM 81,81,28
M 212,28,96
RV 64,35,4720
RV 68,35,4928
PM 82,82,28
M 216,32,96
PM 83,83,28
PH 80,48,28
PH 81,49,28
M 220,36,96
PH 82,50,28
PH 83,51,28
PL 80,52,29
M 224,40,96
s_add_u32 m0, m0, 4096
PL 81,53,29
PL 82,54,29
PL 83,55,29
PH 80,56,29
M 228,44,96
buffer_load_dwordx4 v45, s[36:39], s40 offen sc1 lds
PH 81,57,29
PH 82,58,29
PH 83,59,29
M 160,0,72
RV 92,39,112
RV 48,39,320
s_waitcnt lgkmcnt(2)
PM 88,88,24
PM 89,89,24
M 164,4,72
RV 52,39,4720
RV 56,39,4928
PM 90,90,24
PM 91,91,24
PH 88,60,24
M 168,8,72
RA 24,19,43008
RA 28,19,44032
PH 89,61,24
PH 90,62,24
PH 91,63,24
M 172,12,72
RA 32,19,45056
PL 88,64,25
PL 89,65,25
M 176,16,72
PL 90,66,25
PL 91,67,25
PH 88,68,25
M 180,20,72
buffer_load_dwordx4 v45, s[36:39], s40 offen offset:1024 sc1 lds
PH 89,69,25
PH 90,70,25
PH 91,71,25
M 204,20,76
RV 96,43,112
RV 60,43,320
s_waitcnt lgkmcnt(5)
PM 92,92,26
PM 93,93,26
M 200,16,76
RV 64,43,4720
RV 68,43,4928
PM 94,94,26
PM 95,95,26
PH 92,48,26
M 196,12,76
RA 36,19,46080
RA 40,19,47104
PH 93,49,26
PH 94,50,26
PH 95,51,26
M 192,8,76
RA 44,19,48128
PL 92,52,27
PL 93,53,27
M 188,4,76
PL 94,54,27
PL 95,55,27
PH 92,56,27
M 184,0,76
PH 93,57,27
PH 94,58,27
PH 95,59,27
M 208,0,80
RV 72,32,128
RV 48,32,336
s_waitcnt lgkmcnt(5)
PM 96,96,28
PM 97,97,28
M 212,4,80
RV 52,32,4736
RV 56,32,4944
PM 98,98,28
M 216,8,80
s_add_u32 s40, s40, 24704
PM 99,99,28
PH 96,60,28
PH 97,61,28
M 220,12,80
s_waitcnt vmcnt(6) lgkmcnt(4)
PH 98,62,28
PH 99,63,28
PL 96,64,29
M 224,16,80
s_barrier
PL 97,65,29
PL 98,66,29
PL 99,67,29
M 228,20,80
PH 96,68,29
PH 97,69,29
PH 98,70,29
PH 99,71,29
M 160,24,88
RV 76,36,128
RV 60,36,336
s_waitcnt lgkmcnt(2)
PM 72,72,24
PM 73,73,24
M 164,28,88
RV 64,36,4736
RV 68,36,4944
PM 74,74,24
PM 75,75,24
PH 72,48,24
M 168,32,88
RA 0,20,0
RA 4,20,1024
PH 73,49,24
PH 74,50,24
M 172,36,88
s_branch L_in13_2_%=
.p2align 3
L_sel13_2_%=:
s_cmp_eq_u32 s55, 0
s_cbranch_scc1 L_bodz13_0_%=
s_cmp_eq_u32 s55, 1
s_cbranch_scc1 L_bodz13_1_%=
s_cmp_eq_u32 s55, 4
s_cbranch_scc1 L_tail13_0_%=
s_cmp_eq_u32 s55, 5
s_cbranch_scc1 L_tail13_1_%=
s_cmp_lt_u32 s55, 4
s_cbranch_scc1 L_bodz13_2_%=
s_branch L_tail13_2_%=
L_disp13_2_%=:
M 160,24,88
s_cmp_lg_u32 s55, 7
s_cbranch_scc1 L_sel13_2_%=
L_tail13_2_%=:
s_mul_i32 s63, s25, 6144
v_add_u32 v16, s63, v14
s_mul_i32 s64, s61, 384
v_add_u32_e64 v17, s64, v16
s_mul_i32 s65, s62, 384
M 164,28,88
RA 0,16,0
RA 4,16,1024
v_add_u32 v18, s65, v16
v_add_u32 v44, s60, v155
v_mov_b32_e64 v9, v159
v_pk_ashrrev_i16 v31, 15, v9 op_sel_hi:[0,1]
M 168,32,88
RA 8,16,2048
RA 12,16,3072
v_mad_i32_i16 v44, v31, s53, v44
v_mad_i32_i16 v44, v31, s54, v44 op_sel:[1,0,0,0]
v_add_u32 v32, s60, v152
v_mov_b32 v6, v156
M 172,36,88
RA 16,16,4096
RA 20,16,5120
v_pk_ashrrev_i16 v25, 15, v6 op_sel_hi:[0,1]
v_mad_i32_i16 v32, v25, s53, v32
v_mad_i32_i16 v32, v25, s54, v32 op_sel:[1,0,0,0]
v_add_u32_e64 v36, s60, v153
M 176,40,88
RV 104,44,0
RV 48,44,208
v_mov_b32_e64 v7, v157
v_pk_ashrrev_i16 v27, 15, v7 op_sel_hi:[0,1]
v_mad_i32_i16 v36, v27, s53, v36
v_mad_i32_i16 v36, v27, s54, v36 op_sel:[1,0,0,0]
M 180,44,88
RV 52,44,4608
RV 56,44,4816
v_add_u32 v40, s60, v154
v_mov_b32 v8, v158
v_pk_ashrrev_i16 v29, 15, v8 op_sel_hi:[0,1]
v_mad_i32_i16 v40, v29, s53, v40
M 204,44,92
RV 108,32,0
RV 60,32,208
v_mad_i32_i16 v40, v29, s54, v40 op_sel:[1,0,0,0]
v_add_u32 v33, s61, v32
v_add_u32 v37, s61, v36
M 200,40,92
RV 64,32,4608
RV 68,32,4816
v_add_u32 v41, s61, v40
v_add_u32 v34, s62, v32
v_add_u32_e64 v38, s62, v36
v_add_u32 v42, s62, v40
M 196,36,92
RV 112,33,16
RV 72,33,224
v_and_b32 v31, 0x3c003c00, v31
v_pk_add_f16 v9, v9, v31
v_pk_add_f16 v31, v9, s51 op_sel:[1,0] neg_lo:[1,0]
v_pk_add_f16 v9, v9, s51 op_sel_hi:[0,1] neg_lo:[1,0]
M 192,32,92
RV 76,33,4624
RV 80,33,4832
PM 30,31,9
v_pk_mul_f16 v31, v31, v9 op_sel:[0,1]
v_and_b32 v25, 0x3c003c00, v25
v_pk_add_f16 v6, v6, v25
M 188,28,92
RA 68,17,11264
RA 64,17,10240
v_pk_add_f16 v25, v6, s51 op_sel:[1,0] neg_lo:[1,0]
v_pk_add_f16 v6, v6, s51 op_sel_hi:[0,1] neg_lo:[1,0]
PM 24,25,6
v_pk_mul_f16 v25, v25, v6 op_sel:[0,1]
M 184,24,92
RA 60,17,9216
RA 56,17,8192
v_and_b32 v27, 0x3c003c00, v27
v_pk_add_f16 v7, v7, v27
v_pk_add_f16 v27, v7, s51 op_sel:[1,0] neg_lo:[1,0]
v_pk_add_f16 v7, v7, s51 op_sel_hi:[0,1] neg_lo:[1,0]
M 208,24,96
RA 52,17,7168
RA 48,17,6144
PM 26,27,7
v_pk_mul_f16 v27, v27, v7 op_sel:[0,1]
v_and_b32 v29, 0x3c003c00, v29
v_pk_add_f16 v8, v8, v29
M 212,28,96
v_pk_add_f16 v29, v8, s51 op_sel:[1,0] neg_lo:[1,0]
v_pk_add_f16 v8, v8, s51 op_sel_hi:[0,1] neg_lo:[1,0]
PM 28,29,8
v_pk_mul_f16 v29, v29, v8 op_sel:[0,1]
s_waitcnt lgkmcnt(4)
PM 104,104,30
M 216,32,96
PM 105,105,30
PM 106,106,30
PM 107,107,30
PH 104,48,30
PH 105,49,30
PH 106,50,30
M 220,36,96
PH 107,51,30
PL 104,52,31
PL 105,53,31
PL 106,54,31
PL 107,55,31
PH 104,56,31
PH 105,57,31
PH 106,58,31
PH 107,59,31
M 224,40,96
RV 116,34,32
RV 48,34,240
PM 108,108,24
M 228,44,96
RV 52,34,4640
RV 56,34,4848
PM 109,109,24
PM 110,110,24
PM 111,111,24
PH 108,60,24
M 232,0,104
RA 72,18,12288
RA 76,18,13312
PH 109,61,24
PH 110,62,24
PH 111,63,24
M 236,4,104
RA 80,18,14336
RA 84,18,15360
PL 108,64,25
PL 109,65,25
PL 110,66,25
M 240,8,104
RA 88,18,16384
RA 92,18,17408
PL 111,67,25
PH 108,68,25
M 244,12,104
PH 109,69,25
PH 110,70,25
PH 111,71,25
PM 112,112,24
PM 113,113,24
M 248,16,104
RV 120,36,0
RV 60,36,208
PM 114,114,24
PM 115,115,24
PH 112,72,24
M 252,20,104
RV 64,36,4608
RV 68,36,4816
PH 113,73,24
PH 114,74,24
PH 115,75,24
M 160,0,108
PL 112,76,25
PL 113,77,25
PL 114,78,25
PL 115,79,25
PH 112,80,25
s_mul_i32 s11, s23, 192
s_mul_i32 s16, s21, 43008
s_add_u32 s16, s16, s11
s_add_u32 s17, s16, 43008
s_add_u32 s18, s16, 86016
s_mul_i32 s19, s22, 43008
s_add_u32 s19, s19, s11
s_lshl_b32 s11, s10, 6
s_add_u32 s19, s19, s11
s_lshl_b32 s11, s25, 11
v_add_u32 v21, s11, v14
v_add_u32 v20, s41, v15
v_and_b32 v47, 0xfff, v46
v_add_u32 v12, 162304, v15
s_waitcnt vmcnt(0)
M 164,4,108
M 168,8,108
M 172,12,108
PH 113,81,25
PH 114,82,25
PH 115,83,25
M 176,16,108
ds_write_b128 v20, v[232:235] offset:24576
M 180,20,108
RV 124,37,16
RV 72,37,224
s_waitcnt lgkmcnt(3)
PM 116,116,24
PM 117,117,24
M 180,68,112
ds_write_b128 v20, v[236:239] offset:25600
RV 76,37,4624
PM 118,118,24
PM 119,119,24
M 176,64,112
RV 80,37,4832
RV 84,12,0
PH 116,48,24
PH 117,49,24
PH 118,50,24
M 172,60,112
ds_write_b128 v20, v[240:243] offset:26624
PH 119,51,24
PL 116,52,25
PL 117,53,25
M 168,56,112
PL 118,54,25
PL 119,55,25
PH 116,56,25
PH 117,57,25
PH 118,58,25
PH 119,59,25
M 164,52,112
ds_write_b128 v20, v[244:247] offset:27648
RV 128,38,32
M 160,48,112
RV 48,38,240
RV 52,38,4640
PM 120,120,26
PM 121,121,26
PM 122,122,26
M 160,72,116
ds_write_b128 v20, v[248:251] offset:28672
RV 56,38,4848
PM 123,123,26
PH 120,60,26
M 164,76,116
PH 121,61,26
PH 122,62,26
PH 123,63,26
PL 120,64,27
PL 121,65,27
M 168,80,116
ds_write_b128 v20, v[252:255] offset:29696
PL 122,66,27
PL 123,67,27
PH 120,68,27
M 172,84,116
PH 121,69,27
PH 122,70,27
PH 123,71,27
s_waitcnt lgkmcnt(1)
PM 124,124,26
PM 125,125,26
M 176,88,116
RV 132,40,0
RV 60,40,208
PM 126,126,26
PM 127,127,26
PH 124,72,26
M 180,92,116
RV 64,40,4608
RV 68,40,4816
PH 125,73,26
PH 126,74,26
M 184,0,120
PH 127,75,26
PL 124,76,27
PL 125,77,27
PL 126,78,27
PL 127,79,27
M 188,4,120
PH 124,80,27
PH 125,81,27
PH 126,82,27
PH 127,83,27
PM 128,128,26
M 192,8,120
RV 136,41,16
RV 72,41,224
PM 129,129,26
PM 130,130,26
PM 131,131,26
M 196,12,120
RV 76,41,4624
RV 80,41,4832
PH 128,48,26
PH 129,49,26
PH 130,50,26
M 200,16,120
PH 131,51,26
PL 128,52,27
PL 129,53,27
PL 130,54,27
PL 131,55,27
M 204,20,120
PH 128,56,27
PH 129,57,27
PH 130,58,27
PH 131,59,27
M 204,68,124
RV 140,42,32
RV 48,42,240
s_waitcnt lgkmcnt(4)
PM 132,132,28
PM 133,133,28
PM 134,134,28
M 200,64,124
RV 52,42,4640
RV 56,42,4848
PM 135,135,28
PH 132,60,28
PH 133,61,28
M 196,60,124
PH 134,62,28
PH 135,63,28
PL 132,64,29
PL 133,65,29
PL 134,66,29
M 192,56,124
PL 135,67,29
PH 132,68,29
PH 133,69,29
PH 134,70,29
PH 135,71,29
M 188,52,124
v_cvt_pk_bf16_f32 v88, v160, v161
v_cvt_pk_bf16_f32 v89, v162, v163
v_cvt_pk_bf16_f32 v90, v164, v165
v_cvt_pk_bf16_f32 v91, v166, v167
v_cvt_pk_bf16_f32 v92, v168, v169
M 184,48,124
buffer_store_dwordx4 v[88:91], v47, s[68:71], s16 offen offset:0 sc0 sc1 nt
v_cvt_pk_bf16_f32 v93, v170, v171
v_cvt_pk_bf16_f32 v94, v172, v173
v_cvt_pk_bf16_f32 v95, v174, v175
M 184,72,128
s_waitcnt lgkmcnt(0)
PM 136,136,28
PM 137,137,28
PM 138,138,28
PM 139,139,28
M 188,76,128
PH 136,72,28
PH 137,73,28
PH 138,74,28
PH 139,75,28
PL 136,76,29
M 192,80,128
buffer_store_dwordx4 v[92:95], v47, s[68:71], s16 offen offset:64 sc0 sc1 nt
PL 137,77,29
PL 138,78,29
PL 139,79,29
M 196,84,128
PH 136,80,29
PH 137,81,29
PH 138,82,29
PH 139,83,29
v_cvt_pk_bf16_f32 v96, v176, v177
M 200,88,128
v_cvt_pk_bf16_f32 v97, v178, v179
v_cvt_pk_bf16_f32 v98, v180, v181
v_cvt_pk_bf16_f32 v99, v182, v183
v_cvt_pk_bf16_f32 v88, v184, v185
v_cvt_pk_bf16_f32 v89, v186, v187
M 204,92,128
buffer_store_dwordx4 v[96:99], v47, s[68:71], s16 offen offset:128 sc0 sc1 nt
v_cvt_pk_bf16_f32 v90, v188, v189
v_cvt_pk_bf16_f32 v91, v190, v191
v_cvt_pk_bf16_f32 v92, v192, v193
M 208,0,132
ds_write_b128 v4, v[88:91] offset:0
PM 140,140,28
PM 141,141,28
PM 142,142,28
M 212,4,132
PM 143,143,28
PH 140,48,28
PH 141,49,28
PH 142,50,28
M 216,8,132
PH 143,51,28
PL 140,52,29
PL 141,53,29
PL 142,54,29
PL 143,55,29
M 220,12,132
PH 140,56,29
PH 141,57,29
PH 142,58,29
PH 143,59,29
v_cvt_pk_bf16_f32 v93, v194, v195
M 224,16,132
v_cvt_pk_bf16_f32 v94, v196, v197
v_cvt_pk_bf16_f32 v95, v198, v199
v_cvt_pk_bf16_f32 v96, v200, v201
v_cvt_pk_bf16_f32 v97, v202, v203
v_cvt_pk_bf16_f32 v98, v204, v205
M 228,20,132
ds_write_b128 v4, v[92:95] offset:64
v_cvt_pk_bf16_f32 v99, v206, v207
s_waitcnt lgkmcnt(2)
s_barrier
M 228,68,136
RV 232,21,24576
RV 236,21,25600
v_or3_b32 v84, v84, v85, v86
v_or3_b32 v84, v84, v87, v5
v_and_b32 v85, 0x40004001, v84
M 224,64,136
ds_write_b128 v4, v[96:99] offset:128
RV 144,1,0
v_and_b32 v86, 0x70007000, v84
v_and_b32_e64 v87, 2, v84
M 220,60,136
RV 148,2,0
RV 100,3,0
v_cmp_ne_u32 vcc, 0, v85
v_cmp_eq_u32 s[30:31], 0, v86
M 216,56,136
RV 240,21,30720
RV 244,21,31744
v_cmp_eq_u32 s[58:59], 0, v87
s_cmp_eq_u64 s[30:31], -1
s_cselect_b64 vcc, -1, vcc
M 212,52,136
RV 248,21,36864
RV 252,21,37888
s_cmp_eq_u64 s[58:59], -1
s_cselect_b64 vcc, -1, vcc
M 208,48,136
RV 104,21,43008
RV 108,21,44032
M 208,72,140
M 212,76,140
M 216,80,140
s_waitcnt lgkmcnt(5)
buffer_store_dwordx4 v[144:147], v15, s[68:71], s17 offen offset:0 sc0 sc1 nt
M 220,84,140
M 224,88,140
M 228,92,140
buffer_store_dwordx4 v[148:151], v15, s[68:71], s17 offen offset:1024 sc0 sc1 nt
v_cvt_pk_bf16_f32 v88, v208, v209
v_cvt_pk_bf16_f32 v89, v210, v211
v_cvt_pk_bf16_f32 v90, v212, v213
v_cvt_pk_bf16_f32 v91, v214, v215
v_cvt_pk_bf16_f32 v92, v216, v217
v_cvt_pk_bf16_f32 v93, v218, v219
ds_write_b128 v4, v[88:91] offset:0
buffer_store_dwordx4 v[100:103], v15, s[68:71], s17 offen offset:2048 sc0 sc1 nt
v_cvt_pk_bf16_f32 v94, v220, v221
v_cvt_pk_bf16_f32 v95, v222, v223
v_cvt_pk_bf16_f32 v96, v224, v225
v_cvt_pk_bf16_f32 v97, v226, v227
ds_write_b128 v4, v[92:95] offset:64
v_cvt_pk_bf16_f32 v98, v228, v229
v_cvt_pk_bf16_f32 v99, v230, v231
s_waitcnt lgkmcnt(1)
v_add_f32 v232, v232, v240
v_add_f32 v233, v233, v241
v_add_f32 v234, v234, v242
v_add_f32_e64 v235, v235, v243
ds_write_b128 v4, v[96:99] offset:128
RV 144,1,0
RV 148,2,0
RV 100,3,0
v_add_f32 v236, v236, v244
v_add_f32 v237, v237, v245
v_add_f32 v238, v238, v246
v_add_f32 v239, v239, v247
v_add_f32 v248, v248, v104
v_add_f32 v249, v249, v105
v_add_f32 v250, v250, v106
v_add_f32 v251, v251, v107
v_add_f32 v252, v252, v108
v_add_f32 v253, v253, v109
v_add_f32 v254, v254, v110
v_add_f32 v255, v255, v111
v_add_f32 v232, v232, v248
v_add_f32 v233, v233, v249
v_add_f32 v234, v234, v250
v_add_f32 v235, v235, v251
v_add_f32 v236, v236, v252
v_add_f32 v237, v237, v253
v_add_f32 v238, v238, v254
v_add_f32 v239, v239, v255
v_cvt_pk_bf16_f32 v240, v232, v233
v_cvt_pk_bf16_f32 v241, v234, v235
v_cvt_pk_bf16_f32 v242, v236, v237
v_cvt_pk_bf16_f32 v243, v238, v239
s_waitcnt lgkmcnt(0)
buffer_store_dwordx4 v[144:147], v15, s[68:71], s18 offen offset:0 sc0 sc1 nt
buffer_store_dwordx4 v[148:151], v15, s[68:71], s18 offen offset:1024 sc0 sc1 nt
buffer_store_dwordx4 v[100:103], v15, s[68:71], s18 offen offset:2048 sc0 sc1 nt
buffer_store_dwordx4 v[240:243], v46, s[68:71], s19 offen offset:0 sc0 sc1 nt
s_cbranch_vccnz L_done_%=
s_endpgm
L_slow_%=:
s_mov_b32 m0, s57
s_waitcnt vmcnt(0)
L_end_%=:
.purgem M
.purgem RV
.purgem RA
.purgem PM
.purgem PH
.purgem PL
)ASM"
                 : "={s9}"(flags), "={s21}"(row0), "={s22}"(row3), "={s23}"(x0), "={s24}"(geom), "+{s[4:5]}"(ka),
                   "+{v0}"(tid)
                 : "{s[6:7]}"((uint64_t)TILE_TABLE), "{s8}"(blockIdx.x)
                 : DCN_CLOB);
    kernarg_t* a = (kernarg_t*)ka;
    dcn_generic(a->x, a->off, a->w, a->out, (u32)(uintptr_t)smem, row0, row3, x0, geom, tid);
}

extern "C" void kernel_launch(void* const* d_in, const int* in_sizes, int n_in, void* d_out, int out_size,
                              void* d_ws, size_t ws_size, hipStream_t stream) {
    dcn_main<<<256, 256, 0, stream>>>((const uint16_t*)d_in[0], (const uint16_t*)d_in[1], (const uint16_t*)d_in[2],
                                      d_ws, (uint16_t*)d_out);
}
```
